```python
import torch

def _softmax(x, axis=-1):
    return torch.softmax(x, dim=axis)
def _gelu(x):
    return torch.nn.functional.gelu(x, approximate="tanh")
def _silu(x):
    return torch.nn.functional.silu(x)
def _sigmoid(x):
    return torch.sigmoid(x)
def _layernorm(x, axis=-1, eps=1e-5):
    m = x.mean(dim=axis, keepdim=True)
    v = x.var(dim=axis, keepdim=True, unbiased=False)
    return (x - m) / torch.sqrt(v + eps)
def _scatter_add(tgt, idx, src, axis=0):
    return tgt.clone().index_add_(axis, idx, src)
def _segment_sum(src, seg, n):
    out = torch.zeros((n,) + src.shape[1:], dtype=src.dtype)
    return out.index_add_(0, seg, src)
def _pad(x, pw):
    flat = tuple(v for pair in reversed(pw) for v in pair)
    return torch.nn.functional.pad(x, flat)

kernel_name = 'standalone__mha_block_cached__near_ridge'

def setup_inputs():

    _w = (lambda a: (lambda t: t.to(torch.bfloat16) if t.is_floating_point() else t)(torch.as_tensor(a)))
    B, Sq, D = 8, 288, 512
    H, Sk, Dh = 32, 2048, 128
    return {
        "x": _w((lambda *s: torch.randn(*s, dtype=torch.bfloat16) * 0.8)(B, Sq, D)),
        "k_cache": _w((lambda *s: torch.randn(*s, dtype=torch.bfloat16) * 0.8)(B, H, Sk, Dh)),
        "v_cache": _w((lambda *s: torch.randn(*s, dtype=torch.bfloat16) * 0.05)(B, H, Sk, Dh)),
        "wq": _w((lambda *s: torch.randn(*s, dtype=torch.bfloat16) * 0.8)(D, H * Dh) * (D ** -0.5)),
        "wo": _w((lambda *s: torch.randn(*s, dtype=torch.bfloat16) * 0.05)(H * Dh, D) * ((H * Dh) ** -0.5)),
    }

def reference(x, k_cache, v_cache, wq, wo):
    B, Sq, D = x.shape
    H, Dh = 32, 128
    q = (lambda x, *d: x.permute(*d))((x @ wq).reshape(B, Sq, H, Dh), 0, 2, 1, 3)
    scores = torch.einsum("bhqd,bhkd->bhqk", q, k_cache) * (Dh ** -0.5)
    p = _softmax(scores, axis=-1)
    o = torch.einsum("bhqk,bhkd->bhqd", p, v_cache)
    return (lambda x, *d: x.permute(*d))(o, 0, 2, 1, 3).reshape(B, Sq, H * Dh) @ wo
```

```cpp
#include <hip/hip_runtime.h>
#include <hip/hip_bf16.h>
typedef float f32x16 __attribute__((ext_vector_type(16)));
typedef __bf16 bf16x8 __attribute__((ext_vector_type(8)));
typedef unsigned short u16;
constexpr int B=8, SQ=288, D=512, H=32, SK=2048, DH=128;
__device__ inline float bf2f(u16 x){ return __uint_as_float(((unsigned)x)<<16); }
__device__ inline u16 f2bf(float f){ __bf16 b=(__bf16)f; return __builtin_bit_cast(u16,b); }
__global__ void gemm_simple(const u16* A, const u16* W, u16* C, int M, int N, int K){
  int wave = (blockIdx.x*blockDim.x+threadIdx.x)>>6, lane=threadIdx.x&63;
  int nt = N/32; int tm = wave/nt, tn = wave%nt;
  if (tm*32>=M) return;
  int r=lane&31,h=lane>>5;
  f32x16 acc={0};
  for(int k0=0;k0<K;k0+=16){
    bf16x8 a = *(const bf16x8*)(A + (size_t)(tm*32+r)*K + k0+8*h);
    bf16x8 b;
    for(int j=0;j<8;j++) b[j]=__builtin_bit_cast(__bf16, W[(size_t)(k0+8*h+j)*N + tn*32+r]);
    acc=__builtin_amdgcn_mfma_f32_32x32x16_bf16(a,b,acc,0,0,0);
  }
  for(int i=0;i<16;i++){ int row=(i&3)+8*(i>>2)+4*h; C[(size_t)(tm*32+row)*N+tn*32+r]=f2bf(acc[i]); }
}
__global__ __launch_bounds__(64) void attn_simple(const u16* Q, const u16* Kc, const u16* Vc, u16* O){
  int wave = blockIdx.x; int lane=threadIdx.x&63;
  int qb = wave%9; int bh = wave/9; int b=bh/H, h=bh%H;
  int r=lane&31,hi=lane>>5;
  bf16x8 q[8];
  for(int d=0;d<8;d++) q[d]=*(const bf16x8*)(Q + (size_t)(b*SQ+qb*32+r)*(H*DH) + h*DH + d*16+8*hi);
  f32x16 o[4]; for(int i=0;i<4;i++) o[i]=(f32x16){0};
  float m=-1e30f,l=0;
  const u16* Kb=Kc+(size_t)bh*SK*DH; const u16* Vb=Vc+(size_t)bh*SK*DH;
  const float c=0.08838834764831845f*1.4426950408889634f;
  for(int k0=0;k0<SK;k0+=32){
    f32x16 s={0};
    for(int d=0;d<8;d++){ bf16x8 a=*(const bf16x8*)(Kb+(size_t)(k0+r)*DH+d*16+8*hi); s=__builtin_amdgcn_mfma_f32_32x32x16_bf16(a,q[d],s,0,0,0);}
    float mx=-1e30f; for(int i=0;i<16;i++) mx=fmaxf(mx,s[i]*c);
    mx=fmaxf(mx,__shfl_xor(mx,32));
    float mn=fmaxf(m,mx); float al=exp2f(m-mn); m=mn; l*=al;
    for(int i=0;i<4;i++) o[i]*=al;
    bf16x8 p[2];
    for(int i=0;i<16;i++){ float e=exp2f(s[i]*c-mn); l+=e; p[i>>3][i&7]=(__bf16)e; }
    for(int ks=0;ks<2;ks++) for(int db=0;db<4;db++){
      bf16x8 v; for(int j=0;j<8;j++){ int key=16*ks+8*(j>>2)+4*hi+(j&3); v[j]=__builtin_bit_cast(__bf16,Vb[(size_t)(k0+key)*DH+db*32+r]); }
      o[db]=__builtin_amdgcn_mfma_f32_32x32x16_bf16(v,p[ks],o[db],0,0,0);
    }
  }
  l+=__shfl_xor(l,32);
  float il=1.f/l;
  for(int db=0;db<4;db++) for(int i=0;i<16;i++){ int d=db*32+(i&3)+8*(i>>2)+4*hi; O[(size_t)(b*SQ+qb*32+r)*(H*DH)+h*DH+d]=f2bf(o[db][i]*il);}
}
extern "C" void kernel_launch(void* const* d_in, const int* in_sizes, int n_in, void* d_out, int out_size, void* d_ws, size_t ws_size, hipStream_t stream){
  const u16* x=(const u16*)d_in[0]; const u16* k=(const u16*)d_in[1]; const u16* v=(const u16*)d_in[2]; const u16* wq=(const u16*)d_in[3]; const u16* wo=(const u16*)d_in[4];
  u16* Q=(u16*)d_ws; u16* O=Q+(size_t)B*SQ*H*DH;
  gemm_simple<<<(72*128)/4,256,0,stream>>>(x,wq,Q,B*SQ,H*DH,D);
  attn_simple<<<B*H*9,64,0,stream>>>(Q,k,v,O);
  gemm_simple<<<(72*16)/4,256,0,stream>>>(O,wo,(u16*)d_out,B*SQ,D,H*DH);
}
```

```cpp
#include <hip/hip_runtime.h>
#include <hip/hip_bf16.h>

__global__ __launch_bounds__(256, 1) void mha_fused_kernel(const char* x, const char* k, const char* v, const char* wq, const char* wo, char* out, char* ws) {
  __shared__ __attribute__((aligned(1024))) char lds[163840];
  const unsigned w = __builtin_amdgcn_readfirstlane(threadIdx.x >> 6);
  asm volatile(
  "s_mov_b32 s76, 0\n"
  "L_top_%=:\n"
  "s_load_dwordx8 s[8:15], %0, 0x0\n"
  "s_load_dwordx4 s[16:19], %0, 0x20\n"
  "s_load_dwordx2 s[20:21], %0, 0x30\n"
  "s_getpc_b64 s[44:45]\n"
  "L_pc_%=:\n"
  "s_mov_b32 s46, L_tail_%=-L_pc_%=\n"
  "s_mov_b32 s47, 0x20000\n"
  "s_lshl_b32 s68, %2, 13\n"
  "v_mbcnt_lo_u32_b32 v30, -1, 0\n"
  "v_mbcnt_hi_u32_b32 v30, -1, v30\n"
  "v_lshlrev_b32 v53, 7, v30\n"
  "buffer_load_dword v54, v53, s[44:47], s68 offen\n"
  "s_mov_b32 s64, %2\n"
  "s_bfe_u32 s5, %1, 0x20001\n"
  "s_lshr_b32 s33, %1, 5\n"
  "s_bfe_u32 s34, %1, 0x20003\n"
  "s_and_b32 s35, %1, 1\n"
  "s_lshl2_add_u32 s35, s35, s34\n"
  "s_lshl3_add_u32 s7, s5, s33\n"
  "s_lshl_b32 s67, s64, 12\n"
  "s_mul_i32 s87, s64, 4112\n"
  "s_lshr_b32 s77, s35, 2\n"
  "s_mul_i32 s69, s77, 512\n"
  "s_mul_i32 s77, s77, 16\n"
  "s_add_u32 s77, s77, s64\n"
  "s_lshl_b32 s70, s77, 17\n"
  "s_mov_b32 s42, 4186368\n"
  "s_mov_b32 s43, 0x20000\n"
  "s_mov_b32 s38, 299008\n"
  "s_mov_b32 s39, 0x20000\n"
  "v_lshrrev_b32 v31, 4, v30\n"
  "v_and_b32 v32, 15, v30\n"
  "v_lshlrev_b32 v38, 2, v31\n"
  "v_xor_b32 v38, v38, v32\n"
  "v_lshlrev_b32 v39, 13, v31\n"
  "v_lshl_add_u32 v25, v38, 4, v39\n"
  "v_add_u32 v26, 31744, v25\n"
  "v_add_u32 v27, 63480, v25\n"
  "v_add_u32 v28, 95224, v25\n"
  "s_and_b32 s65, s64, 1\n"
  "s_lshl_b32 s66, s64, 10\n"
  "s_lshl_b32 s77, s64, 16\n"
  "s_add_u32 s86, s68, 266240\n"
  "v_lshrrev_b32 v33, 3, v30\n"
  "v_and_b32 v34, 7, v30\n"
  "v_lshlrev_b32 v35, 10, v33\n"
  "v_xor_b32 v36, v34, v31\n"
  "v_lshl_add_u32 v36, v36, 4, v35\n"
  "s_xor_b32 s85, s5, 3\n"
  "s_lshl_b32 s85, s85, 14\n"
  "s_add_u32 s77, s77, s85\n"
  "v_add_u32 v37, s77, v36\n"
  "v_add_u32 v16, 4096, v37\n"
  "v_xor_b32 v40, 8256, v37\n"
  "v_add_u32 v17, 3072, v40\n"
  "v_xor_b32 v40, 16384, v37\n"
  "v_add_u32 v18, 2048, v40\n"
  "v_xor_b32 v40, 24640, v37\n"
  "v_add_u32 v19, 1024, v40\n"
  "v_xor_b32 v40, 32768, v37\n"
  "v_add_u32 v20, 4096, v40\n"
  "v_xor_b32 v40, 41024, v37\n"
  "v_add_u32 v21, 3072, v40\n"
  "v_xor_b32 v40, 49152, v37\n"
  "v_add_u32 v22, 2048, v40\n"
  "v_xor_b32 v40, 57408, v37\n"
  "v_add_u32 v23, 1024, v40\n"
  "s_lshl_b32 s77, s65, 6\n"
  "v_xor_b32 v36, s77, v36\n"
  "v_add_u32 v24, s86, v36\n"
  "s_lshl_b32 s77, s7, 8\n"
  "s_mul_i32 s86, s35, 294912\n"
  "s_waitcnt lgkmcnt(0)\n"
  "s_add_u32 s40, s14, s77\n"
  "s_addc_u32 s41, s15, 0\n"
  "s_add_u32 m0, s87, 36864\n"
  "s_nop 0\n"
  "buffer_load_dwordx4 v25, s[40:43], s70 offen lds\n"
  "buffer_load_dwordx4 v26, s[40:43], s70 offen offset:1024 lds\n"
  "buffer_load_dwordx4 v27, s[40:43], s70 offen offset:2056 lds\n"
  "buffer_load_dwordx4 v28, s[40:43], s70 offen offset:3080 lds\n"
  "s_add_u32 s70, s70, 524288\n"
  "s_sub_u32 s36, s8, 4096\n"
  "s_subb_u32 s37, s9, 0\n"
  "s_add_u32 s36, s36, s86\n"
  "s_addc_u32 s37, s37, 0\n"
  "s_add_u32 m0, s68, 0\n"
  "s_nop 0\n"
  "buffer_load_dwordx4 v16, s[36:39], s69 offen lds\n"
  "buffer_load_dwordx4 v17, s[36:39], s69 offen offset:1024 lds\n"
  "buffer_load_dwordx4 v18, s[36:39], s69 offen offset:2048 lds\n"
  "buffer_load_dwordx4 v19, s[36:39], s69 offen offset:3072 lds\n"
  "s_add_u32 s18, s18, s86\n"
  "s_addc_u32 s19, s19, 0\n"
  "s_lshr_b32 s77, s33, 1\n"
  "s_lshl_b32 s77, s77, 8\n"
  "s_add_u32 s18, s18, s77\n"
  "s_addc_u32 s19, s19, 0\n"
  "s_lshl_b32 s86, s5, 20\n"
  "s_add_u32 s77, s77, s86\n"
  "s_add_u32 s16, s16, s77\n"
  "s_addc_u32 s17, s17, 0\n"
  "s_add_u32 m0, s68, 4096\n"
  "s_nop 0\n"
  "buffer_load_dwordx4 v20, s[36:39], s69 offen lds\n"
  "s_lshl_b32 s77, s35, 5\n"
  "s_add_u32 s77, s77, s7\n"
  "s_lshl_b32 s77, s77, 19\n"
  "s_add_u32 s10, s10, s77\n"
  "s_addc_u32 s11, s11, 0\n"
  "s_add_u32 s12, s12, s77\n"
  "s_addc_u32 s13, s13, 0\n"
  "s_mul_i32 s77, s35, 2359296\n"
  "s_lshl_b32 s86, s5, 15\n"
  "s_add_u32 s77, s77, s86\n"
  "s_add_u32 s24, s20, s77\n"
  "s_addc_u32 s25, s21, 0\n"
  "s_lshl_b32 s77, s33, 12\n"
  "s_add_u32 s22, s24, s77\n"
  "s_addc_u32 s23, s25, 0\n"
  "s_lshl3_add_u32 s77, s35, s33\n"
  "s_mul_i32 s77, s77, 147456\n"
  "s_add_u32 s77, s77, 67108864\n"
  "s_add_u32 s26, s20, s77\n"
  "s_addc_u32 s27, s21, 0\n"
  "buffer_load_dwordx4 v21, s[36:39], s69 offen offset:1024 lds\n"
  "s_lshl_b32 s77, s35, 15\n"
  "s_add_u32 s77, s77, 50331648\n"
  "s_add_u32 s28, s20, s77\n"
  "s_addc_u32 s29, s21, 0\n"
  "s_mov_b64 s[56:57], s[28:29]\n"
  "s_mov_b32 s58, 32768\n"
  "s_mov_b32 s59, 0x20000\n"
  "s_lshl_b32 s77, s5, 3\n"
  "s_add_u32 s77, s77, s33\n"
  "s_lshl_b32 s77, s77, 10\n"
  "s_cmp_eq_u32 s64, 0\n"
  "s_cselect_b32 s6, 0, 0x3fff0000\n"
  "s_and_b32 s84, s76, 0x3fff0000\n"
  "s_or_b32 s84, s84, s6\n"
  "v_lshl_add_u32 v60, v30, 4, s77\n"
  "v_mov_b32 v61, 0x3fff0000\n"
  "v_cmp_gt_u32 s[98:99], 8, v30\n"
  "buffer_load_dwordx4 v22, s[36:39], s69 offen offset:2048 lds\n"
  "v_mov_b32 v62, s77\n"
  "v_mov_b32 v72, 0\n"
  "v_mov_b32 v73, 0\n"
  "v_mov_b32 v74, 0\n"
  "v_mov_b32 v75, 0\n"
  "v_cndmask_b32 v64, v60, v61, s[98:99]\n"
  "v_cmp_gt_u32 vcc, 12, v30\n"
  "v_lshl_add_u32 v60, v30, 2, s77\n"
  "s_lshl_b32 s77, s5, 3\n"
  "v_add_u32 v66, s77, v30\n"
  "v_cndmask_b32 v67, v61, v60, vcc\n"
  "v_accvgpr_write_b32 a251, v67\n"
  "v_subrev_u32 v68, 8, v30\n"
  "v_lshlrev_b32 v68, 3, v68\n"
  "v_add_u32 v68, s33, v68\n"
  "buffer_load_dwordx4 v23, s[36:39], s69 offen offset:3072 lds\n"
  "s_add_u32 s77, s5, 8\n"
  "v_mov_b32 v69, s77\n"
  "v_mov_b32 v70, s33\n"
  "v_cndmask_b32 v66, v68, v66, s[98:99]\n"
  "v_cndmask_b32 v69, v69, v70, s[98:99]\n"
  "v_lshlrev_b32 v66, 10, v66\n"
  "v_cndmask_b32 v66, v61, v66, vcc\n"
  "v_accvgpr_write_b32 a249, v66\n"
  "v_lshl_add_u32 v69, v69, 2, v66\n"
  "v_accvgpr_write_b32 a250, v69\n"
  "s_mov_b64 s[44:45], s[10:11]\n"
  "s_mov_b32 s46, 524288\n"
  "s_mov_b32 s47, 0x20000\n"
  "s_mov_b64 s[48:49], s[12:13]\n"
  "s_add_u32 m0, s66, 32768\n"
  "s_nop 0\n"
  "buffer_load_dwordx4 v24, s[36:39], s69 offen lds\n"
  "s_add_u32 s69, s69, 128\n"
  "s_mov_b32 s50, 524288\n"
  "s_mov_b32 s51, 0x20000\n"
  "s_mov_b64 s[60:61], s[16:17]\n"
  "s_mov_b32 s62, 1047808\n"
  "s_mov_b32 s63, 0x20000\n"
  "s_mov_b32 s71, 0\n"
  "s_mov_b32 s72, 0\n"
  "s_mov_b32 s73, 102400\n"
  "s_mov_b32 s74, 102400\n"
  "s_mov_b32 s8, 4294864896\n"
  "s_mov_b32 s9, 4294864896\n"
  "s_mov_b32 s78, 0x3e0293ee\n"
  "s_mov_b32 s79, 0x3e0293ee\n"
  "v_lshlrev_b32 v40, 4, v30\n"
  "v_accvgpr_write_b32 a248, v40\n"
  "s_add_u32 m0, s87, 90176\n"
  "s_nop 0\n"
  "buffer_load_dwordx4 v25, s[40:43], s70 offen lds\n"
  "v_lshrrev_b32 v40, 1, v32\n"
  "v_xor_b32 v40, v40, v31\n"
  "v_lshlrev_b32 v41, 7, v32\n"
  "v_lshl_add_u32 v41, v40, 4, v41\n"
  "v_add_u32 v0, s68, v41\n"
  "v_xor_b32 v1, 64, v0\n"
  "s_lshr_b32 s77, s64, 1\n"
  "s_lshl_b32 s77, s77, 11\n"
  "s_add_u32 s77, s77, 32768\n"
  "s_mul_i32 s86, s65, 272\n"
  "v_xor_b32 v41, s86, v41\n"
  "v_add_u32 v4, s77, v41\n"
  "v_xor_b32 v5, 64, v4\n"
  "v_bfe_u32 v42, v30, 2, 2\n"
  "v_and_b32 v43, 3, v30\n"
  "buffer_load_dwordx4 v26, s[40:43], s70 offen offset:1024 lds\n"
  "v_lshl_add_u32 v44, v31, 3, v42\n"
  "v_lshlrev_b32 v44, 8, v44\n"
  "v_lshl_add_u32 v44, v43, 4, v44\n"
  "v_lshl_add_u32 v44, v42, 6, v44\n"
  "s_lshl_b32 s77, s65, 7\n"
  "v_xor_b32 v44, s77, v44\n"
  "v_lshrrev_b32 v46, 1, v31\n"
  "v_and_b32 v47, 1, v31\n"
  "v_mul_u32_u24 v47, 8, v47\n"
  "v_lshl_add_u32 v46, v46, 4, v47\n"
  "v_add_u32 v46, 36864, v46\n"
  "v_add_u32 v8, v46, v44\n"
  "v_xor_b32 v47, 64, v44\n"
  "v_add_u32 v9, v46, v47\n"
  "buffer_load_dwordx4 v27, s[40:43], s70 offen offset:2056 lds\n"
  "v_xor_b32 v47, 0x80, v44\n"
  "v_add_u32 v10, v46, v47\n"
  "v_xor_b32 v47, 0xc0, v44\n"
  "v_add_u32 v11, v46, v47\n"
  "v_add_u32 v2, 106624, v0\n"
  "v_add_u32 v6, 106624, v4\n"
  "v_add_u32 v3, 106624, v1\n"
  "v_add_u32 v7, 106624, v5\n"
  "v_add_u32 v12, 106624, v8\n"
  "v_add_u32 v13, 106624, v9\n"
  "v_add_u32 v14, 106624, v10\n"
  "v_add_u32 v15, 106624, v11\n"
  "v_xor_b32 v45, v31, v32\n"
  "v_bfe_u32 v46, v30, 3, 1\n"
  "v_lshl_add_u32 v46, v46, 2, v43\n"
  "buffer_load_dwordx4 v28, s[40:43], s70 offen offset:3080 lds\n"
  "s_add_u32 s70, s70, 524288\n"
  "v_bfe_u32 v47, v30, 2, 1\n"
  "v_lshl_add_u32 v46, v47, 4, v46\n"
  "v_lshlrev_b32 v46, 8, v46\n"
  "v_lshl_add_u32 v46, v45, 4, v46\n"
  "s_mul_i32 s77, s65, 8320\n"
  "v_xor_b32 v46, s77, v46\n"
  "v_add_u32 v228, 114688, v46\n"
  "v_xor_b32 v229, 64, v228\n"
  "v_xor_b32 v230, 128, v228\n"
  "v_xor_b32 v231, 192, v228\n"
  "v_xor_b32 v232, 8192, v228\n"
  "v_xor_b32 v233, 8192, v229\n"
  "v_xor_b32 v234, 8192, v230\n"
  "v_xor_b32 v235, 8192, v231\n"
  "v_lshrrev_b32 v47, 2, v30\n"
  "s_add_u32 m0, s68, 53312\n"
  "s_nop 0\n"
  "buffer_load_dwordx4 v16, s[36:39], s69 offen lds\n"
  "v_lshlrev_b32 v47, 7, v47\n"
  "v_and_b32 v48, 3, v33\n"
  "v_lshl_add_u32 v47, v48, 5, v47\n"
  "v_lshl_add_u32 v47, v43, 3, v47\n"
  "s_lshl_b32 s77, s65, 12\n"
  "v_xor_b32 v47, s77, v47\n"
  "v_xor_b32 v236, 0, v47\n"
  "v_xor_b32 v237, 32, v47\n"
  "v_xor_b32 v238, 64, v47\n"
  "v_xor_b32 v239, 96, v47\n"
  "v_xor_b32 v240, 4096, v47\n"
  "v_xor_b32 v241, 4128, v47\n"
  "v_xor_b32 v242, 4160, v47\n"
  "v_xor_b32 v243, 4192, v47\n"
  "v_xor_b32 v244, 32768, v47\n"
  "buffer_load_dwordx4 v17, s[36:39], s69 offen offset:1024 lds\n"
  "v_xor_b32 v245, 32800, v47\n"
  "v_xor_b32 v246, 32832, v47\n"
  "v_xor_b32 v247, 32864, v47\n"
  "v_xor_b32 v248, 36864, v47\n"
  "v_xor_b32 v249, 36896, v47\n"
  "v_xor_b32 v250, 36928, v47\n"
  "v_xor_b32 v251, 36960, v47\n"
  "s_lshr_b32 s86, s64, 1\n"
  "s_lshl_b32 s77, s86, 12\n"
  "s_lshl_b32 s17, s65, 13\n"
  "s_add_u32 s16, s17, s77\n"
  "s_add_u32 s17, s16, 32768\n"
  "s_lshl_b32 s86, s86, 16\n"
  "v_and_b32 v49, 1, v31\n"
  "buffer_load_dwordx4 v18, s[36:39], s69 offen offset:2048 lds\n"
  "v_lshrrev_b32 v50, 1, v31\n"
  "v_lshlrev_b32 v51, 8, v49\n"
  "v_lshl_add_u32 v51, v50, 15, v51\n"
  "v_lshl_add_u32 v51, v45, 4, v51\n"
  "s_mul_i32 s77, s65, 576\n"
  "s_add_u32 s77, s77, s86\n"
  "v_xor_b32 v252, s77, v51\n"
  "v_xor_b32 v253, 0x80, v252\n"
  "v_and_b32 v51, 1, v33\n"
  "v_lshlrev_b32 v51, 8, v51\n"
  "v_lshl_add_u32 v51, v49, 15, v51\n"
  "v_lshl_add_u32 v51, v50, 9, v51\n"
  "v_lshlrev_b32 v52, 1, v31\n"
  "v_xor_b32 v52, v52, v34\n"
  "v_lshl_add_u32 v51, v52, 4, v51\n"
  "buffer_load_dwordx4 v19, s[36:39], s69 offen offset:3072 lds\n"
  "s_lshl_b32 s77, s65, 7\n"
  "s_add_u32 s77, s77, s86\n"
  "v_add_u32 v254, s77, v51\n"
  "v_mov_b32 v254, v254\n"
  "v_mov_b32 v255, v254\n"
  "v_and_b32 v51, 1, v30\n"
  "v_sub_u32 v51, 0, v51\n"
  "v_and_b32 v51, 0xaaaaaaaa, v51\n"
  "v_or_b32 v224, 0x44444444, v51\n"
  "v_lshlrev_b32 v52, 12, v31\n"
  "v_lshl_add_u32 v51, v32, 4, v52\n"
  "s_mul_i32 s77, s64, 16448\n"
  "v_xor_b32 v225, s77, v51\n"
  "v_accvgpr_write_b32 a252, v225\n"
  "v_and_b32 v51, 1, v31\n"
  "s_add_u32 m0, s68, 57408\n"
  "s_nop 0\n"
  "buffer_load_dwordx4 v20, s[36:39], s69 offen lds\n"
  "v_lshl_add_u32 v51, v42, 1, v51\n"
  "s_lshl_b32 s77, s64, 1\n"
  "v_xor_b32 v51, s77, v51\n"
  "v_lshl_add_u32 v52, v42, 2, v31\n"
  "v_lshlrev_b32 v52, 10, v52\n"
  "v_lshl_add_u32 v52, v51, 5, v52\n"
  "v_lshl_add_u32 v52, v43, 3, v52\n"
  "v_accvgpr_write_b32 a244, v52\n"
  "v_lshlrev_b32 v52, 8, v31\n"
  "v_lshl_add_u32 v52, v38, 4, v52\n"
  "v_accvgpr_write_b32 a245, v52\n"
  "v_lshlrev_b32 v52, 4, v31\n"
  "v_accvgpr_write_b32 a246, v52\n"
  "v_lshl_add_u32 v52, v43, 2, v42\n"
  "buffer_load_dwordx4 v21, s[36:39], s69 offen offset:1024 lds\n"
  "v_lshlrev_b32 v52, 8, v52\n"
  "v_lshl_add_u32 v52, v45, 4, v52\n"
  "v_accvgpr_write_b32 a247, v52\n"
  "s_lshl_b32 s77, s64, 2\n"
  "s_xor_b32 s77, s77, s5\n"
  "s_lshl_b32 s13, s77, 17\n"
  "s_xor_b32 s12, s13, 131072\n"
  "s_xor_b32 s11, s13, 262144\n"
  "s_xor_b32 s10, s13, 393216\n"
  "s_lshr_b32 s77, s64, 1\n"
  "s_add_u32 s77, s77, 16\n"
  "s_lshl_b32 s14, s77, 17\n"
  "s_add_u32 s15, s67, 65536\n"
  "s_and_b32 s77, s33, 1\n"
  "s_lshl2_add_u32 s77, s77, s34\n"
  "buffer_load_dwordx4 v22, s[36:39], s69 offen offset:2048 lds\n"
  "s_lshl2_add_u32 s77, s77, s64\n"
  "s_mul_i32 s81, s77, 6144\n"
  "v_lshrrev_b32 v52, 1, v30\n"
  "v_lshlrev_b32 v52, 10, v52\n"
  "v_and_b32 v51, 1, v30\n"
  "v_lshl_add_u32 v52, v51, 7, v52\n"
  "v_cmp_gt_u32 vcc, 12, v30\n"
  "s_mov_b32 s75, 0\n"
  "s_mov_b32 s82, 0\n"
  "s_mov_b32 s83, 0\n"
  "v_cndmask_b32 v226, v61, v52, vcc\n"
  "v_mov_b32 v52, 0x3f803f80\n"
  "v_cmp_gt_u32 vcc, 2, v32\n"
  "s_nop 1\n"
  "v_cndmask_b32 v52, 0, v52, vcc\n"
  "v_accvgpr_write_b32 a240, v52\n"
  "buffer_load_dwordx4 v23, s[36:39], s69 offen offset:3072 lds\n"
  "v_accvgpr_write_b32 a241, v52\n"
  "v_accvgpr_write_b32 a242, v52\n"
  "v_accvgpr_write_b32 a243, v52\n"
  "v_accvgpr_write_b32 a224, 0\n"
  "v_accvgpr_write_b32 a225, 0\n"
  "v_accvgpr_write_b32 a226, 0\n"
  "v_accvgpr_write_b32 a227, 0\n"
  "v_accvgpr_write_b32 a228, 0\n"
  "v_accvgpr_write_b32 a229, 0\n"
  "v_accvgpr_write_b32 a230, 0\n"
  "v_accvgpr_write_b32 a231, 0\n"
  "v_accvgpr_write_b32 a232, 0\n"
  "v_accvgpr_write_b32 a233, 0\n"
  "v_accvgpr_write_b32 a234, 0\n"
  "v_accvgpr_write_b32 a235, 0\n"
  "s_add_u32 m0, s66, 86080\n"
  "s_nop 0\n"
  "buffer_load_dwordx4 v24, s[36:39], s69 offen lds\n"
  "s_add_u32 s69, s69, 128\n"
  "v_accvgpr_write_b32 a236, 0\n"
  "v_accvgpr_write_b32 a237, 0\n"
  "v_accvgpr_write_b32 a238, 0\n"
  "v_accvgpr_write_b32 a239, 0\n"
  "s_mov_b64 s[52:53], s[22:23]\n"
  "s_mov_b32 s54, 2232320\n"
  "s_mov_b32 s55, 0x20000\n"
  "buffer_load_dword v65, v62, s[56:59], 0 offen sc1\n"
  "buffer_store_dwordx4 v[72:75], v64, s[56:59], s84 offen sc1\n"
  "s_waitcnt vmcnt(15)\n"
  "s_barrier\n"
  "ds_read_b64_tr_b16 a[80:81], v8 offset:0\n"
  "ds_read_b64_tr_b16 a[82:83], v8 offset:1024\n"
  "ds_read_b128 a[0:3], v0 offset:0\n"
  "ds_read_b64_tr_b16 a[84:85], v8 offset:8224\n"
  "ds_read_b64_tr_b16 a[86:87], v8 offset:9248\n"
  "ds_read_b128 a[4:7], v1 offset:0\n"
  "ds_read_b128 a[8:11], v0 offset:2048\n"
  "ds_read_b128 a[12:15], v1 offset:2048\n"
  "ds_read_b128 a[16:19], v0 offset:4096\n"
  "ds_read_b128 a[20:23], v1 offset:4096\n"
  "ds_read_b128 a[24:27], v0 offset:6144\n"
  "ds_read_b128 a[28:31], v1 offset:6144\n"
  "ds_read_b128 a[32:35], v4 offset:0\n"
  "ds_read_b128 a[36:39], v5 offset:0\n"
  "ds_read_b64_tr_b16 a[88:89], v8 offset:8\n"
  "ds_read_b64_tr_b16 a[90:91], v8 offset:1032\n"
  "ds_read_b64_tr_b16 a[92:93], v8 offset:8232\n"
  "ds_read_b64_tr_b16 a[94:95], v8 offset:9256\n"
  "ds_read_b64_tr_b16 a[96:97], v9 offset:0\n"
  "ds_read_b64_tr_b16 a[98:99], v9 offset:1024\n"
  "ds_read_b64_tr_b16 a[100:101], v9 offset:8224\n"
  "ds_read_b64_tr_b16 a[102:103], v9 offset:9248\n"
  "s_waitcnt lgkmcnt(14)\n"
  "v_mfma_f32_16x16x32_bf16 v[192:195], a[80:83], a[0:3], 0\n"
  "s_waitcnt lgkmcnt(14)\n"
  "ds_read_b64_tr_b16 a[104:105], v9 offset:8\n"
  "v_mfma_f32_16x16x32_bf16 v[192:195], a[84:87], a[4:7], v[192:195]\n"
  "s_add_u32 m0, s87, 143488\n"
  "s_waitcnt lgkmcnt(14)\n"
  "ds_read_b64_tr_b16 a[106:107], v9 offset:1032\n"
  "v_mfma_f32_16x16x32_bf16 v[80:83], a[80:83], a[8:11], 0\n"
  "s_waitcnt lgkmcnt(14)\n"
  "buffer_load_dwordx4 v25, s[40:43], s70 offen lds\n"
  "v_mfma_f32_16x16x32_bf16 v[80:83], a[84:87], a[12:15], v[80:83]\n"
  "s_waitcnt lgkmcnt(14)\n"
  "ds_read_b64_tr_b16 a[108:109], v9 offset:8232\n"
  "v_mfma_f32_16x16x32_bf16 v[112:115], a[80:83], a[16:19], 0\n"
  "s_waitcnt lgkmcnt(14)\n"
  "v_mfma_f32_16x16x32_bf16 v[112:115], a[84:87], a[20:23], v[112:115]\n"
  "s_waitcnt lgkmcnt(14)\n"
  "ds_read_b64_tr_b16 a[110:111], v9 offset:9256\n"
  "v_mfma_f32_16x16x32_bf16 v[144:147], a[80:83], a[24:27], 0\n"
  "s_waitcnt lgkmcnt(14)\n"
  "buffer_load_dwordx4 v26, s[40:43], s70 offen offset:1024 lds\n"
  "v_mfma_f32_16x16x32_bf16 v[144:147], a[84:87], a[28:31], v[144:147]\n"
  "s_waitcnt lgkmcnt(13)\n"
  "ds_read_b64_tr_b16 a[112:113], v10 offset:0\n"
  "v_mfma_f32_16x16x32_bf16 v[176:179], a[80:83], a[32:35], 0\n"
  "s_waitcnt lgkmcnt(13)\n"
  "v_mfma_f32_16x16x32_bf16 v[176:179], a[84:87], a[36:39], v[176:179]\n"
  "s_waitcnt lgkmcnt(11)\n"
  "buffer_load_dwordx4 v27, s[40:43], s70 offen offset:2056 lds\n"
  "v_mfma_f32_16x16x32_bf16 v[180:183], a[88:91], a[32:35], 0\n"
  "s_waitcnt lgkmcnt(9)\n"
  "ds_read_b64_tr_b16 a[114:115], v10 offset:1024\n"
  "v_mfma_f32_16x16x32_bf16 v[180:183], a[92:95], a[36:39], v[180:183]\n"
  "ds_read_b64_tr_b16 a[116:117], v10 offset:8224\n"
  "v_mfma_f32_16x16x32_bf16 v[148:151], a[88:91], a[24:27], 0\n"
  "v_mfma_f32_16x16x32_bf16 v[148:151], a[92:95], a[28:31], v[148:151]\n"
  "buffer_load_dwordx4 v28, s[40:43], s70 offen offset:3080 lds\n"
  "v_mfma_f32_16x16x32_bf16 v[116:119], a[88:91], a[16:19], 0\n"
  "s_add_u32 s70, s70, 524288\n"
  "ds_read_b64_tr_b16 a[118:119], v10 offset:9248\n"
  "v_mfma_f32_16x16x32_bf16 v[116:119], a[92:95], a[20:23], v[116:119]\n"
  "s_add_u32 m0, s68, 106624\n"
  "ds_read_b64_tr_b16 a[120:121], v10 offset:8\n"
  "v_mfma_f32_16x16x32_bf16 v[84:87], a[88:91], a[8:11], 0\n"
  "buffer_load_dwordx4 v16, s[36:39], s69 offen lds\n"
  "v_mfma_f32_16x16x32_bf16 v[84:87], a[92:95], a[12:15], v[84:87]\n"
  "v_mfma_f32_16x16x32_bf16 v[196:199], a[88:91], a[0:3], 0\n"
  "ds_read_b64_tr_b16 a[122:123], v10 offset:1032\n"
  "v_mfma_f32_16x16x32_bf16 v[196:199], a[92:95], a[4:7], v[196:199]\n"
  "s_waitcnt lgkmcnt(12)\n"
  "ds_read_b64_tr_b16 a[124:125], v10 offset:8232\n"
  "v_mfma_f32_16x16x32_bf16 v[200:203], a[96:99], a[0:3], 0\n"
  "s_waitcnt lgkmcnt(11)\n"
  "buffer_load_dwordx4 v17, s[36:39], s69 offen offset:1024 lds\n"
  "v_mfma_f32_16x16x32_bf16 v[200:203], a[100:103], a[4:7], v[200:203]\n"
  "ds_read_b64_tr_b16 a[126:127], v10 offset:9256\n"
  "v_mfma_f32_16x16x32_bf16 v[88:91], a[96:99], a[8:11], 0\n"
  "v_mfma_f32_16x16x32_bf16 v[88:91], a[100:103], a[12:15], v[88:91]\n"
  "buffer_load_dwordx4 v18, s[36:39], s69 offen offset:2048 lds\n"
  "v_mfma_f32_16x16x32_bf16 v[120:123], a[96:99], a[16:19], 0\n"
  "ds_read_b64_tr_b16 a[128:129], v11 offset:0\n"
  "v_mfma_f32_16x16x32_bf16 v[120:123], a[100:103], a[20:23], v[120:123]\n"
  "ds_read_b64_tr_b16 a[130:131], v11 offset:1024\n"
  "v_mfma_f32_16x16x32_bf16 v[152:155], a[96:99], a[24:27], 0\n"
  "buffer_load_dwordx4 v19, s[36:39], s69 offen offset:3072 lds\n"
  "v_mfma_f32_16x16x32_bf16 v[152:155], a[100:103], a[28:31], v[152:155]\n"
  "v_mfma_f32_16x16x32_bf16 v[184:187], a[96:99], a[32:35], 0\n"
  "s_add_u32 m0, s68, 110720\n"
  "ds_read_b64_tr_b16 a[132:133], v11 offset:8224\n"
  "v_mfma_f32_16x16x32_bf16 v[184:187], a[100:103], a[36:39], v[184:187]\n"
  "s_waitcnt lgkmcnt(13)\n"
  "ds_read_b64_tr_b16 a[134:135], v11 offset:9248\n"
  "v_mfma_f32_16x16x32_bf16 v[188:191], a[104:107], a[32:35], 0\n"
  "s_waitcnt lgkmcnt(12)\n"
  "buffer_load_dwordx4 v20, s[36:39], s69 offen lds\n"
  "v_mfma_f32_16x16x32_bf16 v[188:191], a[108:111], a[36:39], v[188:191]\n"
  "v_mfma_f32_16x16x32_bf16 v[156:159], a[104:107], a[24:27], 0\n"
  "ds_read_b64_tr_b16 a[136:137], v11 offset:8\n"
  "v_mfma_f32_16x16x32_bf16 v[156:159], a[108:111], a[28:31], v[156:159]\n"
  "buffer_load_dwordx4 v21, s[36:39], s69 offen offset:1024 lds\n"
  "v_mfma_f32_16x16x32_bf16 v[124:127], a[104:107], a[16:19], 0\n"
  "ds_read_b64_tr_b16 a[138:139], v11 offset:1032\n"
  "v_mfma_f32_16x16x32_bf16 v[124:127], a[108:111], a[20:23], v[124:127]\n"
  "v_mfma_f32_16x16x32_bf16 v[92:95], a[104:107], a[8:11], 0\n"
  "ds_read_b64_tr_b16 a[140:141], v11 offset:8232\n"
  "v_mfma_f32_16x16x32_bf16 v[92:95], a[108:111], a[12:15], v[92:95]\n"
  "buffer_load_dwordx4 v22, s[36:39], s69 offen offset:2048 lds\n"
  "v_mfma_f32_16x16x32_bf16 v[204:207], a[104:107], a[0:3], 0\n"
  "ds_read_b64_tr_b16 a[142:143], v11 offset:9256\n"
  "v_mfma_f32_16x16x32_bf16 v[204:207], a[108:111], a[4:7], v[204:207]\n"
  "s_waitcnt lgkmcnt(14)\n"
  "v_mfma_f32_16x16x32_bf16 v[208:211], a[112:115], a[0:3], 0\n"
  "s_waitcnt lgkmcnt(12)\n"
  "buffer_load_dwordx4 v23, s[36:39], s69 offen offset:3072 lds\n"
  "v_mfma_f32_16x16x32_bf16 v[208:211], a[116:119], a[4:7], v[208:211]\n"
  "v_mfma_f32_16x16x32_bf16 v[96:99], a[112:115], a[8:11], 0\n"
  "v_mfma_f32_16x16x32_bf16 v[96:99], a[116:119], a[12:15], v[96:99]\n"
  "s_add_u32 m0, s66, 139392\n"
  "v_mfma_f32_16x16x32_bf16 v[128:131], a[112:115], a[16:19], 0\n"
  "buffer_load_dwordx4 v24, s[36:39], s69 offen lds\n"
  "v_mfma_f32_16x16x32_bf16 v[128:131], a[116:119], a[20:23], v[128:131]\n"
  "s_add_u32 s69, s69, 128\n"
  "v_mfma_f32_16x16x32_bf16 v[160:163], a[112:115], a[24:27], 0\n"
  "s_waitcnt vmcnt(15) lgkmcnt(0)\n"
  "s_barrier\n"
  "v_mfma_f32_16x16x32_bf16 v[160:163], a[116:119], a[28:31], v[160:163]\n"
  "ds_read_b64_tr_b16 a[80:81], v8 offset:53312\n"
  "v_mfma_f32_16x16x32_bf16 v[164:167], a[120:123], a[24:27], 0\n"
  "ds_read_b64_tr_b16 a[82:83], v8 offset:54336\n"
  "v_mfma_f32_16x16x32_bf16 v[164:167], a[124:127], a[28:31], v[164:167]\n"
  "s_add_u32 m0, s87, 36864\n"
  "ds_read_b128 a[40:43], v0 offset:53312\n"
  "v_mfma_f32_16x16x32_bf16 v[132:135], a[120:123], a[16:19], 0\n"
  "buffer_load_dwordx4 v25, s[40:43], s70 offen lds\n"
  "v_mfma_f32_16x16x32_bf16 v[132:135], a[124:127], a[20:23], v[132:135]\n"
  "ds_read_b64_tr_b16 a[84:85], v8 offset:61536\n"
  "v_mfma_f32_16x16x32_bf16 v[100:103], a[120:123], a[8:11], 0\n"
  "v_mfma_f32_16x16x32_bf16 v[100:103], a[124:127], a[12:15], v[100:103]\n"
  "ds_read_b64_tr_b16 a[86:87], v8 offset:62560\n"
  "v_mfma_f32_16x16x32_bf16 v[212:215], a[120:123], a[0:3], 0\n"
  "ds_read_b128 a[44:47], v1 offset:53312\n"
  "v_mfma_f32_16x16x32_bf16 v[212:215], a[124:127], a[4:7], v[212:215]\n"
  "ds_read_b128 a[48:51], v0 offset:55360\n"
  "v_mfma_f32_16x16x32_bf16 v[216:219], a[128:131], a[0:3], 0\n"
  "buffer_load_dwordx4 v26, s[40:43], s70 offen offset:1024 lds\n"
  "v_mfma_f32_16x16x32_bf16 v[216:219], a[132:135], a[4:7], v[216:219]\n"
  "v_mfma_f32_16x16x32_bf16 v[104:107], a[128:131], a[8:11], 0\n"
  "ds_read_b128 a[52:55], v1 offset:55360\n"
  "v_mfma_f32_16x16x32_bf16 v[104:107], a[132:135], a[12:15], v[104:107]\n"
  "ds_read_b128 a[56:59], v0 offset:57408\n"
  "v_mfma_f32_16x16x32_bf16 v[136:139], a[128:131], a[16:19], 0\n"
  "ds_read_b128 a[60:63], v1 offset:57408\n"
  "v_mfma_f32_16x16x32_bf16 v[136:139], a[132:135], a[20:23], v[136:139]\n"
  "buffer_load_dwordx4 v27, s[40:43], s70 offen offset:2056 lds\n"
  "v_mfma_f32_16x16x32_bf16 v[168:171], a[128:131], a[24:27], 0\n"
  "ds_read_b128 a[64:67], v0 offset:59456\n"
  "v_mfma_f32_16x16x32_bf16 v[168:171], a[132:135], a[28:31], v[168:171]\n"
  "v_mfma_f32_16x16x32_bf16 v[172:175], a[136:139], a[24:27], 0\n"
  "ds_read_b128 a[68:71], v1 offset:59456\n"
  "v_mfma_f32_16x16x32_bf16 v[172:175], a[140:143], a[28:31], v[172:175]\n"
  "ds_read_b128 a[72:75], v4 offset:53312\n"
  "v_mfma_f32_16x16x32_bf16 v[140:143], a[136:139], a[16:19], 0\n"
  "buffer_load_dwordx4 v28, s[40:43], s70 offen offset:3080 lds\n"
  "v_mfma_f32_16x16x32_bf16 v[140:143], a[140:143], a[20:23], v[140:143]\n"
  "s_add_u32 s70, s70, 524288\n"
  "ds_read_b128 a[76:79], v5 offset:53312\n"
  "v_mfma_f32_16x16x32_bf16 v[108:111], a[136:139], a[8:11], 0\n"
  "s_and_b32 s70, s70, 0x3fffff\n"
  "v_mfma_f32_16x16x32_bf16 v[108:111], a[140:143], a[12:15], v[108:111]\n"
  "ds_read_b64_tr_b16 a[88:89], v8 offset:53320\n"
  "v_mfma_f32_16x16x32_bf16 v[220:223], a[136:139], a[0:3], 0\n"
  "s_add_u32 m0, s68, 0\n"
  "ds_read_b64_tr_b16 a[90:91], v8 offset:54344\n"
  "v_mfma_f32_16x16x32_bf16 v[220:223], a[140:143], a[4:7], v[220:223]\n"
  "s_waitcnt lgkmcnt(13)\n"
  "ds_read_b64_tr_b16 a[92:93], v8 offset:61544\n"
  "v_mfma_f32_16x16x32_bf16 v[192:195], a[80:83], a[40:43], v[192:195]\n"
  "s_waitcnt lgkmcnt(11)\n"
  "buffer_load_dwordx4 v16, s[36:39], s69 offen lds\n"
  "v_mfma_f32_16x16x32_bf16 v[192:195], a[84:87], a[44:47], v[192:195]\n"
  "s_waitcnt lgkmcnt(10)\n"
  "ds_read_b64_tr_b16 a[94:95], v8 offset:62568\n"
  "v_mfma_f32_16x16x32_bf16 v[80:83], a[80:83], a[48:51], v[80:83]\n"
  "s_waitcnt lgkmcnt(10)\n"
  "v_mfma_f32_16x16x32_bf16 v[80:83], a[84:87], a[52:55], v[80:83]\n"
  "s_waitcnt lgkmcnt(9)\n"
  "ds_read_b64_tr_b16 a[96:97], v9 offset:53312\n"
  "v_mfma_f32_16x16x32_bf16 v[112:115], a[80:83], a[56:59], v[112:115]\n"
  "s_waitcnt lgkmcnt(9)\n"
  "ds_read_b64_tr_b16 a[98:99], v9 offset:54336\n"
  "v_mfma_f32_16x16x32_bf16 v[112:115], a[84:87], a[60:63], v[112:115]\n"
  "s_waitcnt lgkmcnt(9)\n"
  "buffer_load_dwordx4 v17, s[36:39], s69 offen offset:1024 lds\n"
  "v_mfma_f32_16x16x32_bf16 v[144:147], a[80:83], a[64:67], v[144:147]\n"
  "s_waitcnt lgkmcnt(8)\n"
  "ds_read_b64_tr_b16 a[100:101], v9 offset:61536\n"
  "v_mfma_f32_16x16x32_bf16 v[144:147], a[84:87], a[68:71], v[144:147]\n"
  "s_waitcnt lgkmcnt(8)\n"
  "v_mfma_f32_16x16x32_bf16 v[176:179], a[80:83], a[72:75], v[176:179]\n"
  "s_waitcnt lgkmcnt(7)\n"
  "ds_read_b64_tr_b16 a[102:103], v9 offset:62560\n"
  "v_mfma_f32_16x16x32_bf16 v[176:179], a[84:87], a[76:79], v[176:179]\n"
  "s_waitcnt lgkmcnt(6)\n"
  "ds_read_b64_tr_b16 a[104:105], v9 offset:53320\n"
  "v_mfma_f32_16x16x32_bf16 v[180:183], a[88:91], a[72:75], v[180:183]\n"
  "s_waitcnt lgkmcnt(5)\n"
  "ds_read_b64_tr_b16 a[106:107], v9 offset:54344\n"
  "v_mfma_f32_16x16x32_bf16 v[180:183], a[92:95], a[76:79], v[180:183]\n"
  "buffer_load_dwordx4 v18, s[36:39], s69 offen offset:2048 lds\n"
  "v_mfma_f32_16x16x32_bf16 v[148:151], a[88:91], a[64:67], v[148:151]\n"
  "ds_read_b64_tr_b16 a[108:109], v9 offset:61544\n"
  "v_mfma_f32_16x16x32_bf16 v[148:151], a[92:95], a[68:71], v[148:151]\n"
  "v_mfma_f32_16x16x32_bf16 v[116:119], a[88:91], a[56:59], v[116:119]\n"
  "ds_read_b64_tr_b16 a[110:111], v9 offset:62568\n"
  "v_mfma_f32_16x16x32_bf16 v[116:119], a[92:95], a[60:63], v[116:119]\n"
  "ds_read_b64_tr_b16 a[112:113], v10 offset:53312\n"
  "v_mfma_f32_16x16x32_bf16 v[84:87], a[88:91], a[48:51], v[84:87]\n"
  "buffer_load_dwordx4 v19, s[36:39], s69 offen offset:3072 lds\n"
  "v_mfma_f32_16x16x32_bf16 v[84:87], a[92:95], a[52:55], v[84:87]\n"
  "ds_read_b64_tr_b16 a[114:115], v10 offset:54336\n"
  "v_mfma_f32_16x16x32_bf16 v[196:199], a[88:91], a[40:43], v[196:199]\n"
  "s_add_u32 m0, s68, 4096\n"
  "v_mfma_f32_16x16x32_bf16 v[196:199], a[92:95], a[44:47], v[196:199]\n"
  "s_waitcnt lgkmcnt(8)\n"
  "ds_read_b64_tr_b16 a[116:117], v10 offset:61536\n"
  "v_mfma_f32_16x16x32_bf16 v[200:203], a[96:99], a[40:43], v[200:203]\n"
  "s_waitcnt lgkmcnt(7)\n"
  "ds_read_b64_tr_b16 a[118:119], v10 offset:62560\n"
  "v_mfma_f32_16x16x32_bf16 v[200:203], a[100:103], a[44:47], v[200:203]\n"
  "buffer_load_dwordx4 v20, s[36:39], s69 offen lds\n"
  "v_mfma_f32_16x16x32_bf16 v[88:91], a[96:99], a[48:51], v[88:91]\n"
  "ds_read_b64_tr_b16 a[120:121], v10 offset:53320\n"
  "v_mfma_f32_16x16x32_bf16 v[88:91], a[100:103], a[52:55], v[88:91]\n"
  "ds_read_b64_tr_b16 a[122:123], v10 offset:54344\n"
  "v_mfma_f32_16x16x32_bf16 v[120:123], a[96:99], a[56:59], v[120:123]\n"
  "v_mfma_f32_16x16x32_bf16 v[120:123], a[100:103], a[60:63], v[120:123]\n"
  "ds_read_b64_tr_b16 a[124:125], v10 offset:61544\n"
  "v_mfma_f32_16x16x32_bf16 v[152:155], a[96:99], a[64:67], v[152:155]\n"
  "ds_read_b64_tr_b16 a[126:127], v10 offset:62568\n"
  "v_mfma_f32_16x16x32_bf16 v[152:155], a[100:103], a[68:71], v[152:155]\n"
  "buffer_load_dwordx4 v21, s[36:39], s69 offen offset:1024 lds\n"
  "v_mfma_f32_16x16x32_bf16 v[184:187], a[96:99], a[72:75], v[184:187]\n"
  "ds_read_b64_tr_b16 a[128:129], v11 offset:53312\n"
  "v_mfma_f32_16x16x32_bf16 v[184:187], a[100:103], a[76:79], v[184:187]\n"
  "s_waitcnt lgkmcnt(11)\n"
  "v_mfma_f32_16x16x32_bf16 v[188:191], a[104:107], a[72:75], v[188:191]\n"
  "s_waitcnt lgkmcnt(9)\n"
  "ds_read_b64_tr_b16 a[130:131], v11 offset:54336\n"
  "v_mfma_f32_16x16x32_bf16 v[188:191], a[108:111], a[76:79], v[188:191]\n"
  "ds_read_b64_tr_b16 a[132:133], v11 offset:61536\n"
  "v_mfma_f32_16x16x32_bf16 v[156:159], a[104:107], a[64:67], v[156:159]\n"
  "buffer_load_dwordx4 v22, s[36:39], s69 offen offset:2048 lds\n"
  "v_mfma_f32_16x16x32_bf16 v[156:159], a[108:111], a[68:71], v[156:159]\n"
  "ds_read_b64_tr_b16 a[134:135], v11 offset:62560\n"
  "v_mfma_f32_16x16x32_bf16 v[124:127], a[104:107], a[56:59], v[124:127]\n"
  "ds_read_b64_tr_b16 a[136:137], v11 offset:53320\n"
  "v_mfma_f32_16x16x32_bf16 v[124:127], a[108:111], a[60:63], v[124:127]\n"
  "v_mfma_f32_16x16x32_bf16 v[92:95], a[104:107], a[48:51], v[92:95]\n"
  "ds_read_b64_tr_b16 a[138:139], v11 offset:54344\n"
  "v_mfma_f32_16x16x32_bf16 v[92:95], a[108:111], a[52:55], v[92:95]\n"
  "buffer_load_dwordx4 v23, s[36:39], s69 offen offset:3072 lds\n"
  "v_mfma_f32_16x16x32_bf16 v[204:207], a[104:107], a[40:43], v[204:207]\n"
  "ds_read_b64_tr_b16 a[140:141], v11 offset:61544\n"
  "v_mfma_f32_16x16x32_bf16 v[204:207], a[108:111], a[44:47], v[204:207]\n"
  "s_waitcnt lgkmcnt(13)\n"
  "ds_read_b64_tr_b16 a[142:143], v11 offset:62568\n"
  "v_mfma_f32_16x16x32_bf16 v[208:211], a[112:115], a[40:43], v[208:211]\n"
  "s_waitcnt lgkmcnt(12)\n"
  "v_mfma_f32_16x16x32_bf16 v[208:211], a[116:119], a[44:47], v[208:211]\n"
  "v_mfma_f32_16x16x32_bf16 v[96:99], a[112:115], a[48:51], v[96:99]\n"
  "s_add_u32 m0, s66, 32768\n"
  "v_mfma_f32_16x16x32_bf16 v[96:99], a[116:119], a[52:55], v[96:99]\n"
  "buffer_load_dwordx4 v24, s[36:39], s69 offen lds\n"
  "v_mfma_f32_16x16x32_bf16 v[128:131], a[112:115], a[56:59], v[128:131]\n"
  "s_add_u32 s69, s69, 128\n"
  "v_mfma_f32_16x16x32_bf16 v[128:131], a[116:119], a[60:63], v[128:131]\n"
  "s_and_b32 s69, s69, 1023\n"
  "v_mfma_f32_16x16x32_bf16 v[160:163], a[112:115], a[64:67], v[160:163]\n"
  "s_waitcnt vmcnt(13) lgkmcnt(0)\n"
  "s_barrier\n"
  "v_mfma_f32_16x16x32_bf16 v[160:163], a[116:119], a[68:71], v[160:163]\n"
  "ds_read_b64_tr_b16 a[80:81], v12 offset:0\n"
  "v_mfma_f32_16x16x32_bf16 v[164:167], a[120:123], a[64:67], v[164:167]\n"
  "ds_read_b64_tr_b16 a[82:83], v12 offset:1024\n"
  "v_mfma_f32_16x16x32_bf16 v[164:167], a[124:127], a[68:71], v[164:167]\n"
  "s_add_u32 m0, s87, 90176\n"
  "ds_read_b128 a[0:3], v2 offset:0\n"
  "v_mfma_f32_16x16x32_bf16 v[132:135], a[120:123], a[56:59], v[132:135]\n"
  "buffer_load_dwordx4 v25, s[40:43], s70 offen lds\n"
  "v_mfma_f32_16x16x32_bf16 v[132:135], a[124:127], a[60:63], v[132:135]\n"
  "ds_read_b64_tr_b16 a[84:85], v12 offset:8224\n"
  "v_mfma_f32_16x16x32_bf16 v[100:103], a[120:123], a[48:51], v[100:103]\n"
  "v_mfma_f32_16x16x32_bf16 v[100:103], a[124:127], a[52:55], v[100:103]\n"
  "ds_read_b64_tr_b16 a[86:87], v12 offset:9248\n"
  "v_mfma_f32_16x16x32_bf16 v[212:215], a[120:123], a[40:43], v[212:215]\n"
  "ds_read_b128 a[4:7], v3 offset:0\n"
  "v_mfma_f32_16x16x32_bf16 v[212:215], a[124:127], a[44:47], v[212:215]\n"
  "ds_read_b128 a[8:11], v2 offset:2048\n"
  "v_mfma_f32_16x16x32_bf16 v[216:219], a[128:131], a[40:43], v[216:219]\n"
  "buffer_load_dwordx4 v26, s[40:43], s70 offen offset:1024 lds\n"
  "v_mfma_f32_16x16x32_bf16 v[216:219], a[132:135], a[44:47], v[216:219]\n"
  "v_mfma_f32_16x16x32_bf16 v[104:107], a[128:131], a[48:51], v[104:107]\n"
  "ds_read_b128 a[12:15], v3 offset:2048\n"
  "v_mfma_f32_16x16x32_bf16 v[104:107], a[132:135], a[52:55], v[104:107]\n"
  "ds_read_b128 a[16:19], v2 offset:4096\n"
  "v_mfma_f32_16x16x32_bf16 v[136:139], a[128:131], a[56:59], v[136:139]\n"
  "ds_read_b128 a[20:23], v3 offset:4096\n"
  "v_mfma_f32_16x16x32_bf16 v[136:139], a[132:135], a[60:63], v[136:139]\n"
  "buffer_load_dwordx4 v27, s[40:43], s70 offen offset:2056 lds\n"
  "v_mfma_f32_16x16x32_bf16 v[168:171], a[128:131], a[64:67], v[168:171]\n"
  "ds_read_b128 a[24:27], v2 offset:6144\n"
  "v_mfma_f32_16x16x32_bf16 v[168:171], a[132:135], a[68:71], v[168:171]\n"
  "v_mfma_f32_16x16x32_bf16 v[172:175], a[136:139], a[64:67], v[172:175]\n"
  "ds_read_b128 a[28:31], v3 offset:6144\n"
  "v_mfma_f32_16x16x32_bf16 v[172:175], a[140:143], a[68:71], v[172:175]\n"
  "ds_read_b128 a[32:35], v6 offset:0\n"
  "v_mfma_f32_16x16x32_bf16 v[140:143], a[136:139], a[56:59], v[140:143]\n"
  "buffer_load_dwordx4 v28, s[40:43], s70 offen offset:3080 lds\n"
  "v_mfma_f32_16x16x32_bf16 v[140:143], a[140:143], a[60:63], v[140:143]\n"
  "s_add_u32 s70, s70, 524288\n"
  "ds_read_b128 a[36:39], v7 offset:0\n"
  "v_mfma_f32_16x16x32_bf16 v[108:111], a[136:139], a[48:51], v[108:111]\n"
  "v_mfma_f32_16x16x32_bf16 v[108:111], a[140:143], a[52:55], v[108:111]\n"
  "ds_read_b64_tr_b16 a[88:89], v12 offset:8\n"
  "v_mfma_f32_16x16x32_bf16 v[220:223], a[136:139], a[40:43], v[220:223]\n"
  "s_add_u32 m0, s68, 53312\n"
  "ds_read_b64_tr_b16 a[90:91], v12 offset:1032\n"
  "v_mfma_f32_16x16x32_bf16 v[220:223], a[140:143], a[44:47], v[220:223]\n"
  "s_waitcnt lgkmcnt(13)\n"
  "ds_read_b64_tr_b16 a[92:93], v12 offset:8232\n"
  "v_mfma_f32_16x16x32_bf16 v[192:195], a[80:83], a[0:3], v[192:195]\n"
  "s_waitcnt lgkmcnt(11)\n"
  "buffer_load_dwordx4 v16, s[36:39], s69 offen lds\n"
  "v_mfma_f32_16x16x32_bf16 v[192:195], a[84:87], a[4:7], v[192:195]\n"
  "s_waitcnt lgkmcnt(10)\n"
  "ds_read_b64_tr_b16 a[94:95], v12 offset:9256\n"
  "v_mfma_f32_16x16x32_bf16 v[80:83], a[80:83], a[8:11], v[80:83]\n"
  "s_waitcnt lgkmcnt(10)\n"
  "v_mfma_f32_16x16x32_bf16 v[80:83], a[84:87], a[12:15], v[80:83]\n"
  "s_waitcnt lgkmcnt(9)\n"
  "ds_read_b64_tr_b16 a[96:97], v13 offset:0\n"
  "v_mfma_f32_16x16x32_bf16 v[112:115], a[80:83], a[16:19], v[112:115]\n"
  "s_waitcnt lgkmcnt(9)\n"
  "ds_read_b64_tr_b16 a[98:99], v13 offset:1024\n"
  "v_mfma_f32_16x16x32_bf16 v[112:115], a[84:87], a[20:23], v[112:115]\n"
  "s_waitcnt lgkmcnt(9)\n"
  "buffer_load_dwordx4 v17, s[36:39], s69 offen offset:1024 lds\n"
  "v_mfma_f32_16x16x32_bf16 v[144:147], a[80:83], a[24:27], v[144:147]\n"
  "s_waitcnt lgkmcnt(8)\n"
  "ds_read_b64_tr_b16 a[100:101], v13 offset:8224\n"
  "v_mfma_f32_16x16x32_bf16 v[144:147], a[84:87], a[28:31], v[144:147]\n"
  "s_waitcnt lgkmcnt(8)\n"
  "v_mfma_f32_16x16x32_bf16 v[176:179], a[80:83], a[32:35], v[176:179]\n"
  "s_waitcnt lgkmcnt(7)\n"
  "ds_read_b64_tr_b16 a[102:103], v13 offset:9248\n"
  "v_mfma_f32_16x16x32_bf16 v[176:179], a[84:87], a[36:39], v[176:179]\n"
  "s_waitcnt lgkmcnt(6)\n"
  "ds_read_b64_tr_b16 a[104:105], v13 offset:8\n"
  "v_mfma_f32_16x16x32_bf16 v[180:183], a[88:91], a[32:35], v[180:183]\n"
  "s_waitcnt lgkmcnt(5)\n"
  "ds_read_b64_tr_b16 a[106:107], v13 offset:1032\n"
  "v_mfma_f32_16x16x32_bf16 v[180:183], a[92:95], a[36:39], v[180:183]\n"
  "buffer_load_dwordx4 v18, s[36:39], s69 offen offset:2048 lds\n"
  "v_mfma_f32_16x16x32_bf16 v[148:151], a[88:91], a[24:27], v[148:151]\n"
  "ds_read_b64_tr_b16 a[108:109], v13 offset:8232\n"
  "v_mfma_f32_16x16x32_bf16 v[148:151], a[92:95], a[28:31], v[148:151]\n"
  "v_mfma_f32_16x16x32_bf16 v[116:119], a[88:91], a[16:19], v[116:119]\n"
  "ds_read_b64_tr_b16 a[110:111], v13 offset:9256\n"
  "v_mfma_f32_16x16x32_bf16 v[116:119], a[92:95], a[20:23], v[116:119]\n"
  "ds_read_b64_tr_b16 a[112:113], v14 offset:0\n"
  "v_mfma_f32_16x16x32_bf16 v[84:87], a[88:91], a[8:11], v[84:87]\n"
  "buffer_load_dwordx4 v19, s[36:39], s69 offen offset:3072 lds\n"
  "v_mfma_f32_16x16x32_bf16 v[84:87], a[92:95], a[12:15], v[84:87]\n"
  "ds_read_b64_tr_b16 a[114:115], v14 offset:1024\n"
  "v_mfma_f32_16x16x32_bf16 v[196:199], a[88:91], a[0:3], v[196:199]\n"
  "s_add_u32 m0, s68, 57408\n"
  "v_mfma_f32_16x16x32_bf16 v[196:199], a[92:95], a[4:7], v[196:199]\n"
  "s_waitcnt lgkmcnt(8)\n"
  "ds_read_b64_tr_b16 a[116:117], v14 offset:8224\n"
  "v_mfma_f32_16x16x32_bf16 v[200:203], a[96:99], a[0:3], v[200:203]\n"
  "s_waitcnt lgkmcnt(7)\n"
  "ds_read_b64_tr_b16 a[118:119], v14 offset:9248\n"
  "v_mfma_f32_16x16x32_bf16 v[200:203], a[100:103], a[4:7], v[200:203]\n"
  "buffer_load_dwordx4 v20, s[36:39], s69 offen lds\n"
  "v_mfma_f32_16x16x32_bf16 v[88:91], a[96:99], a[8:11], v[88:91]\n"
  "ds_read_b64_tr_b16 a[120:121], v14 offset:8\n"
  "v_mfma_f32_16x16x32_bf16 v[88:91], a[100:103], a[12:15], v[88:91]\n"
  "ds_read_b64_tr_b16 a[122:123], v14 offset:1032\n"
  "v_mfma_f32_16x16x32_bf16 v[120:123], a[96:99], a[16:19], v[120:123]\n"
  "v_mfma_f32_16x16x32_bf16 v[120:123], a[100:103], a[20:23], v[120:123]\n"
  "ds_read_b64_tr_b16 a[124:125], v14 offset:8232\n"
  "v_mfma_f32_16x16x32_bf16 v[152:155], a[96:99], a[24:27], v[152:155]\n"
  "ds_read_b64_tr_b16 a[126:127], v14 offset:9256\n"
  "v_mfma_f32_16x16x32_bf16 v[152:155], a[100:103], a[28:31], v[152:155]\n"
  "buffer_load_dwordx4 v21, s[36:39], s69 offen offset:1024 lds\n"
  "v_mfma_f32_16x16x32_bf16 v[184:187], a[96:99], a[32:35], v[184:187]\n"
  "ds_read_b64_tr_b16 a[128:129], v15 offset:0\n"
  "v_mfma_f32_16x16x32_bf16 v[184:187], a[100:103], a[36:39], v[184:187]\n"
  "s_waitcnt lgkmcnt(11)\n"
  "v_mfma_f32_16x16x32_bf16 v[188:191], a[104:107], a[32:35], v[188:191]\n"
  "s_waitcnt lgkmcnt(9)\n"
  "ds_read_b64_tr_b16 a[130:131], v15 offset:1024\n"
  "v_mfma_f32_16x16x32_bf16 v[188:191], a[108:111], a[36:39], v[188:191]\n"
  "ds_read_b64_tr_b16 a[132:133], v15 offset:8224\n"
  "v_mfma_f32_16x16x32_bf16 v[156:159], a[104:107], a[24:27], v[156:159]\n"
  "buffer_load_dwordx4 v22, s[36:39], s69 offen offset:2048 lds\n"
  "v_mfma_f32_16x16x32_bf16 v[156:159], a[108:111], a[28:31], v[156:159]\n"
  "ds_read_b64_tr_b16 a[134:135], v15 offset:9248\n"
  "v_mfma_f32_16x16x32_bf16 v[124:127], a[104:107], a[16:19], v[124:127]\n"
  "ds_read_b64_tr_b16 a[136:137], v15 offset:8\n"
  "v_mfma_f32_16x16x32_bf16 v[124:127], a[108:111], a[20:23], v[124:127]\n"
  "v_mfma_f32_16x16x32_bf16 v[92:95], a[104:107], a[8:11], v[92:95]\n"
  "ds_read_b64_tr_b16 a[138:139], v15 offset:1032\n"
  "v_mfma_f32_16x16x32_bf16 v[92:95], a[108:111], a[12:15], v[92:95]\n"
  "buffer_load_dwordx4 v23, s[36:39], s69 offen offset:3072 lds\n"
  "v_mfma_f32_16x16x32_bf16 v[204:207], a[104:107], a[0:3], v[204:207]\n"
  "ds_read_b64_tr_b16 a[140:141], v15 offset:8232\n"
  "v_mfma_f32_16x16x32_bf16 v[204:207], a[108:111], a[4:7], v[204:207]\n"
  "s_waitcnt lgkmcnt(13)\n"
  "ds_read_b64_tr_b16 a[142:143], v15 offset:9256\n"
  "v_mfma_f32_16x16x32_bf16 v[208:211], a[112:115], a[0:3], v[208:211]\n"
  "s_waitcnt lgkmcnt(12)\n"
  "v_mfma_f32_16x16x32_bf16 v[208:211], a[116:119], a[4:7], v[208:211]\n"
  "v_mfma_f32_16x16x32_bf16 v[96:99], a[112:115], a[8:11], v[96:99]\n"
  "s_add_u32 m0, s66, 86080\n"
  "v_mfma_f32_16x16x32_bf16 v[96:99], a[116:119], a[12:15], v[96:99]\n"
  "buffer_load_dwordx4 v24, s[36:39], s69 offen lds\n"
  "v_mfma_f32_16x16x32_bf16 v[128:131], a[112:115], a[16:19], v[128:131]\n"
  "s_add_u32 s69, s69, 128\n"
  "v_mfma_f32_16x16x32_bf16 v[128:131], a[116:119], a[20:23], v[128:131]\n"
  "v_mfma_f32_16x16x32_bf16 v[160:163], a[112:115], a[24:27], v[160:163]\n"
  "s_waitcnt vmcnt(13) lgkmcnt(0)\n"
  "s_barrier\n"
  "v_mfma_f32_16x16x32_bf16 v[160:163], a[116:119], a[28:31], v[160:163]\n"
  "ds_read_b64_tr_b16 a[80:81], v8 offset:0\n"
  "v_mfma_f32_16x16x32_bf16 v[164:167], a[120:123], a[24:27], v[164:167]\n"
  "ds_read_b64_tr_b16 a[82:83], v8 offset:1024\n"
  "v_mfma_f32_16x16x32_bf16 v[164:167], a[124:127], a[28:31], v[164:167]\n"
  "s_add_u32 m0, s87, 143488\n"
  "ds_read_b128 a[40:43], v0 offset:0\n"
  "v_mfma_f32_16x16x32_bf16 v[132:135], a[120:123], a[16:19], v[132:135]\n"
  "buffer_load_dwordx4 v25, s[40:43], s70 offen lds\n"
  "v_mfma_f32_16x16x32_bf16 v[132:135], a[124:127], a[20:23], v[132:135]\n"
  "ds_read_b64_tr_b16 a[84:85], v8 offset:8224\n"
  "v_mfma_f32_16x16x32_bf16 v[100:103], a[120:123], a[8:11], v[100:103]\n"
  "v_mfma_f32_16x16x32_bf16 v[100:103], a[124:127], a[12:15], v[100:103]\n"
  "ds_read_b64_tr_b16 a[86:87], v8 offset:9248\n"
  "v_mfma_f32_16x16x32_bf16 v[212:215], a[120:123], a[0:3], v[212:215]\n"
  "ds_read_b128 a[44:47], v1 offset:0\n"
  "v_mfma_f32_16x16x32_bf16 v[212:215], a[124:127], a[4:7], v[212:215]\n"
  "ds_read_b128 a[48:51], v0 offset:2048\n"
  "v_mfma_f32_16x16x32_bf16 v[216:219], a[128:131], a[0:3], v[216:219]\n"
  "buffer_load_dwordx4 v26, s[40:43], s70 offen offset:1024 lds\n"
  "v_mfma_f32_16x16x32_bf16 v[216:219], a[132:135], a[4:7], v[216:219]\n"
  "v_mfma_f32_16x16x32_bf16 v[104:107], a[128:131], a[8:11], v[104:107]\n"
  "ds_read_b128 a[52:55], v1 offset:2048\n"
  "v_mfma_f32_16x16x32_bf16 v[104:107], a[132:135], a[12:15], v[104:107]\n"
  "ds_read_b128 a[56:59], v0 offset:4096\n"
  "v_mfma_f32_16x16x32_bf16 v[136:139], a[128:131], a[16:19], v[136:139]\n"
  "ds_read_b128 a[60:63], v1 offset:4096\n"
  "v_mfma_f32_16x16x32_bf16 v[136:139], a[132:135], a[20:23], v[136:139]\n"
  "buffer_load_dwordx4 v27, s[40:43], s70 offen offset:2056 lds\n"
  "v_mfma_f32_16x16x32_bf16 v[168:171], a[128:131], a[24:27], v[168:171]\n"
  "ds_read_b128 a[64:67], v0 offset:6144\n"
  "v_mfma_f32_16x16x32_bf16 v[168:171], a[132:135], a[28:31], v[168:171]\n"
  "v_mfma_f32_16x16x32_bf16 v[172:175], a[136:139], a[24:27], v[172:175]\n"
  "ds_read_b128 a[68:71], v1 offset:6144\n"
  "v_mfma_f32_16x16x32_bf16 v[172:175], a[140:143], a[28:31], v[172:175]\n"
  "ds_read_b128 a[72:75], v4 offset:0\n"
  "v_mfma_f32_16x16x32_bf16 v[140:143], a[136:139], a[16:19], v[140:143]\n"
  "buffer_load_dwordx4 v28, s[40:43], s70 offen offset:3080 lds\n"
  "v_mfma_f32_16x16x32_bf16 v[140:143], a[140:143], a[20:23], v[140:143]\n"
  "s_add_u32 s70, s70, 524288\n"
  "ds_read_b128 a[76:79], v5 offset:0\n"
  "v_mfma_f32_16x16x32_bf16 v[108:111], a[136:139], a[8:11], v[108:111]\n"
  "v_mfma_f32_16x16x32_bf16 v[108:111], a[140:143], a[12:15], v[108:111]\n"
  "ds_read_b64_tr_b16 a[88:89], v8 offset:8\n"
  "v_mfma_f32_16x16x32_bf16 v[220:223], a[136:139], a[0:3], v[220:223]\n"
  "s_add_u32 m0, s68, 106624\n"
  "ds_read_b64_tr_b16 a[90:91], v8 offset:1032\n"
  "v_mfma_f32_16x16x32_bf16 v[220:223], a[140:143], a[4:7], v[220:223]\n"
  "s_waitcnt lgkmcnt(13)\n"
  "ds_read_b64_tr_b16 a[92:93], v8 offset:8232\n"
  "v_mfma_f32_16x16x32_bf16 v[192:195], a[80:83], a[40:43], v[192:195]\n"
  "s_waitcnt lgkmcnt(11)\n"
  "buffer_load_dwordx4 v16, s[36:39], s69 offen lds\n"
  "v_mfma_f32_16x16x32_bf16 v[192:195], a[84:87], a[44:47], v[192:195]\n"
  "s_waitcnt lgkmcnt(10)\n"
  "ds_read_b64_tr_b16 a[94:95], v8 offset:9256\n"
  "v_mfma_f32_16x16x32_bf16 v[80:83], a[80:83], a[48:51], v[80:83]\n"
  "s_waitcnt lgkmcnt(10)\n"
  "v_mfma_f32_16x16x32_bf16 v[80:83], a[84:87], a[52:55], v[80:83]\n"
  "s_waitcnt lgkmcnt(9)\n"
  "ds_read_b64_tr_b16 a[96:97], v9 offset:0\n"
  "v_mfma_f32_16x16x32_bf16 v[112:115], a[80:83], a[56:59], v[112:115]\n"
  "s_waitcnt lgkmcnt(9)\n"
  "ds_read_b64_tr_b16 a[98:99], v9 offset:1024\n"
  "v_mfma_f32_16x16x32_bf16 v[112:115], a[84:87], a[60:63], v[112:115]\n"
  "s_waitcnt lgkmcnt(9)\n"
  "buffer_load_dwordx4 v17, s[36:39], s69 offen offset:1024 lds\n"
  "v_mfma_f32_16x16x32_bf16 v[144:147], a[80:83], a[64:67], v[144:147]\n"
  "s_waitcnt lgkmcnt(8)\n"
  "ds_read_b64_tr_b16 a[100:101], v9 offset:8224\n"
  "v_mfma_f32_16x16x32_bf16 v[144:147], a[84:87], a[68:71], v[144:147]\n"
  "s_waitcnt lgkmcnt(8)\n"
  "v_mfma_f32_16x16x32_bf16 v[176:179], a[80:83], a[72:75], v[176:179]\n"
  "s_waitcnt lgkmcnt(7)\n"
  "ds_read_b64_tr_b16 a[102:103], v9 offset:9248\n"
  "v_mfma_f32_16x16x32_bf16 v[176:179], a[84:87], a[76:79], v[176:179]\n"
  "s_waitcnt lgkmcnt(6)\n"
  "ds_read_b64_tr_b16 a[104:105], v9 offset:8\n"
  "v_mfma_f32_16x16x32_bf16 v[180:183], a[88:91], a[72:75], v[180:183]\n"
  "s_waitcnt lgkmcnt(5)\n"
  "ds_read_b64_tr_b16 a[106:107], v9 offset:1032\n"
  "v_mfma_f32_16x16x32_bf16 v[180:183], a[92:95], a[76:79], v[180:183]\n"
  "buffer_load_dwordx4 v18, s[36:39], s69 offen offset:2048 lds\n"
  "v_mfma_f32_16x16x32_bf16 v[148:151], a[88:91], a[64:67], v[148:151]\n"
  "ds_read_b64_tr_b16 a[108:109], v9 offset:8232\n"
  "v_mfma_f32_16x16x32_bf16 v[148:151], a[92:95], a[68:71], v[148:151]\n"
  "v_mfma_f32_16x16x32_bf16 v[116:119], a[88:91], a[56:59], v[116:119]\n"
  "ds_read_b64_tr_b16 a[110:111], v9 offset:9256\n"
  "v_mfma_f32_16x16x32_bf16 v[116:119], a[92:95], a[60:63], v[116:119]\n"
  "ds_read_b64_tr_b16 a[112:113], v10 offset:0\n"
  "v_mfma_f32_16x16x32_bf16 v[84:87], a[88:91], a[48:51], v[84:87]\n"
  "buffer_load_dwordx4 v19, s[36:39], s69 offen offset:3072 lds\n"
  "v_mfma_f32_16x16x32_bf16 v[84:87], a[92:95], a[52:55], v[84:87]\n"
  "ds_read_b64_tr_b16 a[114:115], v10 offset:1024\n"
  "v_mfma_f32_16x16x32_bf16 v[196:199], a[88:91], a[40:43], v[196:199]\n"
  "s_add_u32 m0, s68, 110720\n"
  "v_mfma_f32_16x16x32_bf16 v[196:199], a[92:95], a[44:47], v[196:199]\n"
  "s_waitcnt lgkmcnt(8)\n"
  "ds_read_b64_tr_b16 a[116:117], v10 offset:8224\n"
  "v_mfma_f32_16x16x32_bf16 v[200:203], a[96:99], a[40:43], v[200:203]\n"
  "s_waitcnt lgkmcnt(7)\n"
  "ds_read_b64_tr_b16 a[118:119], v10 offset:9248\n"
  "v_mfma_f32_16x16x32_bf16 v[200:203], a[100:103], a[44:47], v[200:203]\n"
  "buffer_load_dwordx4 v20, s[36:39], s69 offen lds\n"
  "v_mfma_f32_16x16x32_bf16 v[88:91], a[96:99], a[48:51], v[88:91]\n"
  "ds_read_b64_tr_b16 a[120:121], v10 offset:8\n"
  "v_mfma_f32_16x16x32_bf16 v[88:91], a[100:103], a[52:55], v[88:91]\n"
  "ds_read_b64_tr_b16 a[122:123], v10 offset:1032\n"
  "v_mfma_f32_16x16x32_bf16 v[120:123], a[96:99], a[56:59], v[120:123]\n"
  "v_mfma_f32_16x16x32_bf16 v[120:123], a[100:103], a[60:63], v[120:123]\n"
  "ds_read_b64_tr_b16 a[124:125], v10 offset:8232\n"
  "v_mfma_f32_16x16x32_bf16 v[152:155], a[96:99], a[64:67], v[152:155]\n"
  "ds_read_b64_tr_b16 a[126:127], v10 offset:9256\n"
  "v_mfma_f32_16x16x32_bf16 v[152:155], a[100:103], a[68:71], v[152:155]\n"
  "buffer_load_dwordx4 v21, s[36:39], s69 offen offset:1024 lds\n"
  "v_mfma_f32_16x16x32_bf16 v[184:187], a[96:99], a[72:75], v[184:187]\n"
  "ds_read_b64_tr_b16 a[128:129], v11 offset:0\n"
  "v_mfma_f32_16x16x32_bf16 v[184:187], a[100:103], a[76:79], v[184:187]\n"
  "s_waitcnt lgkmcnt(11)\n"
  "v_mfma_f32_16x16x32_bf16 v[188:191], a[104:107], a[72:75], v[188:191]\n"
  "s_waitcnt lgkmcnt(9)\n"
  "ds_read_b64_tr_b16 a[130:131], v11 offset:1024\n"
  "v_mfma_f32_16x16x32_bf16 v[188:191], a[108:111], a[76:79], v[188:191]\n"
  "ds_read_b64_tr_b16 a[132:133], v11 offset:8224\n"
  "v_mfma_f32_16x16x32_bf16 v[156:159], a[104:107], a[64:67], v[156:159]\n"
  "buffer_load_dwordx4 v22, s[36:39], s69 offen offset:2048 lds\n"
  "v_mfma_f32_16x16x32_bf16 v[156:159], a[108:111], a[68:71], v[156:159]\n"
  "ds_read_b64_tr_b16 a[134:135], v11 offset:9248\n"
  "v_mfma_f32_16x16x32_bf16 v[124:127], a[104:107], a[56:59], v[124:127]\n"
  "ds_read_b64_tr_b16 a[136:137], v11 offset:8\n"
  "v_mfma_f32_16x16x32_bf16 v[124:127], a[108:111], a[60:63], v[124:127]\n"
  "v_mfma_f32_16x16x32_bf16 v[92:95], a[104:107], a[48:51], v[92:95]\n"
  "ds_read_b64_tr_b16 a[138:139], v11 offset:1032\n"
  "v_mfma_f32_16x16x32_bf16 v[92:95], a[108:111], a[52:55], v[92:95]\n"
  "buffer_load_dwordx4 v23, s[36:39], s69 offen offset:3072 lds\n"
  "v_mfma_f32_16x16x32_bf16 v[204:207], a[104:107], a[40:43], v[204:207]\n"
  "ds_read_b64_tr_b16 a[140:141], v11 offset:8232\n"
  "v_mfma_f32_16x16x32_bf16 v[204:207], a[108:111], a[44:47], v[204:207]\n"
  "s_waitcnt lgkmcnt(13)\n"
  "ds_read_b64_tr_b16 a[142:143], v11 offset:9256\n"
  "v_mfma_f32_16x16x32_bf16 v[208:211], a[112:115], a[40:43], v[208:211]\n"
  "s_waitcnt lgkmcnt(12)\n"
  "v_mfma_f32_16x16x32_bf16 v[208:211], a[116:119], a[44:47], v[208:211]\n"
  "v_mfma_f32_16x16x32_bf16 v[96:99], a[112:115], a[48:51], v[96:99]\n"
  "s_add_u32 s77, s76, 1\n"
  "v_add_u32 v65, s77, v65\n"
  "v_max_u32 v65, 1, v65\n"
  "v_mfma_f32_16x16x32_bf16 v[96:99], a[116:119], a[52:55], v[96:99]\n"
  "s_add_u32 m0, s66, 139392\n"
  "v_readfirstlane_b32 s80, v65\n"
  "v_mfma_f32_16x16x32_bf16 v[128:131], a[112:115], a[56:59], v[128:131]\n"
  "buffer_load_dwordx4 v24, s[36:39], s69 offen lds\n"
  "v_mfma_f32_16x16x32_bf16 v[128:131], a[116:119], a[60:63], v[128:131]\n"
  "s_add_u32 s69, s69, 128\n"
  "v_mfma_f32_16x16x32_bf16 v[160:163], a[112:115], a[64:67], v[160:163]\n"
  "s_waitcnt vmcnt(13) lgkmcnt(0)\n"
  "s_barrier\n"
  "v_mfma_f32_16x16x32_bf16 v[160:163], a[116:119], a[68:71], v[160:163]\n"
  "ds_read_b64_tr_b16 a[80:81], v8 offset:53312\n"
  "v_mfma_f32_16x16x32_bf16 v[164:167], a[120:123], a[64:67], v[164:167]\n"
  "ds_read_b64_tr_b16 a[82:83], v8 offset:54336\n"
  "v_mfma_f32_16x16x32_bf16 v[164:167], a[124:127], a[68:71], v[164:167]\n"
  "s_add_u32 m0, s87, 36864\n"
  "ds_read_b128 a[0:3], v0 offset:53312\n"
  "v_mfma_f32_16x16x32_bf16 v[132:135], a[120:123], a[56:59], v[132:135]\n"
  "buffer_load_dwordx4 v25, s[40:43], s70 offen lds\n"
  "v_mfma_f32_16x16x32_bf16 v[132:135], a[124:127], a[60:63], v[132:135]\n"
  "ds_read_b64_tr_b16 a[84:85], v8 offset:61536\n"
  "v_mfma_f32_16x16x32_bf16 v[100:103], a[120:123], a[48:51], v[100:103]\n"
  "v_mfma_f32_16x16x32_bf16 v[100:103], a[124:127], a[52:55], v[100:103]\n"
  "ds_read_b64_tr_b16 a[86:87], v8 offset:62560\n"
  "v_mfma_f32_16x16x32_bf16 v[212:215], a[120:123], a[40:43], v[212:215]\n"
  "ds_read_b128 a[4:7], v1 offset:53312\n"
  "v_mfma_f32_16x16x32_bf16 v[212:215], a[124:127], a[44:47], v[212:215]\n"
  "ds_read_b128 a[8:11], v0 offset:55360\n"
  "v_mfma_f32_16x16x32_bf16 v[216:219], a[128:131], a[40:43], v[216:219]\n"
  "buffer_load_dwordx4 v26, s[40:43], s70 offen offset:1024 lds\n"
  "v_mfma_f32_16x16x32_bf16 v[216:219], a[132:135], a[44:47], v[216:219]\n"
  "v_mfma_f32_16x16x32_bf16 v[104:107], a[128:131], a[48:51], v[104:107]\n"
  "ds_read_b128 a[12:15], v1 offset:55360\n"
  "v_mfma_f32_16x16x32_bf16 v[104:107], a[132:135], a[52:55], v[104:107]\n"
  "ds_read_b128 a[16:19], v0 offset:57408\n"
  "v_mfma_f32_16x16x32_bf16 v[136:139], a[128:131], a[56:59], v[136:139]\n"
  "ds_read_b128 a[20:23], v1 offset:57408\n"
  "v_mfma_f32_16x16x32_bf16 v[136:139], a[132:135], a[60:63], v[136:139]\n"
  "buffer_load_dwordx4 v27, s[40:43], s70 offen offset:2056 lds\n"
  "v_mfma_f32_16x16x32_bf16 v[168:171], a[128:131], a[64:67], v[168:171]\n"
  "ds_read_b128 a[24:27], v0 offset:59456\n"
  "v_mfma_f32_16x16x32_bf16 v[168:171], a[132:135], a[68:71], v[168:171]\n"
  "v_mfma_f32_16x16x32_bf16 v[172:175], a[136:139], a[64:67], v[172:175]\n"
  "ds_read_b128 a[28:31], v1 offset:59456\n"
  "v_mfma_f32_16x16x32_bf16 v[172:175], a[140:143], a[68:71], v[172:175]\n"
  "ds_read_b128 a[32:35], v4 offset:53312\n"
  "v_mfma_f32_16x16x32_bf16 v[140:143], a[136:139], a[56:59], v[140:143]\n"
  "buffer_load_dwordx4 v28, s[40:43], s70 offen offset:3080 lds\n"
  "v_mfma_f32_16x16x32_bf16 v[140:143], a[140:143], a[60:63], v[140:143]\n"
  "s_add_u32 s70, s70, 524288\n"
  "ds_read_b128 a[36:39], v5 offset:53312\n"
  "v_mfma_f32_16x16x32_bf16 v[108:111], a[136:139], a[48:51], v[108:111]\n"
  "v_mfma_f32_16x16x32_bf16 v[108:111], a[140:143], a[52:55], v[108:111]\n"
  "ds_read_b64_tr_b16 a[88:89], v8 offset:53320\n"
  "v_mfma_f32_16x16x32_bf16 v[220:223], a[136:139], a[40:43], v[220:223]\n"
  "s_add_u32 m0, s68, 0\n"
  "ds_read_b64_tr_b16 a[90:91], v8 offset:54344\n"
  "v_mfma_f32_16x16x32_bf16 v[220:223], a[140:143], a[44:47], v[220:223]\n"
  "s_waitcnt lgkmcnt(13)\n"
  "ds_read_b64_tr_b16 a[92:93], v8 offset:61544\n"
  "v_mfma_f32_16x16x32_bf16 v[192:195], a[80:83], a[0:3], v[192:195]\n"
  "s_waitcnt lgkmcnt(11)\n"
  "buffer_load_dwordx4 v16, s[36:39], s69 offen lds\n"
  "v_mfma_f32_16x16x32_bf16 v[192:195], a[84:87], a[4:7], v[192:195]\n"
  "s_waitcnt lgkmcnt(10)\n"
  "ds_read_b64_tr_b16 a[94:95], v8 offset:62568\n"
  "v_mfma_f32_16x16x32_bf16 v[80:83], a[80:83], a[8:11], v[80:83]\n"
  "s_waitcnt lgkmcnt(10)\n"
  "v_mfma_f32_16x16x32_bf16 v[80:83], a[84:87], a[12:15], v[80:83]\n"
  "s_waitcnt lgkmcnt(9)\n"
  "ds_read_b64_tr_b16 a[96:97], v9 offset:53312\n"
  "v_mfma_f32_16x16x32_bf16 v[112:115], a[80:83], a[16:19], v[112:115]\n"
  "s_waitcnt lgkmcnt(9)\n"
  "ds_read_b64_tr_b16 a[98:99], v9 offset:54336\n"
  "v_mfma_f32_16x16x32_bf16 v[112:115], a[84:87], a[20:23], v[112:115]\n"
  "s_waitcnt lgkmcnt(9)\n"
  "buffer_load_dwordx4 v17, s[36:39], s69 offen offset:1024 lds\n"
  "v_mfma_f32_16x16x32_bf16 v[144:147], a[80:83], a[24:27], v[144:147]\n"
  "s_waitcnt lgkmcnt(8)\n"
  "ds_read_b64_tr_b16 a[100:101], v9 offset:61536\n"
  "v_mfma_f32_16x16x32_bf16 v[144:147], a[84:87], a[28:31], v[144:147]\n"
  "s_waitcnt lgkmcnt(8)\n"
  "v_mfma_f32_16x16x32_bf16 v[176:179], a[80:83], a[32:35], v[176:179]\n"
  "s_waitcnt lgkmcnt(7)\n"
  "ds_read_b64_tr_b16 a[102:103], v9 offset:62560\n"
  "v_mfma_f32_16x16x32_bf16 v[176:179], a[84:87], a[36:39], v[176:179]\n"
  "s_waitcnt lgkmcnt(6)\n"
  "ds_read_b64_tr_b16 a[104:105], v9 offset:53320\n"
  "v_mfma_f32_16x16x32_bf16 v[180:183], a[88:91], a[32:35], v[180:183]\n"
  "s_waitcnt lgkmcnt(5)\n"
  "ds_read_b64_tr_b16 a[106:107], v9 offset:54344\n"
  "v_mfma_f32_16x16x32_bf16 v[180:183], a[92:95], a[36:39], v[180:183]\n"
  "buffer_load_dwordx4 v18, s[36:39], s69 offen offset:2048 lds\n"
  "v_mfma_f32_16x16x32_bf16 v[148:151], a[88:91], a[24:27], v[148:151]\n"
  "ds_read_b64_tr_b16 a[108:109], v9 offset:61544\n"
  "v_mfma_f32_16x16x32_bf16 v[148:151], a[92:95], a[28:31], v[148:151]\n"
  "v_mfma_f32_16x16x32_bf16 v[116:119], a[88:91], a[16:19], v[116:119]\n"
  "ds_read_b64_tr_b16 a[110:111], v9 offset:62568\n"
  "v_mfma_f32_16x16x32_bf16 v[116:119], a[92:95], a[20:23], v[116:119]\n"
  "ds_read_b64_tr_b16 a[112:113], v10 offset:53312\n"
  "v_mfma_f32_16x16x32_bf16 v[84:87], a[88:91], a[8:11], v[84:87]\n"
  "buffer_load_dwordx4 v19, s[36:39], s69 offen offset:3072 lds\n"
  "v_mfma_f32_16x16x32_bf16 v[84:87], a[92:95], a[12:15], v[84:87]\n"
  "ds_read_b64_tr_b16 a[114:115], v10 offset:54336\n"
  "v_mfma_f32_16x16x32_bf16 v[196:199], a[88:91], a[0:3], v[196:199]\n"
  "s_add_u32 m0, s68, 4096\n"
  "v_mfma_f32_16x16x32_bf16 v[196:199], a[92:95], a[4:7], v[196:199]\n"
  "s_waitcnt lgkmcnt(8)\n"
  "ds_read_b64_tr_b16 a[116:117], v10 offset:61536\n"
  "v_mfma_f32_16x16x32_bf16 v[200:203], a[96:99], a[0:3], v[200:203]\n"
  "s_waitcnt lgkmcnt(7)\n"
  "ds_read_b64_tr_b16 a[118:119], v10 offset:62560\n"
  "v_mfma_f32_16x16x32_bf16 v[200:203], a[100:103], a[4:7], v[200:203]\n"
  "buffer_load_dwordx4 v20, s[36:39], s69 offen lds\n"
  "v_mfma_f32_16x16x32_bf16 v[88:91], a[96:99], a[8:11], v[88:91]\n"
  "ds_read_b64_tr_b16 a[120:121], v10 offset:53320\n"
  "v_mfma_f32_16x16x32_bf16 v[88:91], a[100:103], a[12:15], v[88:91]\n"
  "ds_read_b64_tr_b16 a[122:123], v10 offset:54344\n"
  "v_mfma_f32_16x16x32_bf16 v[120:123], a[96:99], a[16:19], v[120:123]\n"
  "v_mfma_f32_16x16x32_bf16 v[120:123], a[100:103], a[20:23], v[120:123]\n"
  "ds_read_b64_tr_b16 a[124:125], v10 offset:61544\n"
  "v_mfma_f32_16x16x32_bf16 v[152:155], a[96:99], a[24:27], v[152:155]\n"
  "ds_read_b64_tr_b16 a[126:127], v10 offset:62568\n"
  "v_mfma_f32_16x16x32_bf16 v[152:155], a[100:103], a[28:31], v[152:155]\n"
  "buffer_load_dwordx4 v21, s[36:39], s69 offen offset:1024 lds\n"
  "v_mfma_f32_16x16x32_bf16 v[184:187], a[96:99], a[32:35], v[184:187]\n"
  "ds_read_b64_tr_b16 a[128:129], v11 offset:53312\n"
  "v_mfma_f32_16x16x32_bf16 v[184:187], a[100:103], a[36:39], v[184:187]\n"
  "s_waitcnt lgkmcnt(11)\n"
  "v_mfma_f32_16x16x32_bf16 v[188:191], a[104:107], a[32:35], v[188:191]\n"
  "s_waitcnt lgkmcnt(9)\n"
  "ds_read_b64_tr_b16 a[130:131], v11 offset:54336\n"
  "v_mfma_f32_16x16x32_bf16 v[188:191], a[108:111], a[36:39], v[188:191]\n"
  "ds_read_b64_tr_b16 a[132:133], v11 offset:61536\n"
  "v_mfma_f32_16x16x32_bf16 v[156:159], a[104:107], a[24:27], v[156:159]\n"
  "buffer_load_dwordx4 v22, s[36:39], s69 offen offset:2048 lds\n"
  "v_mfma_f32_16x16x32_bf16 v[156:159], a[108:111], a[28:31], v[156:159]\n"
  "ds_read_b64_tr_b16 a[134:135], v11 offset:62560\n"
  "v_mfma_f32_16x16x32_bf16 v[124:127], a[104:107], a[16:19], v[124:127]\n"
  "ds_read_b64_tr_b16 a[136:137], v11 offset:53320\n"
  "v_mfma_f32_16x16x32_bf16 v[124:127], a[108:111], a[20:23], v[124:127]\n"
  "v_mfma_f32_16x16x32_bf16 v[92:95], a[104:107], a[8:11], v[92:95]\n"
  "ds_read_b64_tr_b16 a[138:139], v11 offset:54344\n"
  "v_mfma_f32_16x16x32_bf16 v[92:95], a[108:111], a[12:15], v[92:95]\n"
  "buffer_load_dwordx4 v23, s[36:39], s69 offen offset:3072 lds\n"
  "v_mfma_f32_16x16x32_bf16 v[204:207], a[104:107], a[0:3], v[204:207]\n"
  "ds_read_b64_tr_b16 a[140:141], v11 offset:61544\n"
  "v_mfma_f32_16x16x32_bf16 v[204:207], a[108:111], a[4:7], v[204:207]\n"
  "s_waitcnt lgkmcnt(13)\n"
  "ds_read_b64_tr_b16 a[142:143], v11 offset:62568\n"
  "v_mfma_f32_16x16x32_bf16 v[208:211], a[112:115], a[0:3], v[208:211]\n"
  "s_waitcnt lgkmcnt(12)\n"
  "v_mfma_f32_16x16x32_bf16 v[208:211], a[116:119], a[4:7], v[208:211]\n"
  "v_mfma_f32_16x16x32_bf16 v[96:99], a[112:115], a[8:11], v[96:99]\n"
  "s_add_u32 m0, s66, 32768\n"
  "v_mfma_f32_16x16x32_bf16 v[96:99], a[116:119], a[12:15], v[96:99]\n"
  "buffer_load_dwordx4 v24, s[36:39], s69 offen lds\n"
  "v_mfma_f32_16x16x32_bf16 v[128:131], a[112:115], a[16:19], v[128:131]\n"
  "s_add_u32 s69, s69, 128\n"
  "v_mfma_f32_16x16x32_bf16 v[128:131], a[116:119], a[20:23], v[128:131]\n"
  "v_mfma_f32_16x16x32_bf16 v[160:163], a[112:115], a[24:27], v[160:163]\n"
  "s_waitcnt vmcnt(13) lgkmcnt(0)\n"
  "s_barrier\n"
  "v_mfma_f32_16x16x32_bf16 v[160:163], a[116:119], a[28:31], v[160:163]\n"
  "ds_read_b64_tr_b16 a[80:81], v12 offset:0\n"
  "v_mfma_f32_16x16x32_bf16 v[164:167], a[120:123], a[24:27], v[164:167]\n"
  "ds_read_b64_tr_b16 a[82:83], v12 offset:1024\n"
  "v_mfma_f32_16x16x32_bf16 v[164:167], a[124:127], a[28:31], v[164:167]\n"
  "s_add_u32 m0, s87, 90176\n"
  "ds_read_b128 a[40:43], v2 offset:0\n"
  "v_mfma_f32_16x16x32_bf16 v[132:135], a[120:123], a[16:19], v[132:135]\n"
  "buffer_load_dwordx4 v25, s[40:43], s70 offen lds\n"
  "v_mfma_f32_16x16x32_bf16 v[132:135], a[124:127], a[20:23], v[132:135]\n"
  "ds_read_b64_tr_b16 a[84:85], v12 offset:8224\n"
  "v_mfma_f32_16x16x32_bf16 v[100:103], a[120:123], a[8:11], v[100:103]\n"
  "v_mfma_f32_16x16x32_bf16 v[100:103], a[124:127], a[12:15], v[100:103]\n"
  "ds_read_b64_tr_b16 a[86:87], v12 offset:9248\n"
  "v_mfma_f32_16x16x32_bf16 v[212:215], a[120:123], a[0:3], v[212:215]\n"
  "ds_read_b128 a[44:47], v3 offset:0\n"
  "v_mfma_f32_16x16x32_bf16 v[212:215], a[124:127], a[4:7], v[212:215]\n"
  "ds_read_b128 a[48:51], v2 offset:2048\n"
  "v_mfma_f32_16x16x32_bf16 v[216:219], a[128:131], a[0:3], v[216:219]\n"
  "buffer_load_dwordx4 v26, s[40:43], s70 offen offset:1024 lds\n"
  "v_mfma_f32_16x16x32_bf16 v[216:219], a[132:135], a[4:7], v[216:219]\n"
  "v_mfma_f32_16x16x32_bf16 v[104:107], a[128:131], a[8:11], v[104:107]\n"
  "ds_read_b128 a[52:55], v3 offset:2048\n"
  "v_mfma_f32_16x16x32_bf16 v[104:107], a[132:135], a[12:15], v[104:107]\n"
  "ds_read_b128 a[56:59], v2 offset:4096\n"
  "v_mfma_f32_16x16x32_bf16 v[136:139], a[128:131], a[16:19], v[136:139]\n"
  "ds_read_b128 a[60:63], v3 offset:4096\n"
  "v_mfma_f32_16x16x32_bf16 v[136:139], a[132:135], a[20:23], v[136:139]\n"
  "buffer_load_dwordx4 v27, s[40:43], s70 offen offset:2056 lds\n"
  "v_mfma_f32_16x16x32_bf16 v[168:171], a[128:131], a[24:27], v[168:171]\n"
  "ds_read_b128 a[64:67], v2 offset:6144\n"
  "v_mfma_f32_16x16x32_bf16 v[168:171], a[132:135], a[28:31], v[168:171]\n"
  "v_mfma_f32_16x16x32_bf16 v[172:175], a[136:139], a[24:27], v[172:175]\n"
  "ds_read_b128 a[68:71], v3 offset:6144\n"
  "v_mfma_f32_16x16x32_bf16 v[172:175], a[140:143], a[28:31], v[172:175]\n"
  "ds_read_b128 a[72:75], v6 offset:0\n"
  "v_mfma_f32_16x16x32_bf16 v[140:143], a[136:139], a[16:19], v[140:143]\n"
  "buffer_load_dwordx4 v28, s[40:43], s70 offen offset:3080 lds\n"
  "v_mfma_f32_16x16x32_bf16 v[140:143], a[140:143], a[20:23], v[140:143]\n"
  "s_add_u32 s70, s70, 524288\n"
  "ds_read_b128 a[76:79], v7 offset:0\n"
  "v_mfma_f32_16x16x32_bf16 v[108:111], a[136:139], a[8:11], v[108:111]\n"
  "s_and_b32 s70, s70, 0x3fffff\n"
  "v_mfma_f32_16x16x32_bf16 v[108:111], a[140:143], a[12:15], v[108:111]\n"
  "ds_read_b64_tr_b16 a[88:89], v12 offset:8\n"
  "v_mfma_f32_16x16x32_bf16 v[220:223], a[136:139], a[0:3], v[220:223]\n"
  "s_add_u32 m0, s68, 53312\n"
  "ds_read_b64_tr_b16 a[90:91], v12 offset:1032\n"
  "v_mfma_f32_16x16x32_bf16 v[220:223], a[140:143], a[4:7], v[220:223]\n"
  "s_waitcnt lgkmcnt(13)\n"
  "ds_read_b64_tr_b16 a[92:93], v12 offset:8232\n"
  "v_mfma_f32_16x16x32_bf16 v[192:195], a[80:83], a[40:43], v[192:195]\n"
  "s_waitcnt lgkmcnt(11)\n"
  "buffer_load_dwordx4 v16, s[36:39], s69 offen lds\n"
  "v_mfma_f32_16x16x32_bf16 v[192:195], a[84:87], a[44:47], v[192:195]\n"
  "s_waitcnt lgkmcnt(10)\n"
  "ds_read_b64_tr_b16 a[94:95], v12 offset:9256\n"
  "v_mfma_f32_16x16x32_bf16 v[80:83], a[80:83], a[48:51], v[80:83]\n"
  "s_waitcnt lgkmcnt(10)\n"
  "v_mfma_f32_16x16x32_bf16 v[80:83], a[84:87], a[52:55], v[80:83]\n"
  "s_waitcnt lgkmcnt(9)\n"
  "ds_read_b64_tr_b16 a[96:97], v13 offset:0\n"
  "v_mfma_f32_16x16x32_bf16 v[112:115], a[80:83], a[56:59], v[112:115]\n"
  "s_waitcnt lgkmcnt(9)\n"
  "ds_read_b64_tr_b16 a[98:99], v13 offset:1024\n"
  "v_mfma_f32_16x16x32_bf16 v[112:115], a[84:87], a[60:63], v[112:115]\n"
  "s_waitcnt lgkmcnt(9)\n"
  "buffer_load_dwordx4 v17, s[36:39], s69 offen offset:1024 lds\n"
  "v_mfma_f32_16x16x32_bf16 v[144:147], a[80:83], a[64:67], v[144:147]\n"
  "s_waitcnt lgkmcnt(8)\n"
  "ds_read_b64_tr_b16 a[100:101], v13 offset:8224\n"
  "v_mfma_f32_16x16x32_bf16 v[144:147], a[84:87], a[68:71], v[144:147]\n"
  "s_waitcnt lgkmcnt(8)\n"
  "v_mfma_f32_16x16x32_bf16 v[176:179], a[80:83], a[72:75], v[176:179]\n"
  "s_waitcnt lgkmcnt(7)\n"
  "ds_read_b64_tr_b16 a[102:103], v13 offset:9248\n"
  "v_mfma_f32_16x16x32_bf16 v[176:179], a[84:87], a[76:79], v[176:179]\n"
  "s_waitcnt lgkmcnt(6)\n"
  "ds_read_b64_tr_b16 a[104:105], v13 offset:8\n"
  "v_mfma_f32_16x16x32_bf16 v[180:183], a[88:91], a[72:75], v[180:183]\n"
  "s_waitcnt lgkmcnt(5)\n"
  "ds_read_b64_tr_b16 a[106:107], v13 offset:1032\n"
  "v_mfma_f32_16x16x32_bf16 v[180:183], a[92:95], a[76:79], v[180:183]\n"
  "buffer_load_dwordx4 v18, s[36:39], s69 offen offset:2048 lds\n"
  "v_mfma_f32_16x16x32_bf16 v[148:151], a[88:91], a[64:67], v[148:151]\n"
  "ds_read_b64_tr_b16 a[108:109], v13 offset:8232\n"
  "v_mfma_f32_16x16x32_bf16 v[148:151], a[92:95], a[68:71], v[148:151]\n"
  "v_mfma_f32_16x16x32_bf16 v[116:119], a[88:91], a[56:59], v[116:119]\n"
  "ds_read_b64_tr_b16 a[110:111], v13 offset:9256\n"
  "v_mfma_f32_16x16x32_bf16 v[116:119], a[92:95], a[60:63], v[116:119]\n"
  "ds_read_b64_tr_b16 a[112:113], v14 offset:0\n"
  "v_mfma_f32_16x16x32_bf16 v[84:87], a[88:91], a[48:51], v[84:87]\n"
  "buffer_load_dwordx4 v19, s[36:39], s69 offen offset:3072 lds\n"
  "v_mfma_f32_16x16x32_bf16 v[84:87], a[92:95], a[52:55], v[84:87]\n"
  "ds_read_b64_tr_b16 a[114:115], v14 offset:1024\n"
  "v_mfma_f32_16x16x32_bf16 v[196:199], a[88:91], a[40:43], v[196:199]\n"
  "s_add_u32 m0, s68, 57408\n"
  "v_mfma_f32_16x16x32_bf16 v[196:199], a[92:95], a[44:47], v[196:199]\n"
  "s_waitcnt lgkmcnt(8)\n"
  "ds_read_b64_tr_b16 a[116:117], v14 offset:8224\n"
  "v_mfma_f32_16x16x32_bf16 v[200:203], a[96:99], a[40:43], v[200:203]\n"
  "s_waitcnt lgkmcnt(7)\n"
  "ds_read_b64_tr_b16 a[118:119], v14 offset:9248\n"
  "v_mfma_f32_16x16x32_bf16 v[200:203], a[100:103], a[44:47], v[200:203]\n"
  "buffer_load_dwordx4 v20, s[36:39], s69 offen lds\n"
  "v_mfma_f32_16x16x32_bf16 v[88:91], a[96:99], a[48:51], v[88:91]\n"
  "ds_read_b64_tr_b16 a[120:121], v14 offset:8\n"
  "v_mfma_f32_16x16x32_bf16 v[88:91], a[100:103], a[52:55], v[88:91]\n"
  "ds_read_b64_tr_b16 a[122:123], v14 offset:1032\n"
  "v_mfma_f32_16x16x32_bf16 v[120:123], a[96:99], a[56:59], v[120:123]\n"
  "v_mfma_f32_16x16x32_bf16 v[120:123], a[100:103], a[60:63], v[120:123]\n"
  "ds_read_b64_tr_b16 a[124:125], v14 offset:8232\n"
  "v_mfma_f32_16x16x32_bf16 v[152:155], a[96:99], a[64:67], v[152:155]\n"
  "ds_read_b64_tr_b16 a[126:127], v14 offset:9256\n"
  "v_mfma_f32_16x16x32_bf16 v[152:155], a[100:103], a[68:71], v[152:155]\n"
  "buffer_load_dwordx4 v21, s[36:39], s69 offen offset:1024 lds\n"
  "v_mfma_f32_16x16x32_bf16 v[184:187], a[96:99], a[72:75], v[184:187]\n"
  "ds_read_b64_tr_b16 a[128:129], v15 offset:0\n"
  "v_mfma_f32_16x16x32_bf16 v[184:187], a[100:103], a[76:79], v[184:187]\n"
  "s_waitcnt lgkmcnt(11)\n"
  "v_mfma_f32_16x16x32_bf16 v[188:191], a[104:107], a[72:75], v[188:191]\n"
  "s_waitcnt lgkmcnt(9)\n"
  "ds_read_b64_tr_b16 a[130:131], v15 offset:1024\n"
  "v_mfma_f32_16x16x32_bf16 v[188:191], a[108:111], a[76:79], v[188:191]\n"
  "ds_read_b64_tr_b16 a[132:133], v15 offset:8224\n"
  "v_mfma_f32_16x16x32_bf16 v[156:159], a[104:107], a[64:67], v[156:159]\n"
  "buffer_load_dwordx4 v22, s[36:39], s69 offen offset:2048 lds\n"
  "v_mfma_f32_16x16x32_bf16 v[156:159], a[108:111], a[68:71], v[156:159]\n"
  "ds_read_b64_tr_b16 a[134:135], v15 offset:9248\n"
  "v_mfma_f32_16x16x32_bf16 v[124:127], a[104:107], a[56:59], v[124:127]\n"
  "ds_read_b64_tr_b16 a[136:137], v15 offset:8\n"
  "v_mfma_f32_16x16x32_bf16 v[124:127], a[108:111], a[60:63], v[124:127]\n"
  "v_mfma_f32_16x16x32_bf16 v[92:95], a[104:107], a[48:51], v[92:95]\n"
  "ds_read_b64_tr_b16 a[138:139], v15 offset:1032\n"
  "v_mfma_f32_16x16x32_bf16 v[92:95], a[108:111], a[52:55], v[92:95]\n"
  "buffer_load_dwordx4 v23, s[36:39], s69 offen offset:3072 lds\n"
  "v_mfma_f32_16x16x32_bf16 v[204:207], a[104:107], a[40:43], v[204:207]\n"
  "ds_read_b64_tr_b16 a[140:141], v15 offset:8232\n"
  "v_mfma_f32_16x16x32_bf16 v[204:207], a[108:111], a[44:47], v[204:207]\n"
  "s_waitcnt lgkmcnt(13)\n"
  "ds_read_b64_tr_b16 a[142:143], v15 offset:9256\n"
  "v_mfma_f32_16x16x32_bf16 v[208:211], a[112:115], a[40:43], v[208:211]\n"
  "s_waitcnt lgkmcnt(12)\n"
  "v_mfma_f32_16x16x32_bf16 v[208:211], a[116:119], a[44:47], v[208:211]\n"
  "v_mfma_f32_16x16x32_bf16 v[96:99], a[112:115], a[48:51], v[96:99]\n"
  "s_add_u32 m0, s66, 86080\n"
  "v_mfma_f32_16x16x32_bf16 v[96:99], a[116:119], a[52:55], v[96:99]\n"
  "buffer_load_dwordx4 v24, s[36:39], s69 offen lds\n"
  "v_mfma_f32_16x16x32_bf16 v[128:131], a[112:115], a[56:59], v[128:131]\n"
  "s_add_u32 s69, s69, 128\n"
  "v_mfma_f32_16x16x32_bf16 v[128:131], a[116:119], a[60:63], v[128:131]\n"
  "s_and_b32 s69, s69, 1023\n"
  "v_mfma_f32_16x16x32_bf16 v[160:163], a[112:115], a[64:67], v[160:163]\n"
  "s_waitcnt vmcnt(13) lgkmcnt(0)\n"
  "s_barrier\n"
  "v_mfma_f32_16x16x32_bf16 v[160:163], a[116:119], a[68:71], v[160:163]\n"
  "ds_read_b64_tr_b16 a[80:81], v8 offset:0\n"
  "v_mfma_f32_16x16x32_bf16 v[164:167], a[120:123], a[64:67], v[164:167]\n"
  "ds_read_b64_tr_b16 a[82:83], v8 offset:1024\n"
  "v_mfma_f32_16x16x32_bf16 v[164:167], a[124:127], a[68:71], v[164:167]\n"
  "ds_read_b128 a[0:3], v0 offset:0\n"
  "v_mfma_f32_16x16x32_bf16 v[132:135], a[120:123], a[56:59], v[132:135]\n"
  "v_mfma_f32_16x16x32_bf16 v[132:135], a[124:127], a[60:63], v[132:135]\n"
  "ds_read_b64_tr_b16 a[84:85], v8 offset:8224\n"
  "v_mfma_f32_16x16x32_bf16 v[100:103], a[120:123], a[48:51], v[100:103]\n"
  "s_add_u32 m0, s67, 147456\n"
  "ds_read_b64_tr_b16 a[86:87], v8 offset:9248\n"
  "v_mfma_f32_16x16x32_bf16 v[100:103], a[124:127], a[52:55], v[100:103]\n"
  "buffer_load_dwordx4 v252, s[44:47], s71 offen nt lds\n"
  "v_mfma_f32_16x16x32_bf16 v[212:215], a[120:123], a[40:43], v[212:215]\n"
  "v_mfma_f32_16x16x32_bf16 v[212:215], a[124:127], a[44:47], v[212:215]\n"
  "ds_read_b128 a[4:7], v1 offset:0\n"
  "v_mfma_f32_16x16x32_bf16 v[216:219], a[128:131], a[40:43], v[216:219]\n"
  "ds_read_b128 a[8:11], v0 offset:2048\n"
  "v_mfma_f32_16x16x32_bf16 v[216:219], a[132:135], a[44:47], v[216:219]\n"
  "v_mfma_f32_16x16x32_bf16 v[104:107], a[128:131], a[48:51], v[104:107]\n"
  "ds_read_b128 a[12:15], v1 offset:2048\n"
  "v_mfma_f32_16x16x32_bf16 v[104:107], a[132:135], a[52:55], v[104:107]\n"
  "ds_read_b128 a[16:19], v0 offset:4096\n"
  "v_mfma_f32_16x16x32_bf16 v[136:139], a[128:131], a[56:59], v[136:139]\n"
  "ds_read_b128 a[20:23], v1 offset:4096\n"
  "v_mfma_f32_16x16x32_bf16 v[136:139], a[132:135], a[60:63], v[136:139]\n"
  "v_mfma_f32_16x16x32_bf16 v[168:171], a[128:131], a[64:67], v[168:171]\n"
  "ds_read_b128 a[24:27], v0 offset:6144\n"
  "v_mfma_f32_16x16x32_bf16 v[168:171], a[132:135], a[68:71], v[168:171]\n"
  "ds_read_b128 a[28:31], v1 offset:6144\n"
  "v_mfma_f32_16x16x32_bf16 v[172:175], a[136:139], a[64:67], v[172:175]\n"
  "v_mfma_f32_16x16x32_bf16 v[172:175], a[140:143], a[68:71], v[172:175]\n"
  "buffer_load_dwordx4 v253, s[44:47], s71 offen offset:1024 nt lds\n"
  "v_mfma_f32_16x16x32_bf16 v[140:143], a[136:139], a[56:59], v[140:143]\n"
  "ds_read_b128 a[32:35], v4 offset:0\n"
  "v_mfma_f32_16x16x32_bf16 v[140:143], a[140:143], a[60:63], v[140:143]\n"
  "ds_read_b128 a[36:39], v5 offset:0\n"
  "v_mfma_f32_16x16x32_bf16 v[108:111], a[136:139], a[48:51], v[108:111]\n"
  "v_mfma_f32_16x16x32_bf16 v[108:111], a[140:143], a[52:55], v[108:111]\n"
  "ds_read_b64_tr_b16 a[88:89], v8 offset:8\n"
  "v_mfma_f32_16x16x32_bf16 v[220:223], a[136:139], a[40:43], v[220:223]\n"
  "ds_read_b64_tr_b16 a[90:91], v8 offset:1032\n"
  "v_mfma_f32_16x16x32_bf16 v[220:223], a[140:143], a[44:47], v[220:223]\n"
  "s_waitcnt lgkmcnt(13)\n"
  "ds_read_b64_tr_b16 a[92:93], v8 offset:8232\n"
  "v_mfma_f32_16x16x32_bf16 v[192:195], a[80:83], a[0:3], v[192:195]\n"
  "s_waitcnt lgkmcnt(11)\n"
  "v_mfma_f32_16x16x32_bf16 v[192:195], a[84:87], a[4:7], v[192:195]\n"
  "s_waitcnt lgkmcnt(10)\n"
  "ds_read_b64_tr_b16 a[94:95], v8 offset:9256\n"
  "v_mfma_f32_16x16x32_bf16 v[80:83], a[80:83], a[8:11], v[80:83]\n"
  "s_waitcnt lgkmcnt(10)\n"
  "ds_read_b64_tr_b16 a[96:97], v9 offset:0\n"
  "v_mfma_f32_16x16x32_bf16 v[80:83], a[84:87], a[12:15], v[80:83]\n"
  "s_waitcnt lgkmcnt(10)\n"
  "v_mfma_f32_16x16x32_bf16 v[112:115], a[80:83], a[16:19], v[112:115]\n"
  "s_waitcnt lgkmcnt(9)\n"
  "ds_read_b64_tr_b16 a[98:99], v9 offset:1024\n"
  "v_mfma_f32_16x16x32_bf16 v[112:115], a[84:87], a[20:23], v[112:115]\n"
  "s_waitcnt lgkmcnt(9)\n"
  "buffer_load_dwordx4 v252, s[44:47], s71 offen offset:2048 nt lds\n"
  "v_mfma_f32_16x16x32_bf16 v[144:147], a[80:83], a[24:27], v[144:147]\n"
  "s_waitcnt lgkmcnt(8)\n"
  "ds_read_b64_tr_b16 a[100:101], v9 offset:8224\n"
  "v_mfma_f32_16x16x32_bf16 v[144:147], a[84:87], a[28:31], v[144:147]\n"
  "s_waitcnt lgkmcnt(8)\n"
  "v_mfma_f32_16x16x32_bf16 v[176:179], a[80:83], a[32:35], v[176:179]\n"
  "s_waitcnt lgkmcnt(7)\n"
  "ds_read_b64_tr_b16 a[102:103], v9 offset:9248\n"
  "v_mfma_f32_16x16x32_bf16 v[176:179], a[84:87], a[36:39], v[176:179]\n"
  "s_waitcnt lgkmcnt(6)\n"
  "ds_read_b64_tr_b16 a[104:105], v9 offset:8\n"
  "v_mfma_f32_16x16x32_bf16 v[180:183], a[88:91], a[32:35], v[180:183]\n"
  "s_waitcnt lgkmcnt(5)\n"
  "ds_read_b64_tr_b16 a[106:107], v9 offset:1032\n"
  "v_mfma_f32_16x16x32_bf16 v[180:183], a[92:95], a[36:39], v[180:183]\n"
  "v_mfma_f32_16x16x32_bf16 v[148:151], a[88:91], a[24:27], v[148:151]\n"
  "ds_read_b64_tr_b16 a[108:109], v9 offset:8232\n"
  "v_mfma_f32_16x16x32_bf16 v[148:151], a[92:95], a[28:31], v[148:151]\n"
  "ds_read_b64_tr_b16 a[110:111], v9 offset:9256\n"
  "v_mfma_f32_16x16x32_bf16 v[116:119], a[88:91], a[16:19], v[116:119]\n"
  "v_mfma_f32_16x16x32_bf16 v[116:119], a[92:95], a[20:23], v[116:119]\n"
  "ds_read_b64_tr_b16 a[112:113], v10 offset:0\n"
  "v_mfma_f32_16x16x32_bf16 v[84:87], a[88:91], a[8:11], v[84:87]\n"
  "ds_read_b64_tr_b16 a[114:115], v10 offset:1024\n"
  "v_mfma_f32_16x16x32_bf16 v[84:87], a[92:95], a[12:15], v[84:87]\n"
  "buffer_load_dwordx4 v253, s[44:47], s71 offen offset:3072 nt lds\n"
  "v_mfma_f32_16x16x32_bf16 v[196:199], a[88:91], a[0:3], v[196:199]\n"
  "s_add_u32 s71, s71, s73\n"
  "v_mfma_f32_16x16x32_bf16 v[196:199], a[92:95], a[4:7], v[196:199]\n"
  "s_waitcnt lgkmcnt(8)\n"
  "ds_read_b64_tr_b16 a[116:117], v10 offset:8224\n"
  "v_mfma_f32_16x16x32_bf16 v[200:203], a[96:99], a[0:3], v[200:203]\n"
  "s_waitcnt lgkmcnt(7)\n"
  "ds_read_b64_tr_b16 a[118:119], v10 offset:9248\n"
  "v_mfma_f32_16x16x32_bf16 v[200:203], a[100:103], a[4:7], v[200:203]\n"
  "s_and_b32 s71, s71, s8\n"
  "ds_read_b64_tr_b16 a[120:121], v10 offset:8\n"
  "v_mfma_f32_16x16x32_bf16 v[88:91], a[96:99], a[8:11], v[88:91]\n"
  "v_mfma_f32_16x16x32_bf16 v[88:91], a[100:103], a[12:15], v[88:91]\n"
  "ds_read_b64_tr_b16 a[122:123], v10 offset:1032\n"
  "v_mfma_f32_16x16x32_bf16 v[120:123], a[96:99], a[16:19], v[120:123]\n"
  "ds_read_b64_tr_b16 a[124:125], v10 offset:8232\n"
  "v_mfma_f32_16x16x32_bf16 v[120:123], a[100:103], a[20:23], v[120:123]\n"
  "v_mfma_f32_16x16x32_bf16 v[152:155], a[96:99], a[24:27], v[152:155]\n"
  "ds_read_b64_tr_b16 a[126:127], v10 offset:9256\n"
  "v_mfma_f32_16x16x32_bf16 v[152:155], a[100:103], a[28:31], v[152:155]\n"
  "s_add_u32 m0, s67, 131072\n"
  "ds_read_b64_tr_b16 a[128:129], v11 offset:0\n"
  "v_mfma_f32_16x16x32_bf16 v[184:187], a[96:99], a[32:35], v[184:187]\n"
  "buffer_load_dwordx4 v252, s[44:47], s71 offen nt lds\n"
  "v_mfma_f32_16x16x32_bf16 v[184:187], a[100:103], a[36:39], v[184:187]\n"
  "s_waitcnt lgkmcnt(11)\n"
  "v_mfma_f32_16x16x32_bf16 v[188:191], a[104:107], a[32:35], v[188:191]\n"
  "s_waitcnt lgkmcnt(9)\n"
  "ds_read_b64_tr_b16 a[130:131], v11 offset:1024\n"
  "v_mfma_f32_16x16x32_bf16 v[188:191], a[108:111], a[36:39], v[188:191]\n"
  "ds_read_b64_tr_b16 a[132:133], v11 offset:8224\n"
  "v_mfma_f32_16x16x32_bf16 v[156:159], a[104:107], a[24:27], v[156:159]\n"
  "ds_read_b64_tr_b16 a[134:135], v11 offset:9248\n"
  "v_mfma_f32_16x16x32_bf16 v[156:159], a[108:111], a[28:31], v[156:159]\n"
  "v_mfma_f32_16x16x32_bf16 v[124:127], a[104:107], a[16:19], v[124:127]\n"
  "ds_read_b64_tr_b16 a[136:137], v11 offset:8\n"
  "v_mfma_f32_16x16x32_bf16 v[124:127], a[108:111], a[20:23], v[124:127]\n"
  "ds_read_b64_tr_b16 a[138:139], v11 offset:1032\n"
  "v_mfma_f32_16x16x32_bf16 v[92:95], a[104:107], a[8:11], v[92:95]\n"
  "v_mfma_f32_16x16x32_bf16 v[92:95], a[108:111], a[12:15], v[92:95]\n"
  "ds_read_b64_tr_b16 a[140:141], v11 offset:8232\n"
  "v_mfma_f32_16x16x32_bf16 v[204:207], a[104:107], a[0:3], v[204:207]\n"
  "ds_read_b64_tr_b16 a[142:143], v11 offset:9256\n"
  "v_mfma_f32_16x16x32_bf16 v[204:207], a[108:111], a[4:7], v[204:207]\n"
  "s_waitcnt lgkmcnt(14)\n"
  "v_mfma_f32_16x16x32_bf16 v[208:211], a[112:115], a[0:3], v[208:211]\n"
  "s_waitcnt lgkmcnt(12)\n"
  "buffer_load_dwordx4 v253, s[44:47], s71 offen offset:1024 nt lds\n"
  "v_mfma_f32_16x16x32_bf16 v[208:211], a[116:119], a[4:7], v[208:211]\n"
  "v_mfma_f32_16x16x32_bf16 v[96:99], a[112:115], a[8:11], v[96:99]\n"
  "v_mfma_f32_16x16x32_bf16 v[96:99], a[116:119], a[12:15], v[96:99]\n"
  "v_mfma_f32_16x16x32_bf16 v[128:131], a[112:115], a[16:19], v[128:131]\n"
  "v_mfma_f32_16x16x32_bf16 v[128:131], a[116:119], a[20:23], v[128:131]\n"
  "v_mfma_f32_16x16x32_bf16 v[160:163], a[112:115], a[24:27], v[160:163]\n"
  "s_waitcnt vmcnt(6) lgkmcnt(0)\n"
  "s_barrier\n"
  "v_mfma_f32_16x16x32_bf16 v[160:163], a[116:119], a[28:31], v[160:163]\n"
  "ds_read_b64_tr_b16 a[80:81], v8 offset:53312\n"
  "v_mfma_f32_16x16x32_bf16 v[164:167], a[120:123], a[24:27], v[164:167]\n"
  "ds_read_b64_tr_b16 a[82:83], v8 offset:54336\n"
  "v_mfma_f32_16x16x32_bf16 v[164:167], a[124:127], a[28:31], v[164:167]\n"
  "ds_read_b128 a[40:43], v0 offset:53312\n"
  "v_mfma_f32_16x16x32_bf16 v[132:135], a[120:123], a[16:19], v[132:135]\n"
  "ds_read_b64_tr_b16 a[84:85], v8 offset:61536\n"
  "v_mfma_f32_16x16x32_bf16 v[132:135], a[124:127], a[20:23], v[132:135]\n"
  "ds_read_b64_tr_b16 a[86:87], v8 offset:62560\n"
  "v_mfma_f32_16x16x32_bf16 v[100:103], a[120:123], a[8:11], v[100:103]\n"
  "buffer_load_dwordx4 v252, s[44:47], s71 offen offset:2048 nt lds\n"
  "v_mfma_f32_16x16x32_bf16 v[100:103], a[124:127], a[12:15], v[100:103]\n"
  "ds_read_b128 a[44:47], v1 offset:53312\n"
  "v_mfma_f32_16x16x32_bf16 v[212:215], a[120:123], a[0:3], v[212:215]\n"
  "ds_read_b128 a[48:51], v0 offset:55360\n"
  "v_mfma_f32_16x16x32_bf16 v[212:215], a[124:127], a[4:7], v[212:215]\n"
  "ds_read_b128 a[52:55], v1 offset:55360\n"
  "v_mfma_f32_16x16x32_bf16 v[216:219], a[128:131], a[0:3], v[216:219]\n"
  "ds_read_b128 a[56:59], v0 offset:57408\n"
  "v_mfma_f32_16x16x32_bf16 v[216:219], a[132:135], a[4:7], v[216:219]\n"
  "ds_read_b128 a[60:63], v1 offset:57408\n"
  "v_mfma_f32_16x16x32_bf16 v[104:107], a[128:131], a[8:11], v[104:107]\n"
  "ds_read_b128 a[64:67], v0 offset:59456\n"
  "v_mfma_f32_16x16x32_bf16 v[104:107], a[132:135], a[12:15], v[104:107]\n"
  "ds_read_b128 a[68:71], v1 offset:59456\n"
  "v_mfma_f32_16x16x32_bf16 v[136:139], a[128:131], a[16:19], v[136:139]\n"
  "ds_read_b128 a[72:75], v4 offset:53312\n"
  "v_mfma_f32_16x16x32_bf16 v[136:139], a[132:135], a[20:23], v[136:139]\n"
  "ds_read_b128 a[76:79], v5 offset:53312\n"
  "v_mfma_f32_16x16x32_bf16 v[168:171], a[128:131], a[24:27], v[168:171]\n"
  "ds_read_b64_tr_b16 a[88:89], v8 offset:53320\n"
  "v_mfma_f32_16x16x32_bf16 v[168:171], a[132:135], a[28:31], v[168:171]\n"
  "ds_read_b64_tr_b16 a[90:91], v8 offset:54344\n"
  "v_mfma_f32_16x16x32_bf16 v[172:175], a[136:139], a[24:27], v[172:175]\n"
  "buffer_load_dwordx4 v253, s[44:47], s71 offen offset:3072 nt lds\n"
  "v_mfma_f32_16x16x32_bf16 v[172:175], a[140:143], a[28:31], v[172:175]\n"
  "s_add_u32 s71, s71, s73\n"
  "ds_read_b64_tr_b16 a[92:93], v8 offset:61544\n"
  "v_mfma_f32_16x16x32_bf16 v[140:143], a[136:139], a[16:19], v[140:143]\n"
  "s_and_b32 s71, s71, s8\n"
  "ds_read_b64_tr_b16 a[94:95], v8 offset:62568\n"
  "v_mfma_f32_16x16x32_bf16 v[140:143], a[140:143], a[20:23], v[140:143]\n"
  "ds_read_b64_tr_b16 a[96:97], v9 offset:53312\n"
  "v_mfma_f32_16x16x32_bf16 v[108:111], a[136:139], a[8:11], v[108:111]\n"
  "ds_read_b64_tr_b16 a[98:99], v9 offset:54336\n"
  "v_mfma_f32_16x16x32_bf16 v[108:111], a[140:143], a[12:15], v[108:111]\n"
  "ds_read_b64_tr_b16 a[100:101], v9 offset:61536\n"
  "v_mfma_f32_16x16x32_bf16 v[220:223], a[136:139], a[0:3], v[220:223]\n"
  "ds_read_b64_tr_b16 a[102:103], v9 offset:62560\n"
  "v_mfma_f32_16x16x32_bf16 v[220:223], a[140:143], a[4:7], v[220:223]\n"
  "ds_read_b64_tr_b16 a[104:105], v9 offset:53320\n"
  "s_waitcnt lgkmcnt(10)\n"
  "v_mfma_f32_16x16x32_bf16 v[176:179], a[80:83], a[72:75], v[176:179]\n"
  "s_waitcnt lgkmcnt(9)\n"
  "ds_read_b64_tr_b16 a[106:107], v9 offset:54344\n"
  "v_accvgpr_read_b32 v30, a248\n"
  "v_mfma_f32_16x16x32_bf16 v[176:179], a[84:87], a[76:79], v[176:179]\n"
  "s_waitcnt lgkmcnt(8)\n"
  "s_lshl_b32 s77, s64, 11\n"
  "s_add_u32 s77, s77, 32768\n"
  "v_mfma_f32_16x16x32_bf16 v[180:183], a[88:91], a[72:75], v[180:183]\n"
  "s_waitcnt lgkmcnt(6)\n"
  "ds_read_b64_tr_b16 a[108:109], v9 offset:61544\n"
  "v_add_u32 v60, s77, v30\n"
  "v_mfma_f32_16x16x32_bf16 v[180:183], a[92:95], a[76:79], v[180:183]\n"
  "s_waitcnt lgkmcnt(5)\n"
  "ds_read_b64_tr_b16 a[110:111], v9 offset:62568\n"
  "v_xor_b32 v61, 2080, v60\n"
  "v_mfma_f32_16x16x32_bf16 v[184:187], a[96:99], a[72:75], v[184:187]\n"
  "s_waitcnt lgkmcnt(4)\n"
  "ds_read_b64_tr_b16 a[112:113], v10 offset:53312\n"
  "v_lshrrev_b32 v62, 4, v30\n"
  "v_mfma_f32_16x16x32_bf16 v[184:187], a[100:103], a[76:79], v[184:187]\n"
  "s_waitcnt lgkmcnt(3)\n"
  "ds_read_b64_tr_b16 a[114:115], v10 offset:54336\n"
  "v_and_b32 v62, 48, v62\n"
  "v_mfma_f32_16x16x32_bf16 v[188:191], a[104:107], a[72:75], v[188:191]\n"
  "s_waitcnt lgkmcnt(2)\n"
  "s_lshl_b32 s77, s65, 7\n"
  "s_add_u32 s77, s77, 114688\n"
  "v_mfma_f32_16x16x32_bf16 v[188:191], a[108:111], a[76:79], v[188:191]\n"
  "ds_read_b64_tr_b16 a[116:117], v10 offset:61536\n"
  "v_add_u32 v62, s77, v62\n"
  "v_mfma_f32_16x16x32_bf16 v[192:195], a[80:83], a[40:43], v[192:195]\n"
  "ds_read_b64_tr_b16 a[118:119], v10 offset:62560\n"
  "v_xor_b32 v63, 0x80, v62\n"
  "v_mfma_f32_16x16x32_bf16 v[192:195], a[84:87], a[44:47], v[192:195]\n"
  "s_mov_b32 s86, 0x80008000\n"
  "ds_read_b64_tr_b16 a[120:121], v10 offset:53320\n"
  "v_mfma_f32_16x16x32_bf16 v[196:199], a[88:91], a[40:43], v[196:199]\n"
  "ds_read_b64_tr_b16 a[122:123], v10 offset:54344\n"
  "v_mfma_f32_16x16x32_bf16 v[196:199], a[92:95], a[44:47], v[196:199]\n"
  "ds_read_b64_tr_b16 a[124:125], v10 offset:61544\n"
  "v_mfma_f32_16x16x32_bf16 v[200:203], a[96:99], a[40:43], v[200:203]\n"
  "ds_read_b64_tr_b16 a[126:127], v10 offset:62568\n"
  "v_mfma_f32_16x16x32_bf16 v[200:203], a[100:103], a[44:47], v[200:203]\n"
  "ds_read_b64_tr_b16 a[128:129], v11 offset:53312\n"
  "v_mfma_f32_16x16x32_bf16 v[204:207], a[104:107], a[40:43], v[204:207]\n"
  "ds_read_b64_tr_b16 a[130:131], v11 offset:54336\n"
  "v_mfma_f32_16x16x32_bf16 v[204:207], a[108:111], a[44:47], v[204:207]\n"
  "s_waitcnt lgkmcnt(8)\n"
  "ds_read_b64_tr_b16 a[132:133], v11 offset:61536\n"
  "v_mfma_f32_16x16x32_bf16 v[208:211], a[112:115], a[40:43], v[208:211]\n"
  "s_waitcnt lgkmcnt(7)\n"
  "ds_read_b64_tr_b16 a[134:135], v11 offset:62560\n"
  "v_mfma_f32_16x16x32_bf16 v[208:211], a[116:119], a[44:47], v[208:211]\n"
  "s_waitcnt lgkmcnt(6)\n"
  "ds_read_b64_tr_b16 a[136:137], v11 offset:53320\n"
  "v_mfma_f32_16x16x32_bf16 v[212:215], a[120:123], a[40:43], v[212:215]\n"
  "s_waitcnt lgkmcnt(5)\n"
  "ds_read_b64_tr_b16 a[138:139], v11 offset:54344\n"
  "v_mfma_f32_16x16x32_bf16 v[212:215], a[124:127], a[44:47], v[212:215]\n"
  "s_waitcnt lgkmcnt(4)\n"
  "ds_read_b64_tr_b16 a[140:141], v11 offset:61544\n"
  "v_mfma_f32_16x16x32_bf16 v[216:219], a[128:131], a[40:43], v[216:219]\n"
  "s_waitcnt lgkmcnt(3)\n"
  "ds_read_b64_tr_b16 a[142:143], v11 offset:62568\n"
  "v_mul_f32 v176, s78, v176\n"
  "v_mfma_f32_16x16x32_bf16 v[216:219], a[132:135], a[44:47], v[216:219]\n"
  "s_waitcnt lgkmcnt(2)\n"
  "v_mul_f32 v177, s78, v177\n"
  "v_mul_f32 v178, s78, v178\n"
  "v_mfma_f32_16x16x32_bf16 v[220:223], a[136:139], a[40:43], v[220:223]\n"
  "s_waitcnt lgkmcnt(0)\n"
  "v_mul_f32 v179, s78, v179\n"
  "v_mul_f32 v180, s78, v180\n"
  "v_mfma_f32_16x16x32_bf16 v[220:223], a[140:143], a[44:47], v[220:223]\n"
  "v_mul_f32 v181, s78, v181\n"
  "v_mul_f32 v182, s78, v182\n"
  "v_mfma_f32_16x16x32_bf16 v[80:83], a[80:83], a[48:51], v[80:83]\n"
  "v_mul_f32 v183, s78, v183\n"
  "v_cvt_pk_bf16_f32 v64, v176, v177\n"
  "v_mfma_f32_16x16x32_bf16 v[80:83], a[84:87], a[52:55], v[80:83]\n"
  "v_cvt_pk_bf16_f32 v65, v178, v179\n"
  "v_cvt_pk_bf16_f32 v66, v180, v181\n"
  "v_mfma_f32_16x16x32_bf16 v[84:87], a[88:91], a[48:51], v[84:87]\n"
  "v_cvt_pk_bf16_f32 v67, v182, v183\n"
  "v_mul_f32 v184, s78, v184\n"
  "v_mfma_f32_16x16x32_bf16 v[84:87], a[92:95], a[52:55], v[84:87]\n"
  "v_mul_f32 v185, s78, v185\n"
  "v_mul_f32 v186, s78, v186\n"
  "v_mfma_f32_16x16x32_bf16 v[88:91], a[96:99], a[48:51], v[88:91]\n"
  "v_mul_f32 v187, s78, v187\n"
  "v_mul_f32 v188, s78, v188\n"
  "v_mfma_f32_16x16x32_bf16 v[88:91], a[100:103], a[52:55], v[88:91]\n"
  "v_mul_f32 v189, s78, v189\n"
  "v_mul_f32 v190, s78, v190\n"
  "v_mfma_f32_16x16x32_bf16 v[92:95], a[104:107], a[48:51], v[92:95]\n"
  "v_mul_f32 v191, s78, v191\n"
  "v_cvt_pk_bf16_f32 v68, v184, v185\n"
  "v_mfma_f32_16x16x32_bf16 v[92:95], a[108:111], a[52:55], v[92:95]\n"
  "v_cvt_pk_bf16_f32 v69, v186, v187\n"
  "v_cvt_pk_bf16_f32 v70, v188, v189\n"
  "v_mfma_f32_16x16x32_bf16 v[96:99], a[112:115], a[48:51], v[96:99]\n"
  "v_cvt_pk_bf16_f32 v71, v190, v191\n"
  "v_mfma_f32_16x16x32_bf16 v[96:99], a[116:119], a[52:55], v[96:99]\n"
  "ds_write_b128 v60, v[64:67] offset:0\n"
  "v_mfma_f32_16x16x32_bf16 v[100:103], a[120:123], a[48:51], v[100:103]\n"
  "ds_write_b128 v60, v[68:71] offset:1024\n"
  "v_mul_f32 v192, s78, v192\n"
  "v_mfma_f32_16x16x32_bf16 v[100:103], a[124:127], a[52:55], v[100:103]\n"
  "v_mul_f32 v193, s78, v193\n"
  "v_mul_f32 v194, s78, v194\n"
  "v_mfma_f32_16x16x32_bf16 v[104:107], a[128:131], a[48:51], v[104:107]\n"
  "v_mul_f32 v195, s78, v195\n"
  "v_mul_f32 v196, s78, v196\n"
  "v_mfma_f32_16x16x32_bf16 v[104:107], a[132:135], a[52:55], v[104:107]\n"
  "v_mul_f32 v197, s78, v197\n"
  "s_add_u32 m0, s16, 0\n"
  "v_mfma_f32_16x16x32_bf16 v[108:111], a[136:139], a[48:51], v[108:111]\n"
  "buffer_load_dwordx4 v254, s[48:51], s72 offen nt lds\n"
  "v_mfma_f32_16x16x32_bf16 v[108:111], a[140:143], a[52:55], v[108:111]\n"
  "v_mul_f32 v198, s78, v198\n"
  "v_mul_f32 v199, s78, v199\n"
  "v_mfma_f32_16x16x32_bf16 v[112:115], a[80:83], a[56:59], v[112:115]\n"
  "v_cvt_pk_bf16_f32 v0, v192, v193\n"
  "v_cvt_pk_bf16_f32 v1, v194, v195\n"
  "v_mfma_f32_16x16x32_bf16 v[112:115], a[84:87], a[60:63], v[112:115]\n"
  "v_cvt_pk_bf16_f32 v2, v196, v197\n"
  "v_cvt_pk_bf16_f32 v3, v198, v199\n"
  "v_mfma_f32_16x16x32_bf16 v[116:119], a[88:91], a[56:59], v[116:119]\n"
  "v_mul_f32 v200, s78, v200\n"
  "v_mul_f32 v201, s78, v201\n"
  "v_mfma_f32_16x16x32_bf16 v[116:119], a[92:95], a[60:63], v[116:119]\n"
  "v_mul_f32 v202, s78, v202\n"
  "v_mul_f32 v203, s78, v203\n"
  "v_mfma_f32_16x16x32_bf16 v[120:123], a[96:99], a[56:59], v[120:123]\n"
  "v_mul_f32 v204, s78, v204\n"
  "v_mul_f32 v205, s78, v205\n"
  "v_mfma_f32_16x16x32_bf16 v[120:123], a[100:103], a[60:63], v[120:123]\n"
  "buffer_load_dwordx4 v255, s[48:51], s72 offen offset:1024 nt lds\n"
  "v_mfma_f32_16x16x32_bf16 v[124:127], a[104:107], a[56:59], v[124:127]\n"
  "v_mul_f32 v206, s78, v206\n"
  "v_mul_f32 v207, s78, v207\n"
  "v_mfma_f32_16x16x32_bf16 v[124:127], a[108:111], a[60:63], v[124:127]\n"
  "v_cvt_pk_bf16_f32 v4, v200, v201\n"
  "v_cvt_pk_bf16_f32 v5, v202, v203\n"
  "v_mfma_f32_16x16x32_bf16 v[128:131], a[112:115], a[56:59], v[128:131]\n"
  "v_cvt_pk_bf16_f32 v6, v204, v205\n"
  "v_cvt_pk_bf16_f32 v7, v206, v207\n"
  "v_mfma_f32_16x16x32_bf16 v[128:131], a[116:119], a[60:63], v[128:131]\n"
  "v_mul_f32 v208, s78, v208\n"
  "v_mul_f32 v209, s78, v209\n"
  "v_mfma_f32_16x16x32_bf16 v[132:135], a[120:123], a[56:59], v[132:135]\n"
  "v_mul_f32 v210, s78, v210\n"
  "v_mul_f32 v211, s78, v211\n"
  "v_mfma_f32_16x16x32_bf16 v[132:135], a[124:127], a[60:63], v[132:135]\n"
  "v_mul_f32 v212, s78, v212\n"
  "v_mul_f32 v213, s78, v213\n"
  "v_mfma_f32_16x16x32_bf16 v[136:139], a[128:131], a[56:59], v[136:139]\n"
  "buffer_load_dwordx4 v254, s[48:51], s72 offen offset:2048 nt lds\n"
  "v_mfma_f32_16x16x32_bf16 v[136:139], a[132:135], a[60:63], v[136:139]\n"
  "v_mul_f32 v214, s78, v214\n"
  "v_mul_f32 v215, s78, v215\n"
  "v_mfma_f32_16x16x32_bf16 v[140:143], a[136:139], a[56:59], v[140:143]\n"
  "v_cvt_pk_bf16_f32 v8, v208, v209\n"
  "v_cvt_pk_bf16_f32 v9, v210, v211\n"
  "v_mfma_f32_16x16x32_bf16 v[140:143], a[140:143], a[60:63], v[140:143]\n"
  "v_cvt_pk_bf16_f32 v10, v212, v213\n"
  "v_cvt_pk_bf16_f32 v11, v214, v215\n"
  "v_mfma_f32_16x16x32_bf16 v[144:147], a[80:83], a[64:67], v[144:147]\n"
  "v_mul_f32 v216, s78, v216\n"
  "v_mul_f32 v217, s78, v217\n"
  "v_mfma_f32_16x16x32_bf16 v[144:147], a[84:87], a[68:71], v[144:147]\n"
  "v_mul_f32 v218, s78, v218\n"
  "v_mul_f32 v219, s78, v219\n"
  "v_mfma_f32_16x16x32_bf16 v[148:151], a[88:91], a[64:67], v[148:151]\n"
  "v_mul_f32 v220, s78, v220\n"
  "v_mul_f32 v221, s78, v221\n"
  "v_mfma_f32_16x16x32_bf16 v[148:151], a[92:95], a[68:71], v[148:151]\n"
  "buffer_load_dwordx4 v255, s[48:51], s72 offen offset:3072 nt lds\n"
  "s_add_u32 s72, s72, s74\n"
  "v_mfma_f32_16x16x32_bf16 v[152:155], a[96:99], a[64:67], v[152:155]\n"
  "s_and_b32 s72, s72, s9\n"
  "v_mul_f32 v222, s78, v222\n"
  "v_mul_f32 v223, s78, v223\n"
  "v_mfma_f32_16x16x32_bf16 v[152:155], a[100:103], a[68:71], v[152:155]\n"
  "v_cvt_pk_bf16_f32 v12, v216, v217\n"
  "v_cvt_pk_bf16_f32 v13, v218, v219\n"
  "v_mfma_f32_16x16x32_bf16 v[156:159], a[104:107], a[64:67], v[156:159]\n"
  "v_cvt_pk_bf16_f32 v14, v220, v221\n"
  "v_cvt_pk_bf16_f32 v15, v222, v223\n"
  "v_mfma_f32_16x16x32_bf16 v[156:159], a[108:111], a[68:71], v[156:159]\n"
  "s_waitcnt vmcnt(8) lgkmcnt(0)\n"
  "s_barrier\n"
  "v_mfma_f32_16x16x32_bf16 v[160:163], a[112:115], a[64:67], v[160:163]\n"
  "ds_read_b128 v[176:179], v62 offset:32768\n"
  "v_mul_f32 v80, s78, v80\n"
  "v_mfma_f32_16x16x32_bf16 v[160:163], a[116:119], a[68:71], v[160:163]\n"
  "ds_read_b128 v[180:183], v62 offset:32832\n"
  "v_mul_f32 v81, s78, v81\n"
  "v_mfma_f32_16x16x32_bf16 v[164:167], a[120:123], a[64:67], v[164:167]\n"
  "ds_read_b128 v[184:187], v63 offset:32768\n"
  "v_mul_f32 v82, s78, v82\n"
  "v_mfma_f32_16x16x32_bf16 v[164:167], a[124:127], a[68:71], v[164:167]\n"
  "ds_read_b128 v[188:191], v63 offset:32832\n"
  "v_mul_f32 v83, s78, v83\n"
  "v_mfma_f32_16x16x32_bf16 v[168:171], a[128:131], a[64:67], v[168:171]\n"
  "ds_read_b128 a[160:163], v228 offset:32768\n"
  "v_mul_f32 v84, s78, v84\n"
  "v_mfma_f32_16x16x32_bf16 v[168:171], a[132:135], a[68:71], v[168:171]\n"
  "ds_read_b128 a[164:167], v229 offset:32768\n"
  "v_mul_f32 v85, s78, v85\n"
  "v_mfma_f32_16x16x32_bf16 v[172:175], a[136:139], a[64:67], v[172:175]\n"
  "ds_read_b128 a[168:171], v230 offset:32768\n"
  "v_mul_f32 v86, s78, v86\n"
  "v_mfma_f32_16x16x32_bf16 v[172:175], a[140:143], a[68:71], v[172:175]\n"
  "ds_read_b128 a[172:175], v231 offset:32768\n"
  "v_mul_f32 v87, s78, v87\n"
  "ds_read_b128 v[72:75], v61 offset:0\n"
  "v_cvt_pk_bf16_f32 v16, v80, v81\n"
  "ds_read_b128 v[76:79], v61 offset:1024\n"
  "v_cvt_pk_bf16_f32 v17, v82, v83\n"
  "v_cvt_pk_bf16_f32 v18, v84, v85\n"
  "v_cvt_pk_bf16_f32 v19, v86, v87\n"
  "v_mul_f32 v88, s78, v88\n"
  "v_mul_f32 v89, s78, v89\n"
  "v_mul_f32 v90, s78, v90\n"
  "v_mul_f32 v91, s78, v91\n"
  "v_mul_f32 v92, s78, v92\n"
  "v_mul_f32 v93, s78, v93\n"
  "v_mul_f32 v94, s78, v94\n"
  "v_mul_f32 v95, s78, v95\n"
  "v_cvt_pk_bf16_f32 v20, v88, v89\n"
  "v_cvt_pk_bf16_f32 v21, v90, v91\n"
  "v_cvt_pk_bf16_f32 v22, v92, v93\n"
  "v_cvt_pk_bf16_f32 v23, v94, v95\n"
  "s_waitcnt lgkmcnt(9)\n"
  "v_xor_b32 v176, s86, v176\n"
  "v_xor_b32 v177, s86, v177\n"
  "v_xor_b32 v178, s86, v178\n"
  "v_xor_b32 v179, s86, v179\n"
  "s_waitcnt lgkmcnt(8)\n"
  "v_xor_b32 v180, s86, v180\n"
  "v_xor_b32 v181, s86, v181\n"
  "v_mfma_f32_16x16x32_bf16 v[200:203], v[176:179], v[0:3], -4.0\n"
  "v_xor_b32 v182, s86, v182\n"
  "v_xor_b32 v183, s86, v183\n"
  "s_waitcnt lgkmcnt(7)\n"
  "v_xor_b32 v184, s86, v184\n"
  "v_xor_b32 v185, s86, v185\n"
  "v_mfma_f32_16x16x32_bf16 v[200:203], v[180:183], v[4:7], v[200:203]\n"
  "v_xor_b32 v186, s86, v186\n"
  "v_xor_b32 v187, s86, v187\n"
  "s_waitcnt lgkmcnt(6)\n"
  "v_xor_b32 v188, s86, v188\n"
  "v_xor_b32 v189, s86, v189\n"
  "v_mfma_f32_16x16x32_bf16 v[200:203], v[184:187], v[8:11], v[200:203]\n"
  "v_xor_b32 v190, s86, v190\n"
  "v_xor_b32 v191, s86, v191\n"
  "v_mul_f32 v96, s78, v96\n"
  "v_mul_f32 v97, s78, v97\n"
  "v_mfma_f32_16x16x32_bf16 v[200:203], v[188:191], v[12:15], v[200:203]\n"
  "v_mul_f32 v98, s78, v98\n"
  "v_mul_f32 v99, s78, v99\n"
  "v_mfma_f32_16x16x32_bf16 v[216:219], v[176:179], v[64:67], -4.0\n"
  "v_mul_f32 v100, s78, v100\n"
  "v_mul_f32 v101, s78, v101\n"
  "v_mfma_f32_16x16x32_bf16 v[216:219], v[180:183], v[68:71], v[216:219]\n"
  "s_waitcnt lgkmcnt(1)\n"
  "v_mul_f32 v102, s78, v102\n"
  "v_mul_f32 v103, s78, v103\n"
  "v_mfma_f32_16x16x32_bf16 v[216:219], v[184:187], v[72:75], v[216:219]\n"
  "s_waitcnt lgkmcnt(0)\n"
  "v_cvt_pk_bf16_f32 v24, v96, v97\n"
  "v_cvt_pk_bf16_f32 v25, v98, v99\n"
  "v_mfma_f32_16x16x32_bf16 v[216:219], v[188:191], v[76:79], v[216:219]\n"
  "v_cvt_pk_bf16_f32 v26, v100, v101\n"
  "v_cvt_pk_bf16_f32 v27, v102, v103\n"
  "v_mfma_f32_16x16x32_bf16 v[204:207], v[176:179], v[16:19], -4.0\n"
  "v_mul_f32 v104, s78, v104\n"
  "v_mul_f32 v105, s78, v105\n"
  "v_mfma_f32_16x16x32_bf16 v[204:207], v[180:183], v[20:23], v[204:207]\n"
  "v_mul_f32 v106, s78, v106\n"
  "v_mul_f32 v107, s78, v107\n"
  "v_mfma_f32_16x16x32_bf16 v[204:207], v[184:187], v[24:27], v[204:207]\n"
  "v_mul_f32 v108, s78, v108\n"
  "v_mul_f32 v109, s78, v109\n"
  "v_mul_f32 v110, s78, v110\n"
  "v_mul_f32 v111, s78, v111\n"
  "v_cvt_pk_bf16_f32 v28, v104, v105\n"
  "v_cvt_pk_bf16_f32 v29, v106, v107\n"
  "v_cvt_pk_bf16_f32 v30, v108, v109\n"
  "v_cvt_pk_bf16_f32 v31, v110, v111\n"
  "s_add_u32 m0, s67, 114688\n"
  "s_nop 0\n"
  "buffer_load_dwordx4 v252, s[44:47], s71 offen nt lds\n"
  "v_pk_mul_f32 v[112:113], v[112:113], s[78:79]\n"
  "v_pk_mul_f32 v[114:115], v[114:115], s[78:79]\n"
  "v_pk_mul_f32 v[116:117], v[116:117], s[78:79]\n"
  "v_pk_mul_f32 v[118:119], v[118:119], s[78:79]\n"
  "v_cvt_pk_bf16_f32 v32, v112, v113\n"
  "v_cvt_pk_bf16_f32 v33, v114, v115\n"
  "v_cvt_pk_bf16_f32 v34, v116, v117\n"
  "v_cvt_pk_bf16_f32 v35, v118, v119\n"
  "v_pk_mul_f32 v[120:121], v[120:121], s[78:79]\n"
  "v_pk_mul_f32 v[122:123], v[122:123], s[78:79]\n"
  "v_pk_mul_f32 v[124:125], v[124:125], s[78:79]\n"
  "v_pk_mul_f32 v[126:127], v[126:127], s[78:79]\n"
  "v_cvt_pk_bf16_f32 v36, v120, v121\n"
  "v_cvt_pk_bf16_f32 v37, v122, v123\n"
  "v_cvt_pk_bf16_f32 v38, v124, v125\n"
  "v_cvt_pk_bf16_f32 v39, v126, v127\n"
  "buffer_load_dwordx4 v253, s[44:47], s71 offen offset:1024 nt lds\n"
  "v_pk_mul_f32 v[128:129], v[128:129], s[78:79]\n"
  "v_pk_mul_f32 v[130:131], v[130:131], s[78:79]\n"
  "v_pk_mul_f32 v[132:133], v[132:133], s[78:79]\n"
  "v_pk_mul_f32 v[134:135], v[134:135], s[78:79]\n"
  "v_cvt_pk_bf16_f32 v40, v128, v129\n"
  "v_cvt_pk_bf16_f32 v41, v130, v131\n"
  "v_cvt_pk_bf16_f32 v42, v132, v133\n"
  "v_cvt_pk_bf16_f32 v43, v134, v135\n"
  "v_pk_mul_f32 v[136:137], v[136:137], s[78:79]\n"
  "v_pk_mul_f32 v[138:139], v[138:139], s[78:79]\n"
  "v_pk_mul_f32 v[140:141], v[140:141], s[78:79]\n"
  "v_pk_mul_f32 v[142:143], v[142:143], s[78:79]\n"
  "v_cvt_pk_bf16_f32 v44, v136, v137\n"
  "v_cvt_pk_bf16_f32 v45, v138, v139\n"
  "v_cvt_pk_bf16_f32 v46, v140, v141\n"
  "v_cvt_pk_bf16_f32 v47, v142, v143\n"
  "buffer_load_dwordx4 v252, s[44:47], s71 offen offset:2048 nt lds\n"
  "v_pk_mul_f32 v[144:145], v[144:145], s[78:79]\n"
  "v_pk_mul_f32 v[146:147], v[146:147], s[78:79]\n"
  "v_pk_mul_f32 v[148:149], v[148:149], s[78:79]\n"
  "v_pk_mul_f32 v[150:151], v[150:151], s[78:79]\n"
  "v_cvt_pk_bf16_f32 v48, v144, v145\n"
  "v_cvt_pk_bf16_f32 v49, v146, v147\n"
  "v_cvt_pk_bf16_f32 v50, v148, v149\n"
  "v_cvt_pk_bf16_f32 v51, v150, v151\n"
  "v_pk_mul_f32 v[152:153], v[152:153], s[78:79]\n"
  "v_pk_mul_f32 v[154:155], v[154:155], s[78:79]\n"
  "v_pk_mul_f32 v[156:157], v[156:157], s[78:79]\n"
  "v_pk_mul_f32 v[158:159], v[158:159], s[78:79]\n"
  "v_cvt_pk_bf16_f32 v52, v152, v153\n"
  "v_cvt_pk_bf16_f32 v53, v154, v155\n"
  "v_cvt_pk_bf16_f32 v54, v156, v157\n"
  "v_cvt_pk_bf16_f32 v55, v158, v159\n"
  "v_pk_mul_f32 v[160:161], v[160:161], s[78:79]\n"
  "v_pk_mul_f32 v[162:163], v[162:163], s[78:79]\n"
  "v_pk_mul_f32 v[164:165], v[164:165], s[78:79]\n"
  "v_pk_mul_f32 v[166:167], v[166:167], s[78:79]\n"
  "v_pk_mul_f32 v[168:169], v[168:169], s[78:79]\n"
  "v_pk_mul_f32 v[170:171], v[170:171], s[78:79]\n"
  "v_pk_mul_f32 v[172:173], v[172:173], s[78:79]\n"
  "v_pk_mul_f32 v[174:175], v[174:175], s[78:79]\n"
  "buffer_load_dwordx4 v253, s[44:47], s71 offen offset:3072 nt lds\n"
  "s_add_u32 s71, s71, s73\n"
  "s_and_b32 s71, s71, s8\n"
  "v_cvt_pk_bf16_f32 v56, v160, v161\n"
  "v_cvt_pk_bf16_f32 v57, v162, v163\n"
  "v_mfma_f32_16x16x32_bf16 v[204:207], v[188:191], v[28:31], v[204:207]\n"
  "v_cvt_pk_bf16_f32 v58, v164, v165\n"
  "v_cvt_pk_bf16_f32 v59, v166, v167\n"
  "v_mfma_f32_16x16x32_bf16 v[208:211], v[176:179], v[32:35], -4.0\n"
  "v_cvt_pk_bf16_f32 v60, v168, v169\n"
  "v_cvt_pk_bf16_f32 v61, v170, v171\n"
  "v_mfma_f32_16x16x32_bf16 v[208:211], v[180:183], v[36:39], v[208:211]\n"
  "v_cvt_pk_bf16_f32 v62, v172, v173\n"
  "v_cvt_pk_bf16_f32 v63, v174, v175\n"
  "v_mfma_f32_16x16x32_bf16 v[208:211], v[184:187], v[40:43], v[208:211]\n"
  "v_mfma_f32_16x16x32_bf16 v[208:211], v[188:191], v[44:47], v[208:211]\n"
  "v_mfma_f32_16x16x32_bf16 v[212:215], v[176:179], v[48:51], -4.0\n"
  "v_mfma_f32_16x16x32_bf16 v[212:215], v[180:183], v[52:55], v[212:215]\n"
  "v_mfma_f32_16x16x32_bf16 v[212:215], v[184:187], v[56:59], v[212:215]\n"
  "v_mfma_f32_16x16x32_bf16 v[212:215], v[188:191], v[60:63], v[212:215]\n"
  "s_cmp_lg_u32 s76, 0\n"
  "s_cbranch_scc1 L_fbm_%=\n"
  "L_join_%=:\n"
  "v_mfma_f32_16x16x32_bf16 v[188:191], a[160:163], v[64:67], v[216:219]\n"
  "ds_read_b128 a[176:179], v228 offset:34816\n"
  "v_mov_b32 v220, 0\n"
  "v_mfma_f32_16x16x32_bf16 v[188:191], a[164:167], v[68:71], v[188:191]\n"
  "ds_read_b128 a[180:183], v229 offset:34816\n"
  "v_mov_b32 v221, 0\n"
  "v_mfma_f32_16x16x32_bf16 v[188:191], a[168:171], v[72:75], v[188:191]\n"
  "ds_read_b128 a[184:187], v230 offset:34816\n"
  "v_accvgpr_read_b32 v227, a251\n"
  "v_mfma_f32_16x16x32_bf16 v[188:191], a[172:175], v[76:79], v[188:191]\n"
  "ds_read_b128 a[188:191], v231 offset:34816\n"
  "v_mov_b32 v222, s80\n"
  "v_mfma_f32_16x16x32_bf16 v[192:195], a[172:175], v[12:15], v[200:203]\n"
  "v_mov_b32 v152, 0\n"
  "v_mov_b32 v153, 0\n"
  "v_mfma_f32_16x16x32_bf16 v[192:195], a[168:171], v[8:11], v[192:195]\n"
  "v_mov_b32 v154, 0\n"
  "v_mov_b32 v155, 0\n"
  "v_mfma_f32_16x16x32_bf16 v[192:195], a[164:167], v[4:7], v[192:195]\n"
  "s_add_u32 m0, s16, 16384\n"
  "v_exp_f32 v188, v188\n"
  "v_mfma_f32_16x16x32_bf16 v[192:195], a[160:163], v[0:3], v[192:195]\n"
  "v_exp_f32 v189, v189\n"
  "v_mfma_f32_16x16x32_bf16 v[196:199], a[160:163], v[16:19], v[204:207]\n"
  "v_exp_f32 v190, v190\n"
  "v_mfma_f32_16x16x32_bf16 v[196:199], a[164:167], v[20:23], v[196:199]\n"
  "v_exp_f32 v191, v191\n"
  "v_mfma_f32_16x16x32_bf16 v[196:199], a[168:171], v[24:27], v[196:199]\n"
  "v_cvt_pk_bf16_f32 v112, v188, v189\n"
  "v_cvt_pk_bf16_f32 v113, v190, v191\n"
  "v_mfma_f32_16x16x32_bf16 v[196:199], a[172:175], v[28:31], v[196:199]\n"
  "v_add_f32 v220, v188, v220\n"
  "v_add_f32 v221, v189, v221\n"
  "v_add_f32 v220, v190, v220\n"
  "v_add_f32 v221, v191, v221\n"
  "buffer_load_dwordx4 v254, s[48:51], s72 offen nt lds\n"
  "v_exp_f32 v192, v192\n"
  "v_mfma_f32_16x16x32_bf16 v[188:191], a[172:175], v[44:47], v[208:211]\n"
  "v_exp_f32 v193, v193\n"
  "v_mfma_f32_16x16x32_bf16 v[188:191], a[168:171], v[40:43], v[188:191]\n"
  "v_exp_f32 v194, v194\n"
  "v_mfma_f32_16x16x32_bf16 v[188:191], a[164:167], v[36:39], v[188:191]\n"
  "v_exp_f32 v195, v195\n"
  "v_mfma_f32_16x16x32_bf16 v[188:191], a[160:163], v[32:35], v[188:191]\n"
  "v_cvt_pk_bf16_f32 v80, v192, v193\n"
  "v_cvt_pk_bf16_f32 v81, v194, v195\n"
  "v_exp_f32 v196, v196\n"
  "v_exp_f32 v197, v197\n"
  "v_mfma_f32_16x16x32_bf16 v[192:195], a[160:163], v[48:51], v[212:215]\n"
  "v_exp_f32 v198, v198\n"
  "v_mfma_f32_16x16x32_bf16 v[192:195], a[164:167], v[52:55], v[192:195]\n"
  "v_exp_f32 v199, v199\n"
  "v_mfma_f32_16x16x32_bf16 v[192:195], a[168:171], v[56:59], v[192:195]\n"
  "v_cvt_pk_bf16_f32 v88, v196, v197\n"
  "v_cvt_pk_bf16_f32 v89, v198, v199\n"
  "v_mfma_f32_16x16x32_bf16 v[192:195], a[172:175], v[60:63], v[192:195]\n"
  "s_waitcnt lgkmcnt(0)\n"
  "buffer_load_dwordx4 v255, s[48:51], s72 offen offset:1024 nt lds\n"
  "v_mfma_f32_16x16x32_bf16 v[196:199], a[188:191], v[60:63], v[212:215]\n"
  "v_exp_f32 v188, v188\n"
  "v_mfma_f32_16x16x32_bf16 v[196:199], a[184:187], v[56:59], v[196:199]\n"
  "v_exp_f32 v189, v189\n"
  "v_mfma_f32_16x16x32_bf16 v[196:199], a[180:183], v[52:55], v[196:199]\n"
  "v_exp_f32 v190, v190\n"
  "v_mfma_f32_16x16x32_bf16 v[196:199], a[176:179], v[48:51], v[196:199]\n"
  "v_exp_f32 v191, v191\n"
  "v_cvt_pk_bf16_f32 v96, v188, v189\n"
  "v_cvt_pk_bf16_f32 v97, v190, v191\n"
  "v_exp_f32 v192, v192\n"
  "v_exp_f32 v193, v193\n"
  "v_mfma_f32_16x16x32_bf16 v[188:191], a[176:179], v[32:35], v[208:211]\n"
  "v_exp_f32 v194, v194\n"
  "v_mfma_f32_16x16x32_bf16 v[188:191], a[180:183], v[36:39], v[188:191]\n"
  "v_exp_f32 v195, v195\n"
  "v_mfma_f32_16x16x32_bf16 v[188:191], a[184:187], v[40:43], v[188:191]\n"
  "ds_read_b128 a[160:163], v232 offset:32768\n"
  "v_cvt_pk_bf16_f32 v104, v192, v193\n"
  "v_mfma_f32_16x16x32_bf16 v[188:191], a[188:191], v[44:47], v[188:191]\n"
  "ds_read_b128 a[164:167], v233 offset:32768\n"
  "v_cvt_pk_bf16_f32 v105, v194, v195\n"
  "ds_read_b128 a[168:171], v234 offset:32768\n"
  "ds_read_b128 a[172:175], v235 offset:32768\n"
  "v_mfma_f32_16x16x32_bf16 v[192:195], a[188:191], v[28:31], v[204:207]\n"
  "v_exp_f32 v196, v196\n"
  "v_mfma_f32_16x16x32_bf16 v[192:195], a[184:187], v[24:27], v[192:195]\n"
  "v_exp_f32 v197, v197\n"
  "v_mfma_f32_16x16x32_bf16 v[192:195], a[180:183], v[20:23], v[192:195]\n"
  "v_exp_f32 v198, v198\n"
  "v_mfma_f32_16x16x32_bf16 v[192:195], a[176:179], v[16:19], v[192:195]\n"
  "v_exp_f32 v199, v199\n"
  "v_cvt_pk_bf16_f32 v106, v196, v197\n"
  "v_cvt_pk_bf16_f32 v107, v198, v199\n"
  "buffer_load_dwordx4 v254, s[48:51], s72 offen offset:2048 nt lds\n"
  "v_exp_f32 v188, v188\n"
  "v_mfma_f32_16x16x32_bf16 v[196:199], a[176:179], v[0:3], v[200:203]\n"
  "v_exp_f32 v189, v189\n"
  "v_mfma_f32_16x16x32_bf16 v[196:199], a[180:183], v[4:7], v[196:199]\n"
  "v_exp_f32 v190, v190\n"
  "v_mfma_f32_16x16x32_bf16 v[196:199], a[184:187], v[8:11], v[196:199]\n"
  "v_exp_f32 v191, v191\n"
  "v_mfma_f32_16x16x32_bf16 v[196:199], a[188:191], v[12:15], v[196:199]\n"
  "v_cvt_pk_bf16_f32 v98, v188, v189\n"
  "v_cvt_pk_bf16_f32 v99, v190, v191\n"
  "v_exp_f32 v192, v192\n"
  "v_exp_f32 v193, v193\n"
  "v_mfma_f32_16x16x32_bf16 v[188:191], a[188:191], v[76:79], v[216:219]\n"
  "v_exp_f32 v194, v194\n"
  "v_mfma_f32_16x16x32_bf16 v[188:191], a[184:187], v[72:75], v[188:191]\n"
  "v_exp_f32 v195, v195\n"
  "v_mfma_f32_16x16x32_bf16 v[188:191], a[180:183], v[68:71], v[188:191]\n"
  "v_cvt_pk_bf16_f32 v90, v192, v193\n"
  "v_cvt_pk_bf16_f32 v91, v194, v195\n"
  "v_mfma_f32_16x16x32_bf16 v[188:191], a[176:179], v[64:67], v[188:191]\n"
  "s_waitcnt lgkmcnt(3)\n"
  "buffer_load_dwordx4 v255, s[48:51], s72 offen offset:3072 nt lds\n"
  "s_add_u32 s72, s72, s74\n"
  "v_mfma_f32_16x16x32_bf16 v[192:195], a[160:163], v[0:3], v[200:203]\n"
  "s_waitcnt lgkmcnt(2)\n"
  "s_and_b32 s72, s72, s9\n"
  "s_add_u32 m0, s83, s15\n"
  "s_add_u32 s83, s83, 0x3400\n"
  "v_mfma_f32_16x16x32_bf16 v[192:195], a[164:167], v[4:7], v[192:195]\n"
  "s_waitcnt lgkmcnt(1)\n"
  "s_and_b32 s83, s83, 0xcc00\n"
  "v_exp_f32 v196, v196\n"
  "v_mfma_f32_16x16x32_bf16 v[192:195], a[168:171], v[8:11], v[192:195]\n"
  "s_waitcnt lgkmcnt(0)\n"
  "v_exp_f32 v197, v197\n"
  "v_mfma_f32_16x16x32_bf16 v[192:195], a[172:175], v[12:15], v[192:195]\n"
  "v_exp_f32 v198, v198\n"
  "v_exp_f32 v199, v199\n"
  "v_cvt_pk_bf16_f32 v82, v196, v197\n"
  "v_cvt_pk_bf16_f32 v83, v198, v199\n"
  "buffer_load_dwordx4 v225, s[60:63], s82 offen lds\n"
  "s_add_u32 s82, s82, 0xf400\n"
  "v_mfma_f32_16x16x32_bf16 v[196:199], a[172:175], v[28:31], v[204:207]\n"
  "s_and_b32 s82, s82, 0x30c00\n"
  "v_xor_b32 v225, 32, v225\n"
  "v_mfma_f32_16x16x32_bf16 v[196:199], a[168:171], v[24:27], v[196:199]\n"
  "v_exp_f32 v188, v188\n"
  "v_mfma_f32_16x16x32_bf16 v[196:199], a[164:167], v[20:23], v[196:199]\n"
  "v_exp_f32 v189, v189\n"
  "v_mfma_f32_16x16x32_bf16 v[196:199], a[160:163], v[16:19], v[196:199]\n"
  "v_exp_f32 v190, v190\n"
  "v_exp_f32 v191, v191\n"
  "ds_read_b128 a[176:179], v232 offset:34816\n"
  "v_cvt_pk_bf16_f32 v114, v188, v189\n"
  "ds_read_b128 a[180:183], v233 offset:34816\n"
  "v_cvt_pk_bf16_f32 v115, v190, v191\n"
  "ds_read_b128 a[184:187], v234 offset:34816\n"
  "v_add_f32 v220, v188, v220\n"
  "ds_read_b128 a[188:191], v235 offset:34816\n"
  "v_add_f32 v221, v189, v221\n"
  "v_add_f32 v220, v190, v220\n"
  "v_add_f32 v221, v191, v221\n"
  "v_exp_f32 v192, v192\n"
  "v_exp_f32 v193, v193\n"
  "v_mfma_f32_16x16x32_bf16 v[188:191], a[160:163], v[32:35], v[208:211]\n"
  "v_exp_f32 v194, v194\n"
  "v_mfma_f32_16x16x32_bf16 v[188:191], a[164:167], v[36:39], v[188:191]\n"
  "v_exp_f32 v195, v195\n"
  "v_mfma_f32_16x16x32_bf16 v[188:191], a[168:171], v[40:43], v[188:191]\n"
  "v_cvt_pk_bf16_f32 v84, v192, v193\n"
  "v_cvt_pk_bf16_f32 v85, v194, v195\n"
  "v_mfma_f32_16x16x32_bf16 v[188:191], a[172:175], v[44:47], v[188:191]\n"
  "v_exp_f32 v196, v196\n"
  "v_mfma_f32_16x16x32_bf16 v[192:195], a[172:175], v[60:63], v[212:215]\n"
  "v_exp_f32 v197, v197\n"
  "v_mfma_f32_16x16x32_bf16 v[192:195], a[168:171], v[56:59], v[192:195]\n"
  "v_exp_f32 v198, v198\n"
  "v_mfma_f32_16x16x32_bf16 v[192:195], a[164:167], v[52:55], v[192:195]\n"
  "v_exp_f32 v199, v199\n"
  "v_mfma_f32_16x16x32_bf16 v[192:195], a[160:163], v[48:51], v[192:195]\n"
  "v_cvt_pk_bf16_f32 v92, v196, v197\n"
  "v_cvt_pk_bf16_f32 v93, v198, v199\n"
  "s_waitcnt vmcnt(9) lgkmcnt(0)\n"
  "s_barrier\n"
  "v_mfma_f32_16x16x32_bf16 v[196:199], a[176:179], v[0:3], v[200:203]\n"
  "s_add_u32 m0, s67, 147456\n"
  "v_exp_f32 v188, v188\n"
  "v_mfma_f32_16x16x32_bf16 v[196:199], a[180:183], v[4:7], v[196:199]\n"
  "v_exp_f32 v189, v189\n"
  "v_mfma_f32_16x16x32_bf16 v[196:199], a[184:187], v[8:11], v[196:199]\n"
  "v_exp_f32 v190, v190\n"
  "v_mfma_f32_16x16x32_bf16 v[196:199], a[188:191], v[12:15], v[196:199]\n"
  "v_exp_f32 v191, v191\n"
  "ds_read_b64_tr_b16 a[200:201], v236 offset:0\n"
  "v_cvt_pk_bf16_f32 v100, v188, v189\n"
  "ds_read_b64_tr_b16 a[202:203], v236 offset:2048\n"
  "v_cvt_pk_bf16_f32 v101, v190, v191\n"
  "buffer_load_dwordx4 v252, s[44:47], s71 offen nt lds\n"
  "v_exp_f32 v192, v192\n"
  "v_mfma_f32_16x16x32_bf16 v[188:191], a[188:191], v[28:31], v[204:207]\n"
  "v_exp_f32 v193, v193\n"
  "v_mfma_f32_16x16x32_bf16 v[188:191], a[184:187], v[24:27], v[188:191]\n"
  "v_exp_f32 v194, v194\n"
  "v_mfma_f32_16x16x32_bf16 v[188:191], a[180:183], v[20:23], v[188:191]\n"
  "v_exp_f32 v195, v195\n"
  "v_mfma_f32_16x16x32_bf16 v[188:191], a[176:179], v[16:19], v[188:191]\n"
  "ds_read_b128 a[160:163], v228 offset:16384\n"
  "v_cvt_pk_bf16_f32 v108, v192, v193\n"
  "ds_read_b128 a[164:167], v229 offset:16384\n"
  "v_cvt_pk_bf16_f32 v109, v194, v195\n"
  "v_exp_f32 v196, v196\n"
  "v_exp_f32 v197, v197\n"
  "v_mfma_f32_16x16x32_bf16 v[192:195], a[176:179], v[32:35], v[208:211]\n"
  "v_exp_f32 v198, v198\n"
  "v_mfma_f32_16x16x32_bf16 v[192:195], a[180:183], v[36:39], v[192:195]\n"
  "v_exp_f32 v199, v199\n"
  "v_mfma_f32_16x16x32_bf16 v[192:195], a[184:187], v[40:43], v[192:195]\n"
  "ds_read_b128 a[168:171], v230 offset:16384\n"
  "v_cvt_pk_bf16_f32 v86, v196, v197\n"
  "v_mfma_f32_16x16x32_bf16 v[192:195], a[188:191], v[44:47], v[192:195]\n"
  "ds_read_b128 a[172:175], v231 offset:16384\n"
  "v_cvt_pk_bf16_f32 v87, v198, v199\n"
  "v_exp_f32 v188, v188\n"
  "v_exp_f32 v189, v189\n"
  "v_mfma_f32_16x16x32_bf16 v[196:199], a[188:191], v[60:63], v[212:215]\n"
  "v_exp_f32 v190, v190\n"
  "v_mfma_f32_16x16x32_bf16 v[196:199], a[184:187], v[56:59], v[196:199]\n"
  "v_exp_f32 v191, v191\n"
  "v_mfma_f32_16x16x32_bf16 v[196:199], a[180:183], v[52:55], v[196:199]\n"
  "ds_read_b64_tr_b16 a[204:205], v240 offset:0\n"
  "v_cvt_pk_bf16_f32 v94, v188, v189\n"
  "v_mfma_f32_16x16x32_bf16 v[196:199], a[176:179], v[48:51], v[196:199]\n"
  "ds_read_b64_tr_b16 a[206:207], v240 offset:2048\n"
  "v_cvt_pk_bf16_f32 v95, v190, v191\n"
  "buffer_load_dwordx4 v253, s[44:47], s71 offen offset:1024 nt lds\n"
  "v_exp_f32 v192, v192\n"
  "v_exp_f32 v193, v193\n"
  "v_exp_f32 v194, v194\n"
  "v_exp_f32 v195, v195\n"
  "ds_read_b128 a[188:191], v231 offset:18432\n"
  "ds_read_b128 a[184:187], v230 offset:18432\n"
  "v_exp_f32 v196, v196\n"
  "v_exp_f32 v197, v197\n"
  "v_exp_f32 v198, v198\n"
  "v_exp_f32 v199, v199\n"
  "s_waitcnt lgkmcnt(7)\n"
  "v_mfma_f32_16x16x32_bf16 v[188:191], a[160:163], v[64:67], v[216:219]\n"
  "s_waitcnt lgkmcnt(6)\n"
  "ds_read_b128 a[180:183], v229 offset:18432\n"
  "v_cvt_pk_bf16_f32 v102, v192, v193\n"
  "v_mfma_f32_16x16x32_bf16 v[188:191], a[164:167], v[68:71], v[188:191]\n"
  "s_waitcnt lgkmcnt(6)\n"
  "ds_read_b128 a[176:179], v228 offset:18432\n"
  "v_cvt_pk_bf16_f32 v103, v194, v195\n"
  "v_mfma_f32_16x16x32_bf16 v[188:191], a[168:171], v[72:75], v[188:191]\n"
  "s_waitcnt lgkmcnt(6)\n"
  "ds_read_b64_tr_b16 a[216:217], v237 offset:0\n"
  "v_cvt_pk_bf16_f32 v110, v196, v197\n"
  "v_mfma_f32_16x16x32_bf16 v[188:191], a[172:175], v[76:79], v[188:191]\n"
  "ds_read_b64_tr_b16 a[218:219], v237 offset:2048\n"
  "v_cvt_pk_bf16_f32 v111, v198, v199\n"
  "v_mfma_f32_16x16x32_bf16 v[192:195], a[172:175], v[12:15], v[200:203]\n"
  "v_mfma_f32_16x16x32_bf16 v[192:195], a[168:171], v[8:11], v[192:195]\n"
  "v_mfma_f32_16x16x32_bf16 v[192:195], a[164:167], v[4:7], v[192:195]\n"
  "v_mfma_f32_16x16x32_bf16 v[192:195], a[160:163], v[0:3], v[192:195]\n"
  "buffer_load_dwordx4 v252, s[44:47], s71 offen offset:2048 nt lds\n"
  "v_mfma_f32_16x16x32_bf16 a[16:19], v[152:155], v[152:155], 0\n"
  "v_exp_f32 v188, v188\n"
  "v_exp_f32 v189, v189\n"
  "v_mfma_f32_16x16x32_bf16 a[0:3], v[80:83], a[200:203], 0\n"
  "s_waitcnt lgkmcnt(6)\n"
  "v_exp_f32 v190, v190\n"
  "v_mfma_f32_16x16x32_bf16 a[0:3], v[84:87], a[204:207], a[0:3]\n"
  "v_exp_f32 v191, v191\n"
  "v_mfma_f32_16x16x32_bf16 a[32:35], v[92:95], a[204:207], 0\n"
  "v_exp_f32 v192, v192\n"
  "v_mfma_f32_16x16x32_bf16 a[32:35], v[88:91], a[200:203], a[32:35]\n"
  "v_exp_f32 v193, v193\n"
  "v_mfma_f32_16x16x32_bf16 a[48:51], v[152:155], v[152:155], 0\n"
  "v_exp_f32 v194, v194\n"
  "v_exp_f32 v195, v195\n"
  "v_mfma_f32_16x16x32_bf16 v[196:199], a[160:163], v[16:19], v[204:207]\n"
  "v_cvt_pk_bf16_f32 v148, v188, v189\n"
  "v_cvt_pk_bf16_f32 v149, v190, v191\n"
  "v_mfma_f32_16x16x32_bf16 v[196:199], a[164:167], v[20:23], v[196:199]\n"
  "v_add_f32 v220, v188, v220\n"
  "v_add_f32 v221, v189, v221\n"
  "v_mfma_f32_16x16x32_bf16 v[196:199], a[168:171], v[24:27], v[196:199]\n"
  "v_add_f32 v220, v190, v220\n"
  "v_mfma_f32_16x16x32_bf16 v[196:199], a[172:175], v[28:31], v[196:199]\n"
  "ds_read_b64_tr_b16 a[220:221], v241 offset:0\n"
  "v_add_f32 v221, v191, v221\n"
  "v_mfma_f32_16x16x32_bf16 v[188:191], a[172:175], v[44:47], v[208:211]\n"
  "ds_read_b64_tr_b16 a[222:223], v241 offset:2048\n"
  "v_cvt_pk_bf16_f32 v116, v192, v193\n"
  "v_mfma_f32_16x16x32_bf16 v[188:191], a[168:171], v[40:43], v[188:191]\n"
  "v_cvt_pk_bf16_f32 v117, v194, v195\n"
  "v_mfma_f32_16x16x32_bf16 v[188:191], a[164:167], v[36:39], v[188:191]\n"
  "v_mfma_f32_16x16x32_bf16 v[188:191], a[160:163], v[32:35], v[188:191]\n"
  "buffer_load_dwordx4 v253, s[44:47], s71 offen offset:3072 nt lds\n"
  "v_mfma_f32_16x16x32_bf16 a[80:83], v[152:155], v[152:155], 0\n"
  "s_add_u32 s71, s71, s73\n"
  "v_exp_f32 v196, v196\n"
  "v_exp_f32 v197, v197\n"
  "v_mfma_f32_16x16x32_bf16 a[64:67], v[96:99], a[200:203], 0\n"
  "s_and_b32 s71, s71, s8\n"
  "v_exp_f32 v198, v198\n"
  "v_mfma_f32_16x16x32_bf16 a[64:67], v[100:103], a[204:207], a[64:67]\n"
  "v_exp_f32 v199, v199\n"
  "v_mfma_f32_16x16x32_bf16 a[96:99], v[108:111], a[204:207], 0\n"
  "v_exp_f32 v188, v188\n"
  "v_mfma_f32_16x16x32_bf16 a[96:99], v[104:107], a[200:203], a[96:99]\n"
  "v_exp_f32 v189, v189\n"
  "v_mfma_f32_16x16x32_bf16 a[112:115], v[152:155], v[152:155], 0\n"
  "v_exp_f32 v190, v190\n"
  "v_exp_f32 v191, v191\n"
  "v_smfmac_f32_16x16x64_bf16 a[224:227], a[240:243], v[80:87], v224\n"
  "v_cvt_pk_bf16_f32 v124, v196, v197\n"
  "v_mfma_f32_16x16x32_bf16 v[192:195], a[160:163], v[48:51], v[212:215]\n"
  "ds_read_b64_tr_b16 a[204:205], v242 offset:0\n"
  "v_cvt_pk_bf16_f32 v125, v198, v199\n"
  "v_mfma_f32_16x16x32_bf16 v[192:195], a[164:167], v[52:55], v[192:195]\n"
  "ds_read_b64_tr_b16 a[206:207], v242 offset:2048\n"
  "v_cvt_pk_bf16_f32 v132, v188, v189\n"
  "v_mfma_f32_16x16x32_bf16 v[192:195], a[168:171], v[56:59], v[192:195]\n"
  "s_add_u32 m0, s17, 0\n"
  "v_cvt_pk_bf16_f32 v133, v190, v191\n"
  "v_mfma_f32_16x16x32_bf16 v[192:195], a[172:175], v[60:63], v[192:195]\n"
  "s_waitcnt lgkmcnt(9)\n"
  "v_mfma_f32_16x16x32_bf16 v[196:199], a[188:191], v[60:63], v[212:215]\n"
  "s_waitcnt lgkmcnt(8)\n"
  "ds_read_b128 a[160:163], v232 offset:16384\n"
  "v_mfma_f32_16x16x32_bf16 v[196:199], a[184:187], v[56:59], v[196:199]\n"
  "s_waitcnt lgkmcnt(8)\n"
  "ds_read_b128 a[164:167], v233 offset:16384\n"
  "v_mfma_f32_16x16x32_bf16 v[196:199], a[180:183], v[52:55], v[196:199]\n"
  "s_waitcnt lgkmcnt(8)\n"
  "ds_read_b128 a[168:171], v234 offset:16384\n"
  "v_mfma_f32_16x16x32_bf16 v[196:199], a[176:179], v[48:51], v[196:199]\n"
  "buffer_load_dwordx4 v254, s[48:51], s72 offen nt lds\n"
  "v_mfma_f32_16x16x32_bf16 a[144:147], v[152:155], v[152:155], 0\n"
  "v_exp_f32 v192, v192\n"
  "v_mfma_f32_16x16x32_bf16 a[128:131], v[112:115], a[200:203], 0\n"
  "s_waitcnt lgkmcnt(7)\n"
  "v_exp_f32 v193, v193\n"
  "v_mfma_f32_16x16x32_bf16 a[132:135], v[112:115], a[216:219], 0\n"
  "v_exp_f32 v194, v194\n"
  "v_mfma_f32_16x16x32_bf16 a[148:151], v[152:155], v[152:155], 0\n"
  "v_exp_f32 v195, v195\n"
  "v_mfma_f32_16x16x32_bf16 a[20:23], v[152:155], v[152:155], 0\n"
  "v_exp_f32 v196, v196\n"
  "v_mfma_f32_16x16x32_bf16 a[4:7], v[80:83], a[216:219], 0\n"
  "s_waitcnt lgkmcnt(5)\n"
  "v_exp_f32 v197, v197\n"
  "v_exp_f32 v198, v198\n"
  "v_mfma_f32_16x16x32_bf16 a[4:7], v[84:87], a[220:223], a[4:7]\n"
  "v_exp_f32 v199, v199\n"
  "v_mfma_f32_16x16x32_bf16 v[188:191], a[176:179], v[32:35], v[208:211]\n"
  "ds_read_b64_tr_b16 a[200:201], v238 offset:0\n"
  "v_cvt_pk_bf16_f32 v140, v192, v193\n"
  "v_mfma_f32_16x16x32_bf16 v[188:191], a[180:183], v[36:39], v[188:191]\n"
  "ds_read_b64_tr_b16 a[202:203], v238 offset:2048\n"
  "v_cvt_pk_bf16_f32 v141, v194, v195\n"
  "v_mfma_f32_16x16x32_bf16 v[188:191], a[184:187], v[40:43], v[188:191]\n"
  "v_cvt_pk_bf16_f32 v142, v196, v197\n"
  "v_mfma_f32_16x16x32_bf16 v[188:191], a[188:191], v[44:47], v[188:191]\n"
  "v_cvt_pk_bf16_f32 v143, v198, v199\n"
  "v_mfma_f32_16x16x32_bf16 v[192:195], a[188:191], v[28:31], v[204:207]\n"
  "ds_read_b128 a[172:175], v235 offset:16384\n"
  "v_mfma_f32_16x16x32_bf16 v[192:195], a[184:187], v[24:27], v[192:195]\n"
  "v_mfma_f32_16x16x32_bf16 v[192:195], a[180:183], v[20:23], v[192:195]\n"
  "v_mfma_f32_16x16x32_bf16 v[192:195], a[176:179], v[16:19], v[192:195]\n"
  "buffer_load_dwordx4 v255, s[48:51], s72 offen offset:1024 nt lds\n"
  "v_mfma_f32_16x16x32_bf16 a[52:55], v[152:155], v[152:155], 0\n"
  "v_exp_f32 v188, v188\n"
  "v_exp_f32 v189, v189\n"
  "v_mfma_f32_16x16x32_bf16 a[36:39], v[88:91], a[216:219], 0\n"
  "v_exp_f32 v190, v190\n"
  "v_mfma_f32_16x16x32_bf16 a[36:39], v[92:95], a[220:223], a[36:39]\n"
  "v_exp_f32 v191, v191\n"
  "v_mfma_f32_16x16x32_bf16 a[68:71], v[100:103], a[220:223], 0\n"
  "v_exp_f32 v192, v192\n"
  "v_mfma_f32_16x16x32_bf16 a[68:71], v[96:99], a[216:219], a[68:71]\n"
  "v_exp_f32 v193, v193\n"
  "v_mfma_f32_16x16x32_bf16 a[84:87], v[152:155], v[152:155], 0\n"
  "v_exp_f32 v194, v194\n"
  "v_exp_f32 v195, v195\n"
  "v_smfmac_f32_16x16x64_bf16 a[228:231], a[240:243], v[88:95], v224\n"
  "v_cvt_pk_bf16_f32 v134, v188, v189\n"
  "v_mfma_f32_16x16x32_bf16 v[196:199], a[176:179], v[0:3], v[200:203]\n"
  "v_cvt_pk_bf16_f32 v135, v190, v191\n"
  "v_cvt_pk_bf16_f32 v126, v192, v193\n"
  "v_mfma_f32_16x16x32_bf16 v[196:199], a[180:183], v[4:7], v[196:199]\n"
  "v_cvt_pk_bf16_f32 v127, v194, v195\n"
  "v_mfma_f32_16x16x32_bf16 v[196:199], a[184:187], v[8:11], v[196:199]\n"
  "v_mfma_f32_16x16x32_bf16 v[196:199], a[188:191], v[12:15], v[196:199]\n"
  "v_mfma_f32_16x16x32_bf16 v[188:191], a[188:191], v[76:79], v[216:219]\n"
  "ds_read_b128 a[188:191], v235 offset:18432\n"
  "v_mfma_f32_16x16x32_bf16 v[188:191], a[184:187], v[72:75], v[188:191]\n"
  "ds_read_b128 a[184:187], v234 offset:18432\n"
  "v_mfma_f32_16x16x32_bf16 v[188:191], a[180:183], v[68:71], v[188:191]\n"
  "buffer_store_dword v222, v227, s[56:59], s84 offen sc1\n"
  "v_mfma_f32_16x16x32_bf16 v[188:191], a[176:179], v[64:67], v[188:191]\n"
  "buffer_load_dwordx4 v254, s[48:51], s72 offen offset:2048 nt lds\n"
  "v_mfma_f32_16x16x32_bf16 a[116:119], v[152:155], v[152:155], 0\n"
  "v_exp_f32 v196, v196\n"
  "v_exp_f32 v197, v197\n"
  "v_mfma_f32_16x16x32_bf16 a[100:103], v[104:107], a[216:219], 0\n"
  "v_exp_f32 v198, v198\n"
  "v_mfma_f32_16x16x32_bf16 a[100:103], v[108:111], a[220:223], a[100:103]\n"
  "s_waitcnt lgkmcnt(8)\n"
  "v_exp_f32 v199, v199\n"
  "v_mfma_f32_16x16x32_bf16 a[104:107], v[108:111], a[204:207], 0\n"
  "s_waitcnt lgkmcnt(3)\n"
  "v_exp_f32 v188, v188\n"
  "v_mfma_f32_16x16x32_bf16 a[104:107], v[104:107], a[200:203], a[104:107]\n"
  "v_exp_f32 v189, v189\n"
  "v_mfma_f32_16x16x32_bf16 a[120:123], v[152:155], v[152:155], 0\n"
  "v_exp_f32 v190, v190\n"
  "v_exp_f32 v191, v191\n"
  "v_mfma_f32_16x16x32_bf16 v[192:195], a[160:163], v[0:3], v[200:203]\n"
  "v_cvt_pk_bf16_f32 v118, v196, v197\n"
  "v_cvt_pk_bf16_f32 v119, v198, v199\n"
  "v_mfma_f32_16x16x32_bf16 v[192:195], a[164:167], v[4:7], v[192:195]\n"
  "v_cvt_pk_bf16_f32 v150, v188, v189\n"
  "v_cvt_pk_bf16_f32 v151, v190, v191\n"
  "v_mfma_f32_16x16x32_bf16 v[192:195], a[168:171], v[8:11], v[192:195]\n"
  "s_waitcnt lgkmcnt(2)\n"
  "ds_read_b64_tr_b16 a[216:217], v239 offset:0\n"
  "v_add_f32 v220, v188, v220\n"
  "v_mfma_f32_16x16x32_bf16 v[192:195], a[172:175], v[12:15], v[192:195]\n"
  "ds_read_b64_tr_b16 a[218:219], v239 offset:2048\n"
  "v_add_f32 v221, v189, v221\n"
  "v_mfma_f32_16x16x32_bf16 v[196:199], a[172:175], v[28:31], v[204:207]\n"
  "v_add_f32 v220, v190, v220\n"
  "v_mfma_f32_16x16x32_bf16 v[196:199], a[168:171], v[24:27], v[196:199]\n"
  "v_add_f32 v221, v191, v221\n"
  "v_mfma_f32_16x16x32_bf16 v[196:199], a[164:167], v[20:23], v[196:199]\n"
  "ds_read_b128 a[176:179], v232 offset:18432\n"
  "v_mfma_f32_16x16x32_bf16 v[196:199], a[160:163], v[16:19], v[196:199]\n"
  "buffer_load_dwordx4 v255, s[48:51], s72 offen offset:3072 nt lds\n"
  "v_mfma_f32_16x16x32_bf16 a[88:91], v[152:155], v[152:155], 0\n"
  "s_add_u32 s72, s72, s74\n"
  "v_exp_f32 v192, v192\n"
  "v_exp_f32 v193, v193\n"
  "v_mfma_f32_16x16x32_bf16 a[72:75], v[96:99], a[200:203], 0\n"
  "s_and_b32 s72, s72, s9\n"
  "v_exp_f32 v194, v194\n"
  "v_mfma_f32_16x16x32_bf16 a[72:75], v[100:103], a[204:207], a[72:75]\n"
  "v_exp_f32 v195, v195\n"
  "v_mfma_f32_16x16x32_bf16 a[40:43], v[92:95], a[204:207], 0\n"
  "v_exp_f32 v196, v196\n"
  "v_mfma_f32_16x16x32_bf16 a[40:43], v[88:91], a[200:203], a[40:43]\n"
  "v_exp_f32 v197, v197\n"
  "v_mfma_f32_16x16x32_bf16 a[56:59], v[152:155], v[152:155], 0\n"
  "v_exp_f32 v198, v198\n"
  "v_exp_f32 v199, v199\n"
  "v_smfmac_f32_16x16x64_bf16 a[232:235], a[240:243], v[96:103], v224\n"
  "v_cvt_pk_bf16_f32 v120, v192, v193\n"
  "v_mfma_f32_16x16x32_bf16 v[188:191], a[160:163], v[32:35], v[208:211]\n"
  "ds_read_b128 a[180:183], v233 offset:18432\n"
  "v_cvt_pk_bf16_f32 v121, v194, v195\n"
  "v_mfma_f32_16x16x32_bf16 v[188:191], a[164:167], v[36:39], v[188:191]\n"
  "v_cvt_pk_bf16_f32 v128, v196, v197\n"
  "v_mfma_f32_16x16x32_bf16 v[188:191], a[168:171], v[40:43], v[188:191]\n"
  "v_cvt_pk_bf16_f32 v129, v198, v199\n"
  "v_mfma_f32_16x16x32_bf16 v[188:191], a[172:175], v[44:47], v[188:191]\n"
  "ds_read_b64_tr_b16 a[220:221], v243 offset:0\n"
  "v_mfma_f32_16x16x32_bf16 v[192:195], a[172:175], v[60:63], v[212:215]\n"
  "ds_read_b64_tr_b16 a[222:223], v243 offset:2048\n"
  "v_mfma_f32_16x16x32_bf16 v[192:195], a[168:171], v[56:59], v[192:195]\n"
  "s_add_u32 m0, s83, s15\n"
  "s_add_u32 s83, s83, 0x3400\n"
  "s_and_b32 s83, s83, 0xcc00\n"
  "v_mfma_f32_16x16x32_bf16 v[192:195], a[164:167], v[52:55], v[192:195]\n"
  "buffer_load_dwordx4 v225, s[60:63], s82 offen lds\n"
  "v_mfma_f32_16x16x32_bf16 v[192:195], a[160:163], v[48:51], v[192:195]\n"
  "s_add_u32 s82, s82, 0xf400\n"
  "v_xor_b32 v225, 32, v225\n"
  "v_exp_f32 v188, v188\n"
  "v_mfma_f32_16x16x32_bf16 a[8:11], v[84:87], a[204:207], 0\n"
  "s_and_b32 s82, s82, 0x30c00\n"
  "v_exp_f32 v189, v189\n"
  "v_mfma_f32_16x16x32_bf16 a[24:27], v[152:155], v[152:155], 0\n"
  "v_exp_f32 v190, v190\n"
  "v_mfma_f32_16x16x32_bf16 a[8:11], v[80:83], a[200:203], a[8:11]\n"
  "v_exp_f32 v191, v191\n"
  "v_mfma_f32_16x16x32_bf16 a[152:155], v[152:155], v[152:155], 0\n"
  "v_exp_f32 v192, v192\n"
  "v_mfma_f32_16x16x32_bf16 a[136:139], v[112:115], a[200:203], 0\n"
  "s_waitcnt lgkmcnt(4)\n"
  "v_exp_f32 v193, v193\n"
  "v_mfma_f32_16x16x32_bf16 a[140:143], v[112:115], a[216:219], 0\n"
  "v_exp_f32 v194, v194\n"
  "v_mfma_f32_16x16x32_bf16 a[156:159], v[152:155], v[152:155], 0\n"
  "v_exp_f32 v195, v195\n"
  "v_smfmac_f32_16x16x64_bf16 a[236:239], a[240:243], v[104:111], v224\n"
  "s_waitcnt vmcnt(9) lgkmcnt(0)\n"
  "s_barrier\n"
  "v_mfma_f32_16x16x32_bf16 v[196:199], a[176:179], v[0:3], v[200:203]\n"
  "ds_read_b128 a[160:163], v228 offset:0\n"
  "v_cvt_pk_bf16_f32 v136, v188, v189\n"
  "v_mfma_f32_16x16x32_bf16 v[196:199], a[180:183], v[4:7], v[196:199]\n"
  "ds_read_b128 a[164:167], v229 offset:0\n"
  "v_cvt_pk_bf16_f32 v137, v190, v191\n"
  "v_mfma_f32_16x16x32_bf16 v[196:199], a[184:187], v[8:11], v[196:199]\n"
  "ds_read_b128 a[168:171], v230 offset:0\n"
  "v_cvt_pk_bf16_f32 v144, v192, v193\n"
  "v_mfma_f32_16x16x32_bf16 v[196:199], a[188:191], v[12:15], v[196:199]\n"
  "ds_read_b128 a[172:175], v231 offset:0\n"
  "v_cvt_pk_bf16_f32 v145, v194, v195\n"
  "v_mfma_f32_16x16x32_bf16 v[188:191], a[188:191], v[28:31], v[204:207]\n"
  "ds_read_b64_tr_b16 a[192:193], v236 offset:8192\n"
  "v_mfma_f32_16x16x32_bf16 v[188:191], a[184:187], v[24:27], v[188:191]\n"
  "ds_read_b64_tr_b16 a[194:195], v236 offset:10240\n"
  "v_mfma_f32_16x16x32_bf16 v[188:191], a[180:183], v[20:23], v[188:191]\n"
  "s_add_u32 m0, s67, 131072\n"
  "ds_read_b64_tr_b16 a[196:197], v240 offset:8192\n"
  "v_mfma_f32_16x16x32_bf16 v[188:191], a[176:179], v[16:19], v[188:191]\n"
  "buffer_load_dwordx4 v252, s[44:47], s71 offen nt lds\n"
  "v_mfma_f32_16x16x32_bf16 a[28:31], v[152:155], v[152:155], 0\n"
  "v_exp_f32 v196, v196\n"
  "v_exp_f32 v197, v197\n"
  "v_mfma_f32_16x16x32_bf16 a[12:15], v[80:83], a[216:219], 0\n"
  "v_exp_f32 v198, v198\n"
  "v_mfma_f32_16x16x32_bf16 a[12:15], v[84:87], a[220:223], a[12:15]\n"
  "v_exp_f32 v199, v199\n"
  "v_mfma_f32_16x16x32_bf16 a[44:47], v[92:95], a[220:223], 0\n"
  "v_exp_f32 v188, v188\n"
  "v_mfma_f32_16x16x32_bf16 a[44:47], v[88:91], a[216:219], a[44:47]\n"
  "v_exp_f32 v189, v189\n"
  "v_mfma_f32_16x16x32_bf16 a[60:63], v[152:155], v[152:155], 0\n"
  "v_exp_f32 v190, v190\n"
  "v_exp_f32 v191, v191\n"
  "v_mfma_f32_16x16x32_bf16 v[192:195], a[176:179], v[32:35], v[208:211]\n"
  "ds_read_b64_tr_b16 a[198:199], v240 offset:10240\n"
  "v_cvt_pk_bf16_f32 v122, v196, v197\n"
  "v_mfma_f32_16x16x32_bf16 v[192:195], a[180:183], v[36:39], v[192:195]\n"
  "ds_read_b64_tr_b16 a[200:201], v236 offset:24576\n"
  "v_cvt_pk_bf16_f32 v123, v198, v199\n"
  "v_mfma_f32_16x16x32_bf16 v[192:195], a[184:187], v[40:43], v[192:195]\n"
  "ds_read_b64_tr_b16 a[202:203], v236 offset:26624\n"
  "v_cvt_pk_bf16_f32 v130, v188, v189\n"
  "v_mfma_f32_16x16x32_bf16 v[192:195], a[188:191], v[44:47], v[192:195]\n"
  "ds_read_b64_tr_b16 a[204:205], v240 offset:24576\n"
  "v_cvt_pk_bf16_f32 v131, v190, v191\n"
  "v_mfma_f32_16x16x32_bf16 v[196:199], a[188:191], v[60:63], v[212:215]\n"
  "ds_read_b64_tr_b16 a[206:207], v240 offset:26624\n"
  "v_mfma_f32_16x16x32_bf16 v[196:199], a[184:187], v[56:59], v[196:199]\n"
  "ds_read_b128 a[188:191], v231 offset:2048\n"
  "v_mfma_f32_16x16x32_bf16 v[196:199], a[180:183], v[52:55], v[196:199]\n"
  "ds_read_b128 a[184:187], v230 offset:2048\n"
  "v_mfma_f32_16x16x32_bf16 v[196:199], a[176:179], v[48:51], v[196:199]\n"
  "buffer_load_dwordx4 v253, s[44:47], s71 offen offset:1024 nt lds\n"
  "v_mfma_f32_16x16x32_bf16 a[92:95], v[152:155], v[152:155], 0\n"
  "v_exp_f32 v192, v192\n"
  "v_exp_f32 v193, v193\n"
  "v_mfma_f32_16x16x32_bf16 a[76:79], v[96:99], a[216:219], 0\n"
  "v_exp_f32 v194, v194\n"
  "v_mfma_f32_16x16x32_bf16 a[76:79], v[100:103], a[220:223], a[76:79]\n"
  "v_exp_f32 v195, v195\n"
  "v_mfma_f32_16x16x32_bf16 a[108:111], v[108:111], a[220:223], 0\n"
  "v_exp_f32 v196, v196\n"
  "v_mfma_f32_16x16x32_bf16 a[108:111], v[104:107], a[216:219], a[108:111]\n"
  "v_exp_f32 v197, v197\n"
  "v_mfma_f32_16x16x32_bf16 a[124:127], v[152:155], v[152:155], 0\n"
  "v_exp_f32 v198, v198\n"
  "v_exp_f32 v199, v199\n"
  "s_branch L_loop_%=\n"
  "L_side1_%=:\n"
  "v_accvgpr_read_b32 v227, a249\n"
  "v_accvgpr_read_b32 v252, a248\n"
  "s_getpc_b64 s[36:37]\n"
  "L_pc2_%=:\n"
  "s_add_u32 s36, s36, L_tail_%=-L_pc2_%=\n"
  "s_addc_u32 s37, s37, 0\n"
  "s_mov_b32 s38, L_fb_%=-L_tail_%=\n"
  "s_lshr_b32 s77, %1, 3\n"
  "s_lshl2_add_u32 s77, s77, s64\n"
  "s_lshl_b32 s77, s77, 7\n"
  "v_lshlrev_b32 v252, 10, v252\n"
  "buffer_load_dword v222, v227, s[56:59], 0 offen sc1\n"
  "buffer_load_dword v223, v252, s[36:39], s77 offen\n"
  "s_mov_b64 s[44:45], s[60:61]\n"
  "s_mov_b32 s46, s62\n"
  "s_mov_b32 s73, 65536\n"
  "s_mov_b32 s8, -1\n"
  "v_accvgpr_read_b32 v252, a252\n"
  "v_xor_b32 v253, 32, v252\n"
  "s_mov_b64 s[36:37], s[24:25]\n"
  "s_mov_b32 s38, 2260992\n"
  "s_mov_b32 s39, 0x20000\n"
  "s_mov_b64 s[40:41], s[26:27]\n"
  "s_mov_b32 s42, 147456\n"
  "s_mov_b32 s43, 0x20000\n"
  "s_mov_b32 s71, 196608\n"
  "s_branch L_back1_%=\n"
  "L_side2_%=:\n"
  "s_mov_b64 s[48:49], s[60:61]\n"
  "s_mov_b32 s50, s62\n"
  "v_mov_b32 v254, v252\n"
  "v_mov_b32 v255, v253\n"
  "s_mov_b32 s16, s67\n"
  "s_mov_b32 s72, 393216\n"
  "s_branch L_back2_%=\n"
  "L_loop_%=:\n"
  "s_waitcnt lgkmcnt(13)\n"
  "v_mfma_f32_16x16x32_bf16 v[188:191], a[160:163], v[64:67], v[216:219]\n"
  "s_waitcnt lgkmcnt(12)\n"
  "ds_read_b128 a[180:183], v229 offset:2048\n"
  "v_cvt_pk_bf16_f32 v138, v192, v193\n"
  "v_mfma_f32_16x16x32_bf16 v[188:191], a[164:167], v[68:71], v[188:191]\n"
  "s_waitcnt lgkmcnt(12)\n"
  "ds_read_b128 a[176:179], v228 offset:2048\n"
  "v_cvt_pk_bf16_f32 v139, v194, v195\n"
  "v_mfma_f32_16x16x32_bf16 v[188:191], a[168:171], v[72:75], v[188:191]\n"
  "s_waitcnt lgkmcnt(12)\n"
  "ds_read_b64_tr_b16 a[216:217], v237 offset:24576\n"
  "v_cvt_pk_bf16_f32 v146, v196, v197\n"
  "v_mfma_f32_16x16x32_bf16 v[188:191], a[172:175], v[76:79], v[188:191]\n"
  "ds_read_b64_tr_b16 a[218:219], v237 offset:26624\n"
  "v_cvt_pk_bf16_f32 v147, v198, v199\n"
  "v_mfma_f32_16x16x32_bf16 v[192:195], a[172:175], v[12:15], v[200:203]\n"
  "ds_read_b64_tr_b16 a[208:209], v237 offset:8192\n"
  "v_mfma_f32_16x16x32_bf16 v[192:195], a[168:171], v[8:11], v[192:195]\n"
  "ds_read_b64_tr_b16 a[210:211], v237 offset:10240\n"
  "v_mfma_f32_16x16x32_bf16 v[192:195], a[164:167], v[4:7], v[192:195]\n"
  "ds_read_b64_tr_b16 a[212:213], v241 offset:8192\n"
  "v_mfma_f32_16x16x32_bf16 v[192:195], a[160:163], v[0:3], v[192:195]\n"
  "s_waitcnt lgkmcnt(14)\n"
  "buffer_load_dwordx4 v252, s[44:47], s71 offen offset:2048 nt lds\n"
  "v_mfma_f32_16x16x32_bf16 a[16:19], v[80:83], a[192:195], a[16:19]\n"
  "s_waitcnt lgkmcnt(13)\n"
  "v_exp_f32 v188, v188\n"
  "v_mfma_f32_16x16x32_bf16 a[16:19], v[84:87], a[196:199], a[16:19]\n"
  "s_waitcnt lgkmcnt(11)\n"
  "v_exp_f32 v189, v189\n"
  "v_mfma_f32_16x16x32_bf16 a[16:19], v[116:119], a[200:203], a[16:19]\n"
  "s_waitcnt lgkmcnt(9)\n"
  "v_exp_f32 v190, v190\n"
  "v_mfma_f32_16x16x32_bf16 a[16:19], v[120:123], a[204:207], a[16:19]\n"
  "v_exp_f32 v191, v191\n"
  "v_mfma_f32_16x16x32_bf16 a[48:51], v[128:131], a[204:207], a[48:51]\n"
  "v_exp_f32 v192, v192\n"
  "v_mfma_f32_16x16x32_bf16 a[48:51], v[124:127], a[200:203], a[48:51]\n"
  "v_exp_f32 v193, v193\n"
  "v_mfma_f32_16x16x32_bf16 a[48:51], v[88:91], a[192:195], a[48:51]\n"
  "v_exp_f32 v194, v194\n"
  "v_mfma_f32_16x16x32_bf16 a[48:51], v[92:95], a[196:199], a[48:51]\n"
  "v_exp_f32 v195, v195\n"
  "v_mfma_f32_16x16x32_bf16 v[196:199], a[160:163], v[16:19], v[204:207]\n"
  "v_cvt_pk_bf16_f32 v184, v188, v189\n"
  "v_cvt_pk_bf16_f32 v185, v190, v191\n"
  "v_mfma_f32_16x16x32_bf16 v[196:199], a[164:167], v[20:23], v[196:199]\n"
  "v_add_f32 v220, v188, v220\n"
  "v_add_f32 v221, v189, v221\n"
  "v_mfma_f32_16x16x32_bf16 v[196:199], a[168:171], v[24:27], v[196:199]\n"
  "ds_read_b64_tr_b16 a[214:215], v241 offset:10240\n"
  "v_add_f32 v220, v190, v220\n"
  "v_mfma_f32_16x16x32_bf16 v[196:199], a[172:175], v[28:31], v[196:199]\n"
  "ds_read_b64_tr_b16 a[220:221], v241 offset:24576\n"
  "v_add_f32 v221, v191, v221\n"
  "v_mfma_f32_16x16x32_bf16 v[188:191], a[172:175], v[44:47], v[208:211]\n"
  "ds_read_b64_tr_b16 a[222:223], v241 offset:26624\n"
  "v_cvt_pk_bf16_f32 v152, v192, v193\n"
  "v_mfma_f32_16x16x32_bf16 v[188:191], a[168:171], v[40:43], v[188:191]\n"
  "v_cvt_pk_bf16_f32 v153, v194, v195\n"
  "v_mfma_f32_16x16x32_bf16 v[188:191], a[164:167], v[36:39], v[188:191]\n"
  "v_mfma_f32_16x16x32_bf16 v[188:191], a[160:163], v[32:35], v[188:191]\n"
  "buffer_load_dwordx4 v253, s[44:47], s71 offen offset:3072 nt lds\n"
  "v_mfma_f32_16x16x32_bf16 a[80:83], v[96:99], a[192:195], a[80:83]\n"
  "s_add_u32 s71, s71, s73\n"
  "v_exp_f32 v196, v196\n"
  "v_mfma_f32_16x16x32_bf16 a[80:83], v[100:103], a[196:199], a[80:83]\n"
  "s_and_b32 s71, s71, s8\n"
  "v_exp_f32 v197, v197\n"
  "v_mfma_f32_16x16x32_bf16 a[80:83], v[132:135], a[200:203], a[80:83]\n"
  "v_exp_f32 v198, v198\n"
  "v_mfma_f32_16x16x32_bf16 a[80:83], v[136:139], a[204:207], a[80:83]\n"
  "v_exp_f32 v199, v199\n"
  "v_mfma_f32_16x16x32_bf16 a[112:115], v[144:147], a[204:207], a[112:115]\n"
  "v_exp_f32 v188, v188\n"
  "v_mfma_f32_16x16x32_bf16 a[112:115], v[140:143], a[200:203], a[112:115]\n"
  "v_exp_f32 v189, v189\n"
  "v_mfma_f32_16x16x32_bf16 a[112:115], v[104:107], a[192:195], a[112:115]\n"
  "v_exp_f32 v190, v190\n"
  "v_mfma_f32_16x16x32_bf16 a[112:115], v[108:111], a[196:199], a[112:115]\n"
  "v_exp_f32 v191, v191\n"
  "v_smfmac_f32_16x16x64_bf16 a[224:227], a[240:243], v[116:123], v224\n"
  "v_cvt_pk_bf16_f32 v160, v196, v197\n"
  "v_mfma_f32_16x16x32_bf16 v[192:195], a[160:163], v[48:51], v[212:215]\n"
  "ds_read_b64_tr_b16 a[204:205], v242 offset:24576\n"
  "v_cvt_pk_bf16_f32 v161, v198, v199\n"
  "v_mfma_f32_16x16x32_bf16 v[192:195], a[164:167], v[52:55], v[192:195]\n"
  "ds_read_b64_tr_b16 a[206:207], v242 offset:26624\n"
  "v_cvt_pk_bf16_f32 v168, v188, v189\n"
  "v_mfma_f32_16x16x32_bf16 v[192:195], a[168:171], v[56:59], v[192:195]\n"
  "s_add_u32 m0, s17, 16384\n"
  "ds_read_b64_tr_b16 a[196:197], v242 offset:8192\n"
  "v_cvt_pk_bf16_f32 v169, v190, v191\n"
  "v_mfma_f32_16x16x32_bf16 v[192:195], a[172:175], v[60:63], v[192:195]\n"
  "s_waitcnt lgkmcnt(14)\n"
  "ds_read_b64_tr_b16 a[198:199], v242 offset:10240\n"
  "v_mfma_f32_16x16x32_bf16 v[196:199], a[188:191], v[60:63], v[212:215]\n"
  "s_waitcnt lgkmcnt(14)\n"
  "ds_read_b128 a[160:163], v232 offset:0\n"
  "v_mfma_f32_16x16x32_bf16 v[196:199], a[184:187], v[56:59], v[196:199]\n"
  "s_waitcnt lgkmcnt(14)\n"
  "ds_read_b128 a[164:167], v233 offset:0\n"
  "v_mfma_f32_16x16x32_bf16 v[196:199], a[180:183], v[52:55], v[196:199]\n"
  "s_waitcnt lgkmcnt(14)\n"
  "ds_read_b128 a[168:171], v234 offset:0\n"
  "v_mfma_f32_16x16x32_bf16 v[196:199], a[176:179], v[48:51], v[196:199]\n"
  "buffer_load_dwordx4 v254, s[48:51], s72 offen nt lds\n"
  "v_mfma_f32_16x16x32_bf16 a[144:147], v[112:115], a[192:195], a[144:147]\n"
  "v_exp_f32 v192, v192\n"
  "v_mfma_f32_16x16x32_bf16 a[144:147], v[148:151], a[200:203], a[144:147]\n"
  "s_waitcnt lgkmcnt(13)\n"
  "v_exp_f32 v193, v193\n"
  "v_mfma_f32_16x16x32_bf16 a[148:151], v[148:151], a[216:219], a[148:151]\n"
  "s_waitcnt lgkmcnt(11)\n"
  "v_exp_f32 v194, v194\n"
  "v_mfma_f32_16x16x32_bf16 a[148:151], v[112:115], a[208:211], a[148:151]\n"
  "v_exp_f32 v195, v195\n"
  "v_mfma_f32_16x16x32_bf16 a[20:23], v[80:83], a[208:211], a[20:23]\n"
  "v_exp_f32 v196, v196\n"
  "v_mfma_f32_16x16x32_bf16 a[20:23], v[116:119], a[216:219], a[20:23]\n"
  "s_waitcnt lgkmcnt(9)\n"
  "v_exp_f32 v197, v197\n"
  "v_mfma_f32_16x16x32_bf16 a[20:23], v[84:87], a[212:215], a[20:23]\n"
  "s_waitcnt lgkmcnt(7)\n"
  "v_exp_f32 v198, v198\n"
  "v_mfma_f32_16x16x32_bf16 a[20:23], v[120:123], a[220:223], a[20:23]\n"
  "v_exp_f32 v199, v199\n"
  "v_mfma_f32_16x16x32_bf16 v[188:191], a[176:179], v[32:35], v[208:211]\n"
  "ds_read_b64_tr_b16 a[200:201], v238 offset:24576\n"
  "v_cvt_pk_bf16_f32 v176, v192, v193\n"
  "v_mfma_f32_16x16x32_bf16 v[188:191], a[180:183], v[36:39], v[188:191]\n"
  "ds_read_b64_tr_b16 a[202:203], v238 offset:26624\n"
  "v_cvt_pk_bf16_f32 v177, v194, v195\n"
  "v_mfma_f32_16x16x32_bf16 v[188:191], a[184:187], v[40:43], v[188:191]\n"
  "ds_read_b64_tr_b16 a[192:193], v238 offset:8192\n"
  "v_cvt_pk_bf16_f32 v178, v196, v197\n"
  "v_mfma_f32_16x16x32_bf16 v[188:191], a[188:191], v[44:47], v[188:191]\n"
  "ds_read_b64_tr_b16 a[194:195], v238 offset:10240\n"
  "v_cvt_pk_bf16_f32 v179, v198, v199\n"
  "v_mfma_f32_16x16x32_bf16 v[192:195], a[188:191], v[28:31], v[204:207]\n"
  "ds_read_b128 a[172:175], v235 offset:0\n"
  "v_mfma_f32_16x16x32_bf16 v[192:195], a[184:187], v[24:27], v[192:195]\n"
  "v_mfma_f32_16x16x32_bf16 v[192:195], a[180:183], v[20:23], v[192:195]\n"
  "v_mfma_f32_16x16x32_bf16 v[192:195], a[176:179], v[16:19], v[192:195]\n"
  "buffer_load_dwordx4 v255, s[48:51], s72 offen offset:1024 nt lds\n"
  "v_mfma_f32_16x16x32_bf16 a[52:55], v[88:91], a[208:211], a[52:55]\n"
  "v_exp_f32 v188, v188\n"
  "v_mfma_f32_16x16x32_bf16 a[52:55], v[92:95], a[212:215], a[52:55]\n"
  "v_exp_f32 v189, v189\n"
  "v_mfma_f32_16x16x32_bf16 a[52:55], v[124:127], a[216:219], a[52:55]\n"
  "v_exp_f32 v190, v190\n"
  "v_mfma_f32_16x16x32_bf16 a[52:55], v[128:131], a[220:223], a[52:55]\n"
  "v_exp_f32 v191, v191\n"
  "v_mfma_f32_16x16x32_bf16 a[84:87], v[136:139], a[220:223], a[84:87]\n"
  "v_exp_f32 v192, v192\n"
  "v_mfma_f32_16x16x32_bf16 a[84:87], v[132:135], a[216:219], a[84:87]\n"
  "v_exp_f32 v193, v193\n"
  "v_mfma_f32_16x16x32_bf16 a[84:87], v[96:99], a[208:211], a[84:87]\n"
  "v_exp_f32 v194, v194\n"
  "v_mfma_f32_16x16x32_bf16 a[84:87], v[100:103], a[212:215], a[84:87]\n"
  "v_exp_f32 v195, v195\n"
  "v_smfmac_f32_16x16x64_bf16 a[228:231], a[240:243], v[124:131], v224\n"
  "v_cvt_pk_bf16_f32 v170, v188, v189\n"
  "v_mfma_f32_16x16x32_bf16 v[196:199], a[176:179], v[0:3], v[200:203]\n"
  "v_cvt_pk_bf16_f32 v171, v190, v191\n"
  "v_cvt_pk_bf16_f32 v162, v192, v193\n"
  "v_mfma_f32_16x16x32_bf16 v[196:199], a[180:183], v[4:7], v[196:199]\n"
  "v_cvt_pk_bf16_f32 v163, v194, v195\n"
  "v_mfma_f32_16x16x32_bf16 v[196:199], a[184:187], v[8:11], v[196:199]\n"
  "v_mfma_f32_16x16x32_bf16 v[196:199], a[188:191], v[12:15], v[196:199]\n"
  "v_mfma_f32_16x16x32_bf16 v[188:191], a[188:191], v[76:79], v[216:219]\n"
  "ds_read_b128 a[188:191], v235 offset:2048\n"
  "v_mfma_f32_16x16x32_bf16 v[188:191], a[184:187], v[72:75], v[188:191]\n"
  "ds_read_b128 a[184:187], v234 offset:2048\n"
  "v_mfma_f32_16x16x32_bf16 v[188:191], a[180:183], v[68:71], v[188:191]\n"
  "v_mfma_f32_16x16x32_bf16 v[188:191], a[176:179], v[64:67], v[188:191]\n"
  "buffer_load_dwordx4 v254, s[48:51], s72 offen offset:2048 nt lds\n"
  "v_mfma_f32_16x16x32_bf16 a[116:119], v[104:107], a[208:211], a[116:119]\n"
  "v_exp_f32 v196, v196\n"
  "v_mfma_f32_16x16x32_bf16 a[116:119], v[108:111], a[212:215], a[116:119]\n"
  "v_exp_f32 v197, v197\n"
  "v_mfma_f32_16x16x32_bf16 a[116:119], v[140:143], a[216:219], a[116:119]\n"
  "v_exp_f32 v198, v198\n"
  "v_mfma_f32_16x16x32_bf16 a[116:119], v[144:147], a[220:223], a[116:119]\n"
  "s_waitcnt lgkmcnt(12)\n"
  "v_exp_f32 v199, v199\n"
  "v_mfma_f32_16x16x32_bf16 a[120:123], v[144:147], a[204:207], a[120:123]\n"
  "s_waitcnt lgkmcnt(5)\n"
  "v_exp_f32 v188, v188\n"
  "v_mfma_f32_16x16x32_bf16 a[120:123], v[140:143], a[200:203], a[120:123]\n"
  "s_waitcnt lgkmcnt(3)\n"
  "v_exp_f32 v189, v189\n"
  "v_mfma_f32_16x16x32_bf16 a[120:123], v[104:107], a[192:195], a[120:123]\n"
  "v_exp_f32 v190, v190\n"
  "v_mfma_f32_16x16x32_bf16 a[120:123], v[108:111], a[196:199], a[120:123]\n"
  "v_exp_f32 v191, v191\n"
  "v_mfma_f32_16x16x32_bf16 v[192:195], a[160:163], v[0:3], v[200:203]\n"
  "v_cvt_pk_bf16_f32 v154, v196, v197\n"
  "v_cvt_pk_bf16_f32 v155, v198, v199\n"
  "v_mfma_f32_16x16x32_bf16 v[192:195], a[164:167], v[4:7], v[192:195]\n"
  "v_cvt_pk_bf16_f32 v186, v188, v189\n"
  "v_cvt_pk_bf16_f32 v187, v190, v191\n"
  "v_mfma_f32_16x16x32_bf16 v[192:195], a[168:171], v[8:11], v[192:195]\n"
  "s_waitcnt lgkmcnt(2)\n"
  "ds_read_b64_tr_b16 a[216:217], v239 offset:24576\n"
  "v_add_f32 v220, v188, v220\n"
  "v_mfma_f32_16x16x32_bf16 v[192:195], a[172:175], v[12:15], v[192:195]\n"
  "ds_read_b64_tr_b16 a[218:219], v239 offset:26624\n"
  "v_add_f32 v221, v189, v221\n"
  "v_mfma_f32_16x16x32_bf16 v[196:199], a[172:175], v[28:31], v[204:207]\n"
  "ds_read_b64_tr_b16 a[208:209], v239 offset:8192\n"
  "v_add_f32 v220, v190, v220\n"
  "v_mfma_f32_16x16x32_bf16 v[196:199], a[168:171], v[24:27], v[196:199]\n"
  "ds_read_b64_tr_b16 a[210:211], v239 offset:10240\n"
  "v_add_f32 v221, v191, v221\n"
  "v_mfma_f32_16x16x32_bf16 v[196:199], a[164:167], v[20:23], v[196:199]\n"
  "ds_read_b128 a[176:179], v232 offset:2048\n"
  "v_mfma_f32_16x16x32_bf16 v[196:199], a[160:163], v[16:19], v[196:199]\n"
  "buffer_load_dwordx4 v255, s[48:51], s72 offen offset:3072 nt lds\n"
  "v_mfma_f32_16x16x32_bf16 a[88:91], v[96:99], a[192:195], a[88:91]\n"
  "s_add_u32 s72, s72, s74\n"
  "v_exp_f32 v192, v192\n"
  "v_mfma_f32_16x16x32_bf16 a[88:91], v[100:103], a[196:199], a[88:91]\n"
  "s_and_b32 s72, s72, s9\n"
  "v_exp_f32 v193, v193\n"
  "v_mfma_f32_16x16x32_bf16 a[88:91], v[132:135], a[200:203], a[88:91]\n"
  "v_exp_f32 v194, v194\n"
  "v_mfma_f32_16x16x32_bf16 a[88:91], v[136:139], a[204:207], a[88:91]\n"
  "v_exp_f32 v195, v195\n"
  "v_mfma_f32_16x16x32_bf16 a[56:59], v[128:131], a[204:207], a[56:59]\n"
  "v_exp_f32 v196, v196\n"
  "v_mfma_f32_16x16x32_bf16 a[56:59], v[124:127], a[200:203], a[56:59]\n"
  "v_exp_f32 v197, v197\n"
  "v_mfma_f32_16x16x32_bf16 a[56:59], v[88:91], a[192:195], a[56:59]\n"
  "v_exp_f32 v198, v198\n"
  "v_mfma_f32_16x16x32_bf16 a[56:59], v[92:95], a[196:199], a[56:59]\n"
  "v_exp_f32 v199, v199\n"
  "v_smfmac_f32_16x16x64_bf16 a[232:235], a[240:243], v[132:139], v224\n"
  "v_cvt_pk_bf16_f32 v156, v192, v193\n"
  "v_mfma_f32_16x16x32_bf16 v[188:191], a[160:163], v[32:35], v[208:211]\n"
  "ds_read_b128 a[180:183], v233 offset:2048\n"
  "v_cvt_pk_bf16_f32 v157, v194, v195\n"
  "v_mfma_f32_16x16x32_bf16 v[188:191], a[164:167], v[36:39], v[188:191]\n"
  "ds_read_b64_tr_b16 a[212:213], v243 offset:8192\n"
  "v_cvt_pk_bf16_f32 v164, v196, v197\n"
  "v_mfma_f32_16x16x32_bf16 v[188:191], a[168:171], v[40:43], v[188:191]\n"
  "ds_read_b64_tr_b16 a[214:215], v243 offset:10240\n"
  "v_cvt_pk_bf16_f32 v165, v198, v199\n"
  "v_mfma_f32_16x16x32_bf16 v[188:191], a[172:175], v[44:47], v[188:191]\n"
  "ds_read_b64_tr_b16 a[220:221], v243 offset:24576\n"
  "v_mfma_f32_16x16x32_bf16 v[192:195], a[172:175], v[60:63], v[212:215]\n"
  "ds_read_b64_tr_b16 a[222:223], v243 offset:26624\n"
  "v_mfma_f32_16x16x32_bf16 v[192:195], a[168:171], v[56:59], v[192:195]\n"
  "s_add_u32 m0, s83, s15\n"
  "s_add_u32 s83, s83, 0x3400\n"
  "s_and_b32 s83, s83, 0xcc00\n"
  "v_mfma_f32_16x16x32_bf16 v[192:195], a[164:167], v[52:55], v[192:195]\n"
  "buffer_load_dwordx4 v225, s[60:63], s82 offen lds\n"
  "v_mfma_f32_16x16x32_bf16 v[192:195], a[160:163], v[48:51], v[192:195]\n"
  "s_add_u32 s82, s82, 0xf400\n"
  "v_xor_b32 v225, 32, v225\n"
  "v_mfma_f32_16x16x32_bf16 a[24:27], v[84:87], a[196:199], a[24:27]\n"
  "s_and_b32 s82, s82, 0x30c00\n"
  "v_exp_f32 v188, v188\n"
  "v_mfma_f32_16x16x32_bf16 a[24:27], v[120:123], a[204:207], a[24:27]\n"
  "v_exp_f32 v189, v189\n"
  "v_mfma_f32_16x16x32_bf16 a[24:27], v[80:83], a[192:195], a[24:27]\n"
  "v_exp_f32 v190, v190\n"
  "v_mfma_f32_16x16x32_bf16 a[24:27], v[116:119], a[200:203], a[24:27]\n"
  "v_exp_f32 v191, v191\n"
  "v_mfma_f32_16x16x32_bf16 a[152:155], v[112:115], a[192:195], a[152:155]\n"
  "v_exp_f32 v192, v192\n"
  "v_mfma_f32_16x16x32_bf16 a[152:155], v[148:151], a[200:203], a[152:155]\n"
  "s_waitcnt lgkmcnt(8)\n"
  "v_exp_f32 v193, v193\n"
  "v_mfma_f32_16x16x32_bf16 a[156:159], v[148:151], a[216:219], a[156:159]\n"
  "s_waitcnt lgkmcnt(6)\n"
  "v_exp_f32 v194, v194\n"
  "v_mfma_f32_16x16x32_bf16 a[156:159], v[112:115], a[208:211], a[156:159]\n"
  "v_exp_f32 v195, v195\n"
  "v_smfmac_f32_16x16x64_bf16 a[236:239], a[240:243], v[140:147], v224\n"
  "s_waitcnt vmcnt(9) lgkmcnt(0)\n"
  "s_barrier\n"
  "v_mfma_f32_16x16x32_bf16 v[196:199], a[176:179], v[0:3], v[200:203]\n"
  "ds_read_b128 a[160:163], v228 offset:32768\n"
  "v_cvt_pk_bf16_f32 v172, v188, v189\n"
  "v_mfma_f32_16x16x32_bf16 v[196:199], a[180:183], v[4:7], v[196:199]\n"
  "ds_read_b128 a[164:167], v229 offset:32768\n"
  "v_cvt_pk_bf16_f32 v173, v190, v191\n"
  "v_mfma_f32_16x16x32_bf16 v[196:199], a[184:187], v[8:11], v[196:199]\n"
  "ds_read_b128 a[168:171], v230 offset:32768\n"
  "v_cvt_pk_bf16_f32 v180, v192, v193\n"
  "v_mfma_f32_16x16x32_bf16 v[196:199], a[188:191], v[12:15], v[196:199]\n"
  "ds_read_b128 a[172:175], v231 offset:32768\n"
  "v_cvt_pk_bf16_f32 v181, v194, v195\n"
  "v_mfma_f32_16x16x32_bf16 v[188:191], a[188:191], v[28:31], v[204:207]\n"
  "ds_read_b64_tr_b16 a[192:193], v236 offset:16384\n"
  "v_mfma_f32_16x16x32_bf16 v[188:191], a[184:187], v[24:27], v[188:191]\n"
  "ds_read_b64_tr_b16 a[194:195], v236 offset:18432\n"
  "v_mfma_f32_16x16x32_bf16 v[188:191], a[180:183], v[20:23], v[188:191]\n"
  "s_add_u32 m0, s67, 114688\n"
  "ds_read_b64_tr_b16 a[196:197], v240 offset:16384\n"
  "v_mfma_f32_16x16x32_bf16 v[188:191], a[176:179], v[16:19], v[188:191]\n"
  "buffer_load_dwordx4 v252, s[44:47], s71 offen nt lds\n"
  "v_mfma_f32_16x16x32_bf16 a[28:31], v[80:83], a[208:211], a[28:31]\n"
  "v_exp_f32 v196, v196\n"
  "v_mfma_f32_16x16x32_bf16 a[28:31], v[84:87], a[212:215], a[28:31]\n"
  "v_exp_f32 v197, v197\n"
  "v_mfma_f32_16x16x32_bf16 a[28:31], v[116:119], a[216:219], a[28:31]\n"
  "v_exp_f32 v198, v198\n"
  "v_mfma_f32_16x16x32_bf16 a[28:31], v[120:123], a[220:223], a[28:31]\n"
  "v_exp_f32 v199, v199\n"
  "v_mfma_f32_16x16x32_bf16 a[60:63], v[128:131], a[220:223], a[60:63]\n"
  "v_exp_f32 v188, v188\n"
  "v_mfma_f32_16x16x32_bf16 a[60:63], v[124:127], a[216:219], a[60:63]\n"
  "v_exp_f32 v189, v189\n"
  "v_mfma_f32_16x16x32_bf16 a[60:63], v[88:91], a[208:211], a[60:63]\n"
  "v_exp_f32 v190, v190\n"
  "v_mfma_f32_16x16x32_bf16 a[60:63], v[92:95], a[212:215], a[60:63]\n"
  "v_exp_f32 v191, v191\n"
  "v_mfma_f32_16x16x32_bf16 v[192:195], a[176:179], v[32:35], v[208:211]\n"
  "ds_read_b64_tr_b16 a[198:199], v240 offset:18432\n"
  "v_cvt_pk_bf16_f32 v158, v196, v197\n"
  "v_mfma_f32_16x16x32_bf16 v[192:195], a[180:183], v[36:39], v[192:195]\n"
  "ds_read_b64_tr_b16 a[200:201], v244 offset:0\n"
  "v_cvt_pk_bf16_f32 v159, v198, v199\n"
  "v_mfma_f32_16x16x32_bf16 v[192:195], a[184:187], v[40:43], v[192:195]\n"
  "ds_read_b64_tr_b16 a[202:203], v244 offset:2048\n"
  "v_cvt_pk_bf16_f32 v166, v188, v189\n"
  "v_mfma_f32_16x16x32_bf16 v[192:195], a[188:191], v[44:47], v[192:195]\n"
  "ds_read_b64_tr_b16 a[204:205], v248 offset:0\n"
  "v_cvt_pk_bf16_f32 v167, v190, v191\n"
  "v_mfma_f32_16x16x32_bf16 v[196:199], a[188:191], v[60:63], v[212:215]\n"
  "ds_read_b64_tr_b16 a[206:207], v248 offset:2048\n"
  "v_mfma_f32_16x16x32_bf16 v[196:199], a[184:187], v[56:59], v[196:199]\n"
  "ds_read_b128 a[188:191], v231 offset:34816\n"
  "v_mfma_f32_16x16x32_bf16 v[196:199], a[180:183], v[52:55], v[196:199]\n"
  "ds_read_b128 a[184:187], v230 offset:34816\n"
  "v_mfma_f32_16x16x32_bf16 v[196:199], a[176:179], v[48:51], v[196:199]\n"
  "buffer_load_dwordx4 v253, s[44:47], s71 offen offset:1024 nt lds\n"
  "v_mfma_f32_16x16x32_bf16 a[92:95], v[96:99], a[208:211], a[92:95]\n"
  "v_exp_f32 v192, v192\n"
  "v_mfma_f32_16x16x32_bf16 a[92:95], v[100:103], a[212:215], a[92:95]\n"
  "v_exp_f32 v193, v193\n"
  "v_mfma_f32_16x16x32_bf16 a[92:95], v[132:135], a[216:219], a[92:95]\n"
  "v_exp_f32 v194, v194\n"
  "v_mfma_f32_16x16x32_bf16 a[92:95], v[136:139], a[220:223], a[92:95]\n"
  "v_exp_f32 v195, v195\n"
  "v_mfma_f32_16x16x32_bf16 a[124:127], v[144:147], a[220:223], a[124:127]\n"
  "v_exp_f32 v196, v196\n"
  "v_mfma_f32_16x16x32_bf16 a[124:127], v[140:143], a[216:219], a[124:127]\n"
  "v_exp_f32 v197, v197\n"
  "v_mfma_f32_16x16x32_bf16 a[124:127], v[104:107], a[208:211], a[124:127]\n"
  "v_exp_f32 v198, v198\n"
  "v_mfma_f32_16x16x32_bf16 a[124:127], v[108:111], a[212:215], a[124:127]\n"
  "v_exp_f32 v199, v199\n"
  "L_loop2_%=:\n"
  "s_waitcnt lgkmcnt(13)\n"
  "v_mfma_f32_16x16x32_bf16 v[188:191], a[160:163], v[64:67], v[216:219]\n"
  "s_waitcnt lgkmcnt(12)\n"
  "ds_read_b128 a[180:183], v229 offset:34816\n"
  "v_cvt_pk_bf16_f32 v174, v192, v193\n"
  "v_mfma_f32_16x16x32_bf16 v[188:191], a[164:167], v[68:71], v[188:191]\n"
  "s_waitcnt lgkmcnt(12)\n"
  "ds_read_b128 a[176:179], v228 offset:34816\n"
  "v_cvt_pk_bf16_f32 v175, v194, v195\n"
  "v_mfma_f32_16x16x32_bf16 v[188:191], a[168:171], v[72:75], v[188:191]\n"
  "s_waitcnt lgkmcnt(12)\n"
  "ds_read_b64_tr_b16 a[216:217], v245 offset:0\n"
  "v_cvt_pk_bf16_f32 v182, v196, v197\n"
  "v_mfma_f32_16x16x32_bf16 v[188:191], a[172:175], v[76:79], v[188:191]\n"
  "ds_read_b64_tr_b16 a[218:219], v245 offset:2048\n"
  "v_cvt_pk_bf16_f32 v183, v198, v199\n"
  "v_mfma_f32_16x16x32_bf16 v[192:195], a[172:175], v[12:15], v[200:203]\n"
  "ds_read_b64_tr_b16 a[208:209], v237 offset:16384\n"
  "v_mfma_f32_16x16x32_bf16 v[192:195], a[168:171], v[8:11], v[192:195]\n"
  "ds_read_b64_tr_b16 a[210:211], v237 offset:18432\n"
  "v_mfma_f32_16x16x32_bf16 v[192:195], a[164:167], v[4:7], v[192:195]\n"
  "ds_read_b64_tr_b16 a[212:213], v241 offset:16384\n"
  "v_mfma_f32_16x16x32_bf16 v[192:195], a[160:163], v[0:3], v[192:195]\n"
  "s_waitcnt lgkmcnt(14)\n"
  "buffer_load_dwordx4 v252, s[44:47], s71 offen offset:2048 nt lds\n"
  "v_mfma_f32_16x16x32_bf16 a[0:3], v[116:119], a[192:195], a[0:3]\n"
  "s_waitcnt lgkmcnt(13)\n"
  "v_exp_f32 v188, v188\n"
  "v_mfma_f32_16x16x32_bf16 a[0:3], v[120:123], a[196:199], a[0:3]\n"
  "s_waitcnt lgkmcnt(11)\n"
  "v_exp_f32 v189, v189\n"
  "v_mfma_f32_16x16x32_bf16 a[0:3], v[152:155], a[200:203], a[0:3]\n"
  "s_waitcnt lgkmcnt(9)\n"
  "v_exp_f32 v190, v190\n"
  "v_mfma_f32_16x16x32_bf16 a[0:3], v[156:159], a[204:207], a[0:3]\n"
  "v_exp_f32 v191, v191\n"
  "v_mfma_f32_16x16x32_bf16 a[32:35], v[164:167], a[204:207], a[32:35]\n"
  "v_exp_f32 v192, v192\n"
  "v_mfma_f32_16x16x32_bf16 a[32:35], v[160:163], a[200:203], a[32:35]\n"
  "v_exp_f32 v193, v193\n"
  "v_mfma_f32_16x16x32_bf16 a[32:35], v[124:127], a[192:195], a[32:35]\n"
  "v_exp_f32 v194, v194\n"
  "v_mfma_f32_16x16x32_bf16 a[32:35], v[128:131], a[196:199], a[32:35]\n"
  "v_exp_f32 v195, v195\n"
  "v_mfma_f32_16x16x32_bf16 v[196:199], a[160:163], v[16:19], v[204:207]\n"
  "v_cvt_pk_bf16_f32 v112, v188, v189\n"
  "v_cvt_pk_bf16_f32 v113, v190, v191\n"
  "v_mfma_f32_16x16x32_bf16 v[196:199], a[164:167], v[20:23], v[196:199]\n"
  "v_add_f32 v220, v188, v220\n"
  "v_add_f32 v221, v189, v221\n"
  "v_mfma_f32_16x16x32_bf16 v[196:199], a[168:171], v[24:27], v[196:199]\n"
  "ds_read_b64_tr_b16 a[214:215], v241 offset:18432\n"
  "v_add_f32 v220, v190, v220\n"
  "v_mfma_f32_16x16x32_bf16 v[196:199], a[172:175], v[28:31], v[196:199]\n"
  "ds_read_b64_tr_b16 a[220:221], v249 offset:0\n"
  "v_add_f32 v221, v191, v221\n"
  "v_mfma_f32_16x16x32_bf16 v[188:191], a[172:175], v[44:47], v[208:211]\n"
  "ds_read_b64_tr_b16 a[222:223], v249 offset:2048\n"
  "v_cvt_pk_bf16_f32 v80, v192, v193\n"
  "v_mfma_f32_16x16x32_bf16 v[188:191], a[168:171], v[40:43], v[188:191]\n"
  "v_cvt_pk_bf16_f32 v81, v194, v195\n"
  "v_mfma_f32_16x16x32_bf16 v[188:191], a[164:167], v[36:39], v[188:191]\n"
  "v_mfma_f32_16x16x32_bf16 v[188:191], a[160:163], v[32:35], v[188:191]\n"
  "buffer_load_dwordx4 v253, s[44:47], s71 offen offset:3072 nt lds\n"
  "v_mfma_f32_16x16x32_bf16 a[64:67], v[132:135], a[192:195], a[64:67]\n"
  "s_add_u32 s71, s71, s73\n"
  "v_exp_f32 v196, v196\n"
  "v_mfma_f32_16x16x32_bf16 a[64:67], v[136:139], a[196:199], a[64:67]\n"
  "s_and_b32 s71, s71, s8\n"
  "v_exp_f32 v197, v197\n"
  "v_mfma_f32_16x16x32_bf16 a[64:67], v[168:171], a[200:203], a[64:67]\n"
  "v_exp_f32 v198, v198\n"
  "v_mfma_f32_16x16x32_bf16 a[64:67], v[172:175], a[204:207], a[64:67]\n"
  "v_exp_f32 v199, v199\n"
  "v_mfma_f32_16x16x32_bf16 a[96:99], v[180:183], a[204:207], a[96:99]\n"
  "v_exp_f32 v188, v188\n"
  "v_mfma_f32_16x16x32_bf16 a[96:99], v[176:179], a[200:203], a[96:99]\n"
  "v_exp_f32 v189, v189\n"
  "v_mfma_f32_16x16x32_bf16 a[96:99], v[140:143], a[192:195], a[96:99]\n"
  "v_exp_f32 v190, v190\n"
  "v_mfma_f32_16x16x32_bf16 a[96:99], v[144:147], a[196:199], a[96:99]\n"
  "v_exp_f32 v191, v191\n"
  "v_smfmac_f32_16x16x64_bf16 a[224:227], a[240:243], v[152:159], v224\n"
  "v_cvt_pk_bf16_f32 v88, v196, v197\n"
  "v_mfma_f32_16x16x32_bf16 v[192:195], a[160:163], v[48:51], v[212:215]\n"
  "ds_read_b64_tr_b16 a[204:205], v250 offset:0\n"
  "v_cvt_pk_bf16_f32 v89, v198, v199\n"
  "v_mfma_f32_16x16x32_bf16 v[192:195], a[164:167], v[52:55], v[192:195]\n"
  "ds_read_b64_tr_b16 a[206:207], v250 offset:2048\n"
  "v_cvt_pk_bf16_f32 v96, v188, v189\n"
  "v_mfma_f32_16x16x32_bf16 v[192:195], a[168:171], v[56:59], v[192:195]\n"
  "s_add_u32 m0, s16, 0\n"
  "ds_read_b64_tr_b16 a[196:197], v242 offset:16384\n"
  "v_cvt_pk_bf16_f32 v97, v190, v191\n"
  "v_mfma_f32_16x16x32_bf16 v[192:195], a[172:175], v[60:63], v[192:195]\n"
  "s_waitcnt lgkmcnt(14)\n"
  "ds_read_b64_tr_b16 a[198:199], v242 offset:18432\n"
  "v_mfma_f32_16x16x32_bf16 v[196:199], a[188:191], v[60:63], v[212:215]\n"
  "s_waitcnt lgkmcnt(14)\n"
  "ds_read_b128 a[160:163], v232 offset:32768\n"
  "v_mfma_f32_16x16x32_bf16 v[196:199], a[184:187], v[56:59], v[196:199]\n"
  "s_waitcnt lgkmcnt(14)\n"
  "ds_read_b128 a[164:167], v233 offset:32768\n"
  "v_mfma_f32_16x16x32_bf16 v[196:199], a[180:183], v[52:55], v[196:199]\n"
  "s_waitcnt lgkmcnt(14)\n"
  "ds_read_b128 a[168:171], v234 offset:32768\n"
  "v_mfma_f32_16x16x32_bf16 v[196:199], a[176:179], v[48:51], v[196:199]\n"
  "buffer_load_dwordx4 v254, s[48:51], s72 offen nt lds\n"
  "v_mfma_f32_16x16x32_bf16 a[128:131], v[148:151], a[192:195], a[128:131]\n"
  "v_exp_f32 v192, v192\n"
  "v_mfma_f32_16x16x32_bf16 a[128:131], v[184:187], a[200:203], a[128:131]\n"
  "s_waitcnt lgkmcnt(13)\n"
  "v_exp_f32 v193, v193\n"
  "v_mfma_f32_16x16x32_bf16 a[132:135], v[184:187], a[216:219], a[132:135]\n"
  "s_waitcnt lgkmcnt(11)\n"
  "v_exp_f32 v194, v194\n"
  "v_mfma_f32_16x16x32_bf16 a[132:135], v[148:151], a[208:211], a[132:135]\n"
  "v_exp_f32 v195, v195\n"
  "v_mfma_f32_16x16x32_bf16 a[4:7], v[116:119], a[208:211], a[4:7]\n"
  "v_exp_f32 v196, v196\n"
  "v_mfma_f32_16x16x32_bf16 a[4:7], v[152:155], a[216:219], a[4:7]\n"
  "s_waitcnt lgkmcnt(9)\n"
  "v_exp_f32 v197, v197\n"
  "v_mfma_f32_16x16x32_bf16 a[4:7], v[120:123], a[212:215], a[4:7]\n"
  "s_waitcnt lgkmcnt(7)\n"
  "v_exp_f32 v198, v198\n"
  "v_mfma_f32_16x16x32_bf16 a[4:7], v[156:159], a[220:223], a[4:7]\n"
  "v_exp_f32 v199, v199\n"
  "v_mfma_f32_16x16x32_bf16 v[188:191], a[176:179], v[32:35], v[208:211]\n"
  "ds_read_b64_tr_b16 a[200:201], v246 offset:0\n"
  "v_cvt_pk_bf16_f32 v104, v192, v193\n"
  "v_mfma_f32_16x16x32_bf16 v[188:191], a[180:183], v[36:39], v[188:191]\n"
  "ds_read_b64_tr_b16 a[202:203], v246 offset:2048\n"
  "v_cvt_pk_bf16_f32 v105, v194, v195\n"
  "v_mfma_f32_16x16x32_bf16 v[188:191], a[184:187], v[40:43], v[188:191]\n"
  "ds_read_b64_tr_b16 a[192:193], v238 offset:16384\n"
  "v_cvt_pk_bf16_f32 v106, v196, v197\n"
  "v_mfma_f32_16x16x32_bf16 v[188:191], a[188:191], v[44:47], v[188:191]\n"
  "ds_read_b64_tr_b16 a[194:195], v238 offset:18432\n"
  "v_cvt_pk_bf16_f32 v107, v198, v199\n"
  "v_mfma_f32_16x16x32_bf16 v[192:195], a[188:191], v[28:31], v[204:207]\n"
  "ds_read_b128 a[172:175], v235 offset:32768\n"
  "v_mfma_f32_16x16x32_bf16 v[192:195], a[184:187], v[24:27], v[192:195]\n"
  "v_mfma_f32_16x16x32_bf16 v[192:195], a[180:183], v[20:23], v[192:195]\n"
  "v_mfma_f32_16x16x32_bf16 v[192:195], a[176:179], v[16:19], v[192:195]\n"
  "buffer_load_dwordx4 v255, s[48:51], s72 offen offset:1024 nt lds\n"
  "v_mfma_f32_16x16x32_bf16 a[36:39], v[124:127], a[208:211], a[36:39]\n"
  "v_exp_f32 v188, v188\n"
  "v_mfma_f32_16x16x32_bf16 a[36:39], v[128:131], a[212:215], a[36:39]\n"
  "v_exp_f32 v189, v189\n"
  "v_mfma_f32_16x16x32_bf16 a[36:39], v[160:163], a[216:219], a[36:39]\n"
  "v_exp_f32 v190, v190\n"
  "v_mfma_f32_16x16x32_bf16 a[36:39], v[164:167], a[220:223], a[36:39]\n"
  "v_exp_f32 v191, v191\n"
  "v_mfma_f32_16x16x32_bf16 a[68:71], v[172:175], a[220:223], a[68:71]\n"
  "v_exp_f32 v192, v192\n"
  "v_mfma_f32_16x16x32_bf16 a[68:71], v[168:171], a[216:219], a[68:71]\n"
  "v_exp_f32 v193, v193\n"
  "v_mfma_f32_16x16x32_bf16 a[68:71], v[132:135], a[208:211], a[68:71]\n"
  "v_exp_f32 v194, v194\n"
  "v_mfma_f32_16x16x32_bf16 a[68:71], v[136:139], a[212:215], a[68:71]\n"
  "v_exp_f32 v195, v195\n"
  "v_smfmac_f32_16x16x64_bf16 a[228:231], a[240:243], v[160:167], v224\n"
  "v_cvt_pk_bf16_f32 v98, v188, v189\n"
  "v_mfma_f32_16x16x32_bf16 v[196:199], a[176:179], v[0:3], v[200:203]\n"
  "v_cvt_pk_bf16_f32 v99, v190, v191\n"
  "v_cvt_pk_bf16_f32 v90, v192, v193\n"
  "v_mfma_f32_16x16x32_bf16 v[196:199], a[180:183], v[4:7], v[196:199]\n"
  "v_cvt_pk_bf16_f32 v91, v194, v195\n"
  "v_mfma_f32_16x16x32_bf16 v[196:199], a[184:187], v[8:11], v[196:199]\n"
  "v_mfma_f32_16x16x32_bf16 v[196:199], a[188:191], v[12:15], v[196:199]\n"
  "v_mfma_f32_16x16x32_bf16 v[188:191], a[188:191], v[76:79], v[216:219]\n"
  "ds_read_b128 a[188:191], v235 offset:34816\n"
  "v_mfma_f32_16x16x32_bf16 v[188:191], a[184:187], v[72:75], v[188:191]\n"
  "ds_read_b128 a[184:187], v234 offset:34816\n"
  "v_mfma_f32_16x16x32_bf16 v[188:191], a[180:183], v[68:71], v[188:191]\n"
  "s_add_u32 s81, s81, 196608\n"
  "buffer_load_dword v223, v226, s[60:63], s81 offen\n"
  "v_mfma_f32_16x16x32_bf16 v[188:191], a[176:179], v[64:67], v[188:191]\n"
  "buffer_load_dwordx4 v254, s[48:51], s72 offen offset:2048 nt lds\n"
  "v_mfma_f32_16x16x32_bf16 a[100:103], v[140:143], a[208:211], a[100:103]\n"
  "v_exp_f32 v196, v196\n"
  "v_mfma_f32_16x16x32_bf16 a[100:103], v[144:147], a[212:215], a[100:103]\n"
  "v_exp_f32 v197, v197\n"
  "v_mfma_f32_16x16x32_bf16 a[100:103], v[176:179], a[216:219], a[100:103]\n"
  "v_exp_f32 v198, v198\n"
  "v_mfma_f32_16x16x32_bf16 a[100:103], v[180:183], a[220:223], a[100:103]\n"
  "s_waitcnt lgkmcnt(12)\n"
  "v_exp_f32 v199, v199\n"
  "v_mfma_f32_16x16x32_bf16 a[104:107], v[180:183], a[204:207], a[104:107]\n"
  "s_waitcnt lgkmcnt(5)\n"
  "v_exp_f32 v188, v188\n"
  "v_mfma_f32_16x16x32_bf16 a[104:107], v[176:179], a[200:203], a[104:107]\n"
  "s_waitcnt lgkmcnt(3)\n"
  "v_exp_f32 v189, v189\n"
  "v_mfma_f32_16x16x32_bf16 a[104:107], v[140:143], a[192:195], a[104:107]\n"
  "v_exp_f32 v190, v190\n"
  "v_mfma_f32_16x16x32_bf16 a[104:107], v[144:147], a[196:199], a[104:107]\n"
  "v_exp_f32 v191, v191\n"
  "v_mfma_f32_16x16x32_bf16 v[192:195], a[160:163], v[0:3], v[200:203]\n"
  "v_cvt_pk_bf16_f32 v82, v196, v197\n"
  "v_cvt_pk_bf16_f32 v83, v198, v199\n"
  "v_mfma_f32_16x16x32_bf16 v[192:195], a[164:167], v[4:7], v[192:195]\n"
  "v_cvt_pk_bf16_f32 v114, v188, v189\n"
  "v_cvt_pk_bf16_f32 v115, v190, v191\n"
  "v_mfma_f32_16x16x32_bf16 v[192:195], a[168:171], v[8:11], v[192:195]\n"
  "s_waitcnt lgkmcnt(2)\n"
  "ds_read_b64_tr_b16 a[216:217], v247 offset:0\n"
  "v_add_f32 v220, v188, v220\n"
  "v_mfma_f32_16x16x32_bf16 v[192:195], a[172:175], v[12:15], v[192:195]\n"
  "ds_read_b64_tr_b16 a[218:219], v247 offset:2048\n"
  "v_add_f32 v221, v189, v221\n"
  "v_mfma_f32_16x16x32_bf16 v[196:199], a[172:175], v[28:31], v[204:207]\n"
  "ds_read_b64_tr_b16 a[208:209], v239 offset:16384\n"
  "v_add_f32 v220, v190, v220\n"
  "v_mfma_f32_16x16x32_bf16 v[196:199], a[168:171], v[24:27], v[196:199]\n"
  "ds_read_b64_tr_b16 a[210:211], v239 offset:18432\n"
  "v_add_f32 v221, v191, v221\n"
  "v_mfma_f32_16x16x32_bf16 v[196:199], a[164:167], v[20:23], v[196:199]\n"
  "ds_read_b128 a[176:179], v232 offset:34816\n"
  "v_mfma_f32_16x16x32_bf16 v[196:199], a[160:163], v[16:19], v[196:199]\n"
  "buffer_load_dwordx4 v255, s[48:51], s72 offen offset:3072 nt lds\n"
  "v_mfma_f32_16x16x32_bf16 a[72:75], v[132:135], a[192:195], a[72:75]\n"
  "s_add_u32 s72, s72, s74\n"
  "v_exp_f32 v192, v192\n"
  "v_mfma_f32_16x16x32_bf16 a[72:75], v[136:139], a[196:199], a[72:75]\n"
  "s_and_b32 s72, s72, s9\n"
  "v_exp_f32 v193, v193\n"
  "v_mfma_f32_16x16x32_bf16 a[72:75], v[168:171], a[200:203], a[72:75]\n"
  "v_exp_f32 v194, v194\n"
  "v_mfma_f32_16x16x32_bf16 a[72:75], v[172:175], a[204:207], a[72:75]\n"
  "v_exp_f32 v195, v195\n"
  "v_mfma_f32_16x16x32_bf16 a[40:43], v[164:167], a[204:207], a[40:43]\n"
  "v_exp_f32 v196, v196\n"
  "v_mfma_f32_16x16x32_bf16 a[40:43], v[160:163], a[200:203], a[40:43]\n"
  "v_exp_f32 v197, v197\n"
  "v_mfma_f32_16x16x32_bf16 a[40:43], v[124:127], a[192:195], a[40:43]\n"
  "v_exp_f32 v198, v198\n"
  "v_mfma_f32_16x16x32_bf16 a[40:43], v[128:131], a[196:199], a[40:43]\n"
  "v_exp_f32 v199, v199\n"
  "v_smfmac_f32_16x16x64_bf16 a[232:235], a[240:243], v[168:175], v224\n"
  "v_cvt_pk_bf16_f32 v84, v192, v193\n"
  "v_mfma_f32_16x16x32_bf16 v[188:191], a[160:163], v[32:35], v[208:211]\n"
  "ds_read_b128 a[180:183], v233 offset:34816\n"
  "v_cvt_pk_bf16_f32 v85, v194, v195\n"
  "v_mfma_f32_16x16x32_bf16 v[188:191], a[164:167], v[36:39], v[188:191]\n"
  "ds_read_b64_tr_b16 a[212:213], v243 offset:16384\n"
  "v_cvt_pk_bf16_f32 v92, v196, v197\n"
  "v_mfma_f32_16x16x32_bf16 v[188:191], a[168:171], v[40:43], v[188:191]\n"
  "ds_read_b64_tr_b16 a[214:215], v243 offset:18432\n"
  "v_cvt_pk_bf16_f32 v93, v198, v199\n"
  "v_mfma_f32_16x16x32_bf16 v[188:191], a[172:175], v[44:47], v[188:191]\n"
  "ds_read_b64_tr_b16 a[220:221], v251 offset:0\n"
  "v_mfma_f32_16x16x32_bf16 v[192:195], a[172:175], v[60:63], v[212:215]\n"
  "ds_read_b64_tr_b16 a[222:223], v251 offset:2048\n"
  "v_mfma_f32_16x16x32_bf16 v[192:195], a[168:171], v[56:59], v[192:195]\n"
  "v_mfma_f32_16x16x32_bf16 v[192:195], a[164:167], v[52:55], v[192:195]\n"
  "v_mfma_f32_16x16x32_bf16 v[192:195], a[160:163], v[48:51], v[192:195]\n"
  "v_mfma_f32_16x16x32_bf16 a[8:11], v[120:123], a[196:199], a[8:11]\n"
  "s_nop 0\n"
  "v_exp_f32 v188, v188\n"
  "v_mfma_f32_16x16x32_bf16 a[8:11], v[156:159], a[204:207], a[8:11]\n"
  "v_exp_f32 v189, v189\n"
  "v_mfma_f32_16x16x32_bf16 a[8:11], v[116:119], a[192:195], a[8:11]\n"
  "v_exp_f32 v190, v190\n"
  "v_mfma_f32_16x16x32_bf16 a[8:11], v[152:155], a[200:203], a[8:11]\n"
  "v_exp_f32 v191, v191\n"
  "v_mfma_f32_16x16x32_bf16 a[136:139], v[148:151], a[192:195], a[136:139]\n"
  "v_exp_f32 v192, v192\n"
  "v_mfma_f32_16x16x32_bf16 a[136:139], v[184:187], a[200:203], a[136:139]\n"
  "s_waitcnt lgkmcnt(8)\n"
  "v_exp_f32 v193, v193\n"
  "v_mfma_f32_16x16x32_bf16 a[140:143], v[184:187], a[216:219], a[140:143]\n"
  "s_waitcnt lgkmcnt(6)\n"
  "v_exp_f32 v194, v194\n"
  "v_mfma_f32_16x16x32_bf16 a[140:143], v[148:151], a[208:211], a[140:143]\n"
  "v_exp_f32 v195, v195\n"
  "v_smfmac_f32_16x16x64_bf16 a[236:239], a[240:243], v[176:183], v224\n"
  "s_waitcnt vmcnt(9) lgkmcnt(0)\n"
  "s_barrier\n"
  "v_mfma_f32_16x16x32_bf16 v[196:199], a[176:179], v[0:3], v[200:203]\n"
  "ds_read_b128 a[160:163], v228 offset:16384\n"
  "v_cvt_pk_bf16_f32 v100, v188, v189\n"
  "v_mfma_f32_16x16x32_bf16 v[196:199], a[180:183], v[4:7], v[196:199]\n"
  "ds_read_b128 a[164:167], v229 offset:16384\n"
  "v_cvt_pk_bf16_f32 v101, v190, v191\n"
  "v_mfma_f32_16x16x32_bf16 v[196:199], a[184:187], v[8:11], v[196:199]\n"
  "ds_read_b128 a[168:171], v230 offset:16384\n"
  "v_cvt_pk_bf16_f32 v108, v192, v193\n"
  "v_mfma_f32_16x16x32_bf16 v[196:199], a[188:191], v[12:15], v[196:199]\n"
  "ds_read_b128 a[172:175], v231 offset:16384\n"
  "v_cvt_pk_bf16_f32 v109, v194, v195\n"
  "v_mfma_f32_16x16x32_bf16 v[188:191], a[188:191], v[28:31], v[204:207]\n"
  "ds_read_b64_tr_b16 a[192:193], v244 offset:8192\n"
  "v_mfma_f32_16x16x32_bf16 v[188:191], a[184:187], v[24:27], v[188:191]\n"
  "ds_read_b64_tr_b16 a[194:195], v244 offset:10240\n"
  "v_mfma_f32_16x16x32_bf16 v[188:191], a[180:183], v[20:23], v[188:191]\n"
  "s_add_u32 m0, s67, 147456\n"
  "ds_read_b64_tr_b16 a[196:197], v248 offset:8192\n"
  "v_mfma_f32_16x16x32_bf16 v[188:191], a[176:179], v[16:19], v[188:191]\n"
  "buffer_load_dwordx4 v252, s[44:47], s71 offen nt lds\n"
  "v_mfma_f32_16x16x32_bf16 a[12:15], v[116:119], a[208:211], a[12:15]\n"
  "v_exp_f32 v196, v196\n"
  "v_mfma_f32_16x16x32_bf16 a[12:15], v[120:123], a[212:215], a[12:15]\n"
  "v_exp_f32 v197, v197\n"
  "v_mfma_f32_16x16x32_bf16 a[12:15], v[152:155], a[216:219], a[12:15]\n"
  "v_exp_f32 v198, v198\n"
  "v_mfma_f32_16x16x32_bf16 a[12:15], v[156:159], a[220:223], a[12:15]\n"
  "v_exp_f32 v199, v199\n"
  "v_mfma_f32_16x16x32_bf16 a[44:47], v[164:167], a[220:223], a[44:47]\n"
  "v_exp_f32 v188, v188\n"
  "v_mfma_f32_16x16x32_bf16 a[44:47], v[160:163], a[216:219], a[44:47]\n"
  "v_exp_f32 v189, v189\n"
  "v_mfma_f32_16x16x32_bf16 a[44:47], v[124:127], a[208:211], a[44:47]\n"
  "v_exp_f32 v190, v190\n"
  "v_mfma_f32_16x16x32_bf16 a[44:47], v[128:131], a[212:215], a[44:47]\n"
  "v_exp_f32 v191, v191\n"
  "v_mfma_f32_16x16x32_bf16 v[192:195], a[176:179], v[32:35], v[208:211]\n"
  "ds_read_b64_tr_b16 a[198:199], v248 offset:10240\n"
  "v_cvt_pk_bf16_f32 v86, v196, v197\n"
  "v_mfma_f32_16x16x32_bf16 v[192:195], a[180:183], v[36:39], v[192:195]\n"
  "ds_read_b64_tr_b16 a[200:201], v244 offset:24576\n"
  "v_cvt_pk_bf16_f32 v87, v198, v199\n"
  "v_mfma_f32_16x16x32_bf16 v[192:195], a[184:187], v[40:43], v[192:195]\n"
  "ds_read_b64_tr_b16 a[202:203], v244 offset:26624\n"
  "v_cvt_pk_bf16_f32 v94, v188, v189\n"
  "v_mfma_f32_16x16x32_bf16 v[192:195], a[188:191], v[44:47], v[192:195]\n"
  "ds_read_b64_tr_b16 a[204:205], v248 offset:24576\n"
  "v_cvt_pk_bf16_f32 v95, v190, v191\n"
  "v_mfma_f32_16x16x32_bf16 v[196:199], a[188:191], v[60:63], v[212:215]\n"
  "ds_read_b64_tr_b16 a[206:207], v248 offset:26624\n"
  "v_mfma_f32_16x16x32_bf16 v[196:199], a[184:187], v[56:59], v[196:199]\n"
  "ds_read_b128 a[188:191], v231 offset:18432\n"
  "v_mfma_f32_16x16x32_bf16 v[196:199], a[180:183], v[52:55], v[196:199]\n"
  "ds_read_b128 a[184:187], v230 offset:18432\n"
  "v_mfma_f32_16x16x32_bf16 v[196:199], a[176:179], v[48:51], v[196:199]\n"
  "buffer_load_dwordx4 v253, s[44:47], s71 offen offset:1024 nt lds\n"
  "v_mfma_f32_16x16x32_bf16 a[76:79], v[132:135], a[208:211], a[76:79]\n"
  "v_exp_f32 v192, v192\n"
  "v_mfma_f32_16x16x32_bf16 a[76:79], v[136:139], a[212:215], a[76:79]\n"
  "v_exp_f32 v193, v193\n"
  "v_mfma_f32_16x16x32_bf16 a[76:79], v[168:171], a[216:219], a[76:79]\n"
  "v_exp_f32 v194, v194\n"
  "v_mfma_f32_16x16x32_bf16 a[76:79], v[172:175], a[220:223], a[76:79]\n"
  "v_exp_f32 v195, v195\n"
  "v_mfma_f32_16x16x32_bf16 a[108:111], v[180:183], a[220:223], a[108:111]\n"
  "v_exp_f32 v196, v196\n"
  "v_mfma_f32_16x16x32_bf16 a[108:111], v[176:179], a[216:219], a[108:111]\n"
  "v_exp_f32 v197, v197\n"
  "v_mfma_f32_16x16x32_bf16 a[108:111], v[140:143], a[208:211], a[108:111]\n"
  "v_exp_f32 v198, v198\n"
  "v_mfma_f32_16x16x32_bf16 a[108:111], v[144:147], a[212:215], a[108:111]\n"
  "v_exp_f32 v199, v199\n"
  "s_waitcnt lgkmcnt(13)\n"
  "v_mfma_f32_16x16x32_bf16 v[188:191], a[160:163], v[64:67], v[216:219]\n"
  "s_waitcnt lgkmcnt(12)\n"
  "ds_read_b128 a[180:183], v229 offset:18432\n"
  "v_cvt_pk_bf16_f32 v102, v192, v193\n"
  "v_mfma_f32_16x16x32_bf16 v[188:191], a[164:167], v[68:71], v[188:191]\n"
  "s_waitcnt lgkmcnt(12)\n"
  "ds_read_b128 a[176:179], v228 offset:18432\n"
  "v_cvt_pk_bf16_f32 v103, v194, v195\n"
  "v_mfma_f32_16x16x32_bf16 v[188:191], a[168:171], v[72:75], v[188:191]\n"
  "s_waitcnt lgkmcnt(12)\n"
  "ds_read_b64_tr_b16 a[216:217], v245 offset:24576\n"
  "v_cvt_pk_bf16_f32 v110, v196, v197\n"
  "v_mfma_f32_16x16x32_bf16 v[188:191], a[172:175], v[76:79], v[188:191]\n"
  "ds_read_b64_tr_b16 a[218:219], v245 offset:26624\n"
  "v_cvt_pk_bf16_f32 v111, v198, v199\n"
  "v_mfma_f32_16x16x32_bf16 v[192:195], a[172:175], v[12:15], v[200:203]\n"
  "ds_read_b64_tr_b16 a[208:209], v245 offset:8192\n"
  "v_mfma_f32_16x16x32_bf16 v[192:195], a[168:171], v[8:11], v[192:195]\n"
  "ds_read_b64_tr_b16 a[210:211], v245 offset:10240\n"
  "v_mfma_f32_16x16x32_bf16 v[192:195], a[164:167], v[4:7], v[192:195]\n"
  "ds_read_b64_tr_b16 a[212:213], v249 offset:8192\n"
  "v_mfma_f32_16x16x32_bf16 v[192:195], a[160:163], v[0:3], v[192:195]\n"
  "s_waitcnt lgkmcnt(14)\n"
  "buffer_load_dwordx4 v252, s[44:47], s71 offen offset:2048 nt lds\n"
  "v_mfma_f32_16x16x32_bf16 a[16:19], v[152:155], a[192:195], a[16:19]\n"
  "s_waitcnt lgkmcnt(13)\n"
  "v_exp_f32 v188, v188\n"
  "v_mfma_f32_16x16x32_bf16 a[16:19], v[156:159], a[196:199], a[16:19]\n"
  "s_waitcnt lgkmcnt(11)\n"
  "v_exp_f32 v189, v189\n"
  "v_mfma_f32_16x16x32_bf16 a[16:19], v[80:83], a[200:203], a[16:19]\n"
  "s_waitcnt lgkmcnt(9)\n"
  "v_exp_f32 v190, v190\n"
  "v_mfma_f32_16x16x32_bf16 a[16:19], v[84:87], a[204:207], a[16:19]\n"
  "v_exp_f32 v191, v191\n"
  "v_mfma_f32_16x16x32_bf16 a[48:51], v[92:95], a[204:207], a[48:51]\n"
  "v_exp_f32 v192, v192\n"
  "v_mfma_f32_16x16x32_bf16 a[48:51], v[88:91], a[200:203], a[48:51]\n"
  "v_exp_f32 v193, v193\n"
  "v_mfma_f32_16x16x32_bf16 a[48:51], v[160:163], a[192:195], a[48:51]\n"
  "v_exp_f32 v194, v194\n"
  "v_mfma_f32_16x16x32_bf16 a[48:51], v[164:167], a[196:199], a[48:51]\n"
  "v_exp_f32 v195, v195\n"
  "v_mfma_f32_16x16x32_bf16 v[196:199], a[160:163], v[16:19], v[204:207]\n"
  "v_cvt_pk_bf16_f32 v148, v188, v189\n"
  "v_cvt_pk_bf16_f32 v149, v190, v191\n"
  "v_mfma_f32_16x16x32_bf16 v[196:199], a[164:167], v[20:23], v[196:199]\n"
  "v_add_f32 v220, v188, v220\n"
  "v_add_f32 v221, v189, v221\n"
  "v_mfma_f32_16x16x32_bf16 v[196:199], a[168:171], v[24:27], v[196:199]\n"
  "ds_read_b64_tr_b16 a[214:215], v249 offset:10240\n"
  "v_add_f32 v220, v190, v220\n"
  "v_mfma_f32_16x16x32_bf16 v[196:199], a[172:175], v[28:31], v[196:199]\n"
  "ds_read_b64_tr_b16 a[220:221], v249 offset:24576\n"
  "v_add_f32 v221, v191, v221\n"
  "v_mfma_f32_16x16x32_bf16 v[188:191], a[172:175], v[44:47], v[208:211]\n"
  "ds_read_b64_tr_b16 a[222:223], v249 offset:26624\n"
  "v_cvt_pk_bf16_f32 v116, v192, v193\n"
  "v_mfma_f32_16x16x32_bf16 v[188:191], a[168:171], v[40:43], v[188:191]\n"
  "v_cvt_pk_bf16_f32 v117, v194, v195\n"
  "v_mfma_f32_16x16x32_bf16 v[188:191], a[164:167], v[36:39], v[188:191]\n"
  "v_mfma_f32_16x16x32_bf16 v[188:191], a[160:163], v[32:35], v[188:191]\n"
  "buffer_load_dwordx4 v253, s[44:47], s71 offen offset:3072 nt lds\n"
  "v_mfma_f32_16x16x32_bf16 a[80:83], v[168:171], a[192:195], a[80:83]\n"
  "s_add_u32 s71, s71, s73\n"
  "v_exp_f32 v196, v196\n"
  "v_mfma_f32_16x16x32_bf16 a[80:83], v[172:175], a[196:199], a[80:83]\n"
  "s_and_b32 s71, s71, s8\n"
  "v_exp_f32 v197, v197\n"
  "v_mfma_f32_16x16x32_bf16 a[80:83], v[96:99], a[200:203], a[80:83]\n"
  "v_exp_f32 v198, v198\n"
  "v_mfma_f32_16x16x32_bf16 a[80:83], v[100:103], a[204:207], a[80:83]\n"
  "v_exp_f32 v199, v199\n"
  "v_mfma_f32_16x16x32_bf16 a[112:115], v[108:111], a[204:207], a[112:115]\n"
  "v_exp_f32 v188, v188\n"
  "v_mfma_f32_16x16x32_bf16 a[112:115], v[104:107], a[200:203], a[112:115]\n"
  "v_exp_f32 v189, v189\n"
  "v_mfma_f32_16x16x32_bf16 a[112:115], v[176:179], a[192:195], a[112:115]\n"
  "v_exp_f32 v190, v190\n"
  "v_mfma_f32_16x16x32_bf16 a[112:115], v[180:183], a[196:199], a[112:115]\n"
  "v_exp_f32 v191, v191\n"
  "v_smfmac_f32_16x16x64_bf16 a[224:227], a[240:243], v[80:87], v224\n"
  "v_cvt_pk_bf16_f32 v124, v196, v197\n"
  "v_mfma_f32_16x16x32_bf16 v[192:195], a[160:163], v[48:51], v[212:215]\n"
  "ds_read_b64_tr_b16 a[204:205], v250 offset:24576\n"
  "v_cvt_pk_bf16_f32 v125, v198, v199\n"
  "v_mfma_f32_16x16x32_bf16 v[192:195], a[164:167], v[52:55], v[192:195]\n"
  "ds_read_b64_tr_b16 a[206:207], v250 offset:26624\n"
  "v_cvt_pk_bf16_f32 v132, v188, v189\n"
  "v_mfma_f32_16x16x32_bf16 v[192:195], a[168:171], v[56:59], v[192:195]\n"
  "s_add_u32 m0, s16, 16384\n"
  "ds_read_b64_tr_b16 a[196:197], v250 offset:8192\n"
  "v_cvt_pk_bf16_f32 v133, v190, v191\n"
  "v_mfma_f32_16x16x32_bf16 v[192:195], a[172:175], v[60:63], v[192:195]\n"
  "s_waitcnt lgkmcnt(14)\n"
  "ds_read_b64_tr_b16 a[198:199], v250 offset:10240\n"
  "v_mfma_f32_16x16x32_bf16 v[196:199], a[188:191], v[60:63], v[212:215]\n"
  "s_waitcnt lgkmcnt(14)\n"
  "ds_read_b128 a[160:163], v232 offset:16384\n"
  "v_mfma_f32_16x16x32_bf16 v[196:199], a[184:187], v[56:59], v[196:199]\n"
  "s_waitcnt lgkmcnt(14)\n"
  "ds_read_b128 a[164:167], v233 offset:16384\n"
  "v_mfma_f32_16x16x32_bf16 v[196:199], a[180:183], v[52:55], v[196:199]\n"
  "s_waitcnt lgkmcnt(14)\n"
  "ds_read_b128 a[168:171], v234 offset:16384\n"
  "v_mfma_f32_16x16x32_bf16 v[196:199], a[176:179], v[48:51], v[196:199]\n"
  "buffer_load_dwordx4 v254, s[48:51], s72 offen nt lds\n"
  "v_mfma_f32_16x16x32_bf16 a[144:147], v[184:187], a[192:195], a[144:147]\n"
  "v_exp_f32 v192, v192\n"
  "v_mfma_f32_16x16x32_bf16 a[144:147], v[112:115], a[200:203], a[144:147]\n"
  "s_waitcnt lgkmcnt(13)\n"
  "v_exp_f32 v193, v193\n"
  "v_mfma_f32_16x16x32_bf16 a[148:151], v[112:115], a[216:219], a[148:151]\n"
  "s_waitcnt lgkmcnt(11)\n"
  "v_exp_f32 v194, v194\n"
  "v_mfma_f32_16x16x32_bf16 a[148:151], v[184:187], a[208:211], a[148:151]\n"
  "v_exp_f32 v195, v195\n"
  "v_mfma_f32_16x16x32_bf16 a[20:23], v[152:155], a[208:211], a[20:23]\n"
  "v_exp_f32 v196, v196\n"
  "v_mfma_f32_16x16x32_bf16 a[20:23], v[80:83], a[216:219], a[20:23]\n"
  "s_waitcnt lgkmcnt(9)\n"
  "v_exp_f32 v197, v197\n"
  "v_mfma_f32_16x16x32_bf16 a[20:23], v[156:159], a[212:215], a[20:23]\n"
  "s_waitcnt lgkmcnt(7)\n"
  "v_exp_f32 v198, v198\n"
  "v_mfma_f32_16x16x32_bf16 a[20:23], v[84:87], a[220:223], a[20:23]\n"
  "v_exp_f32 v199, v199\n"
  "v_mfma_f32_16x16x32_bf16 v[188:191], a[176:179], v[32:35], v[208:211]\n"
  "ds_read_b64_tr_b16 a[200:201], v246 offset:24576\n"
  "v_cvt_pk_bf16_f32 v140, v192, v193\n"
  "v_mfma_f32_16x16x32_bf16 v[188:191], a[180:183], v[36:39], v[188:191]\n"
  "ds_read_b64_tr_b16 a[202:203], v246 offset:26624\n"
  "v_cvt_pk_bf16_f32 v141, v194, v195\n"
  "v_mfma_f32_16x16x32_bf16 v[188:191], a[184:187], v[40:43], v[188:191]\n"
  "ds_read_b64_tr_b16 a[192:193], v246 offset:8192\n"
  "v_cvt_pk_bf16_f32 v142, v196, v197\n"
  "v_mfma_f32_16x16x32_bf16 v[188:191], a[188:191], v[44:47], v[188:191]\n"
  "ds_read_b64_tr_b16 a[194:195], v246 offset:10240\n"
  "v_cvt_pk_bf16_f32 v143, v198, v199\n"
  "v_mfma_f32_16x16x32_bf16 v[192:195], a[188:191], v[28:31], v[204:207]\n"
  "ds_read_b128 a[172:175], v235 offset:16384\n"
  "v_mfma_f32_16x16x32_bf16 v[192:195], a[184:187], v[24:27], v[192:195]\n"
  "v_mfma_f32_16x16x32_bf16 v[192:195], a[180:183], v[20:23], v[192:195]\n"
  "v_mfma_f32_16x16x32_bf16 v[192:195], a[176:179], v[16:19], v[192:195]\n"
  "buffer_load_dwordx4 v255, s[48:51], s72 offen offset:1024 nt lds\n"
  "v_mfma_f32_16x16x32_bf16 a[52:55], v[160:163], a[208:211], a[52:55]\n"
  "v_exp_f32 v188, v188\n"
  "v_mfma_f32_16x16x32_bf16 a[52:55], v[164:167], a[212:215], a[52:55]\n"
  "v_exp_f32 v189, v189\n"
  "v_mfma_f32_16x16x32_bf16 a[52:55], v[88:91], a[216:219], a[52:55]\n"
  "v_exp_f32 v190, v190\n"
  "v_mfma_f32_16x16x32_bf16 a[52:55], v[92:95], a[220:223], a[52:55]\n"
  "v_exp_f32 v191, v191\n"
  "v_mfma_f32_16x16x32_bf16 a[84:87], v[100:103], a[220:223], a[84:87]\n"
  "v_exp_f32 v192, v192\n"
  "v_mfma_f32_16x16x32_bf16 a[84:87], v[96:99], a[216:219], a[84:87]\n"
  "v_exp_f32 v193, v193\n"
  "v_mfma_f32_16x16x32_bf16 a[84:87], v[168:171], a[208:211], a[84:87]\n"
  "v_exp_f32 v194, v194\n"
  "v_mfma_f32_16x16x32_bf16 a[84:87], v[172:175], a[212:215], a[84:87]\n"
  "v_exp_f32 v195, v195\n"
  "v_smfmac_f32_16x16x64_bf16 a[228:231], a[240:243], v[88:95], v224\n"
  "v_cvt_pk_bf16_f32 v134, v188, v189\n"
  "v_mfma_f32_16x16x32_bf16 v[196:199], a[176:179], v[0:3], v[200:203]\n"
  "v_cvt_pk_bf16_f32 v135, v190, v191\n"
  "v_cvt_pk_bf16_f32 v126, v192, v193\n"
  "v_mfma_f32_16x16x32_bf16 v[196:199], a[180:183], v[4:7], v[196:199]\n"
  "v_cvt_pk_bf16_f32 v127, v194, v195\n"
  "v_mfma_f32_16x16x32_bf16 v[196:199], a[184:187], v[8:11], v[196:199]\n"
  "v_mfma_f32_16x16x32_bf16 v[196:199], a[188:191], v[12:15], v[196:199]\n"
  "v_mfma_f32_16x16x32_bf16 v[188:191], a[188:191], v[76:79], v[216:219]\n"
  "ds_read_b128 a[188:191], v235 offset:18432\n"
  "v_mfma_f32_16x16x32_bf16 v[188:191], a[184:187], v[72:75], v[188:191]\n"
  "ds_read_b128 a[184:187], v234 offset:18432\n"
  "v_mfma_f32_16x16x32_bf16 v[188:191], a[180:183], v[68:71], v[188:191]\n"
  "v_mfma_f32_16x16x32_bf16 v[188:191], a[176:179], v[64:67], v[188:191]\n"
  "buffer_load_dwordx4 v254, s[48:51], s72 offen offset:2048 nt lds\n"
  "v_mfma_f32_16x16x32_bf16 a[116:119], v[176:179], a[208:211], a[116:119]\n"
  "v_exp_f32 v196, v196\n"
  "v_mfma_f32_16x16x32_bf16 a[116:119], v[180:183], a[212:215], a[116:119]\n"
  "v_exp_f32 v197, v197\n"
  "v_mfma_f32_16x16x32_bf16 a[116:119], v[104:107], a[216:219], a[116:119]\n"
  "v_exp_f32 v198, v198\n"
  "v_mfma_f32_16x16x32_bf16 a[116:119], v[108:111], a[220:223], a[116:119]\n"
  "s_waitcnt lgkmcnt(12)\n"
  "v_exp_f32 v199, v199\n"
  "v_mfma_f32_16x16x32_bf16 a[120:123], v[108:111], a[204:207], a[120:123]\n"
  "s_waitcnt lgkmcnt(5)\n"
  "v_exp_f32 v188, v188\n"
  "v_mfma_f32_16x16x32_bf16 a[120:123], v[104:107], a[200:203], a[120:123]\n"
  "s_waitcnt lgkmcnt(3)\n"
  "v_exp_f32 v189, v189\n"
  "v_mfma_f32_16x16x32_bf16 a[120:123], v[176:179], a[192:195], a[120:123]\n"
  "v_exp_f32 v190, v190\n"
  "v_mfma_f32_16x16x32_bf16 a[120:123], v[180:183], a[196:199], a[120:123]\n"
  "v_exp_f32 v191, v191\n"
  "v_mfma_f32_16x16x32_bf16 v[192:195], a[160:163], v[0:3], v[200:203]\n"
  "v_cvt_pk_bf16_f32 v118, v196, v197\n"
  "v_cvt_pk_bf16_f32 v119, v198, v199\n"
  "v_mfma_f32_16x16x32_bf16 v[192:195], a[164:167], v[4:7], v[192:195]\n"
  "v_cvt_pk_bf16_f32 v150, v188, v189\n"
  "v_cvt_pk_bf16_f32 v151, v190, v191\n"
  "v_mfma_f32_16x16x32_bf16 v[192:195], a[168:171], v[8:11], v[192:195]\n"
  "s_waitcnt lgkmcnt(2)\n"
  "ds_read_b64_tr_b16 a[216:217], v247 offset:24576\n"
  "v_add_f32 v220, v188, v220\n"
  "v_mfma_f32_16x16x32_bf16 v[192:195], a[172:175], v[12:15], v[192:195]\n"
  "ds_read_b64_tr_b16 a[218:219], v247 offset:26624\n"
  "v_add_f32 v221, v189, v221\n"
  "v_mfma_f32_16x16x32_bf16 v[196:199], a[172:175], v[28:31], v[204:207]\n"
  "ds_read_b64_tr_b16 a[208:209], v247 offset:8192\n"
  "v_add_f32 v220, v190, v220\n"
  "v_mfma_f32_16x16x32_bf16 v[196:199], a[168:171], v[24:27], v[196:199]\n"
  "ds_read_b64_tr_b16 a[210:211], v247 offset:10240\n"
  "v_add_f32 v221, v191, v221\n"
  "v_mfma_f32_16x16x32_bf16 v[196:199], a[164:167], v[20:23], v[196:199]\n"
  "ds_read_b128 a[176:179], v232 offset:18432\n"
  "v_mfma_f32_16x16x32_bf16 v[196:199], a[160:163], v[16:19], v[196:199]\n"
  "buffer_load_dwordx4 v255, s[48:51], s72 offen offset:3072 nt lds\n"
  "v_mfma_f32_16x16x32_bf16 a[88:91], v[168:171], a[192:195], a[88:91]\n"
  "s_add_u32 s72, s72, s74\n"
  "v_exp_f32 v192, v192\n"
  "v_mfma_f32_16x16x32_bf16 a[88:91], v[172:175], a[196:199], a[88:91]\n"
  "s_and_b32 s72, s72, s9\n"
  "v_exp_f32 v193, v193\n"
  "v_mfma_f32_16x16x32_bf16 a[88:91], v[96:99], a[200:203], a[88:91]\n"
  "v_exp_f32 v194, v194\n"
  "v_mfma_f32_16x16x32_bf16 a[88:91], v[100:103], a[204:207], a[88:91]\n"
  "v_exp_f32 v195, v195\n"
  "v_mfma_f32_16x16x32_bf16 a[56:59], v[92:95], a[204:207], a[56:59]\n"
  "v_exp_f32 v196, v196\n"
  "v_mfma_f32_16x16x32_bf16 a[56:59], v[88:91], a[200:203], a[56:59]\n"
  "v_exp_f32 v197, v197\n"
  "v_mfma_f32_16x16x32_bf16 a[56:59], v[160:163], a[192:195], a[56:59]\n"
  "v_exp_f32 v198, v198\n"
  "v_mfma_f32_16x16x32_bf16 a[56:59], v[164:167], a[196:199], a[56:59]\n"
  "v_exp_f32 v199, v199\n"
  "v_smfmac_f32_16x16x64_bf16 a[232:235], a[240:243], v[96:103], v224\n"
  "v_cvt_pk_bf16_f32 v120, v192, v193\n"
  "v_mfma_f32_16x16x32_bf16 v[188:191], a[160:163], v[32:35], v[208:211]\n"
  "ds_read_b128 a[180:183], v233 offset:18432\n"
  "v_cvt_pk_bf16_f32 v121, v194, v195\n"
  "v_mfma_f32_16x16x32_bf16 v[188:191], a[164:167], v[36:39], v[188:191]\n"
  "ds_read_b64_tr_b16 a[212:213], v251 offset:8192\n"
  "v_cvt_pk_bf16_f32 v128, v196, v197\n"
  "v_mfma_f32_16x16x32_bf16 v[188:191], a[168:171], v[40:43], v[188:191]\n"
  "ds_read_b64_tr_b16 a[214:215], v251 offset:10240\n"
  "v_cvt_pk_bf16_f32 v129, v198, v199\n"
  "v_mfma_f32_16x16x32_bf16 v[188:191], a[172:175], v[44:47], v[188:191]\n"
  "ds_read_b64_tr_b16 a[220:221], v251 offset:24576\n"
  "v_mfma_f32_16x16x32_bf16 v[192:195], a[172:175], v[60:63], v[212:215]\n"
  "ds_read_b64_tr_b16 a[222:223], v251 offset:26624\n"
  "v_mfma_f32_16x16x32_bf16 v[192:195], a[168:171], v[56:59], v[192:195]\n"
  "s_add_u32 m0, s83, s15\n"
  "s_add_u32 s83, s83, 0x3400\n"
  "s_and_b32 s83, s83, 0xcc00\n"
  "v_mfma_f32_16x16x32_bf16 v[192:195], a[164:167], v[52:55], v[192:195]\n"
  "buffer_load_dwordx4 v225, s[60:63], s82 offen lds\n"
  "v_mfma_f32_16x16x32_bf16 v[192:195], a[160:163], v[48:51], v[192:195]\n"
  "s_add_u32 s82, s82, 0xf400\n"
  "v_xor_b32 v225, 32, v225\n"
  "v_mfma_f32_16x16x32_bf16 a[24:27], v[156:159], a[196:199], a[24:27]\n"
  "s_and_b32 s82, s82, 0x30c00\n"
  "v_exp_f32 v188, v188\n"
  "v_mfma_f32_16x16x32_bf16 a[24:27], v[84:87], a[204:207], a[24:27]\n"
  "v_exp_f32 v189, v189\n"
  "v_mfma_f32_16x16x32_bf16 a[24:27], v[152:155], a[192:195], a[24:27]\n"
  "v_exp_f32 v190, v190\n"
  "v_mfma_f32_16x16x32_bf16 a[24:27], v[80:83], a[200:203], a[24:27]\n"
  "v_exp_f32 v191, v191\n"
  "v_mfma_f32_16x16x32_bf16 a[152:155], v[184:187], a[192:195], a[152:155]\n"
  "v_exp_f32 v192, v192\n"
  "v_mfma_f32_16x16x32_bf16 a[152:155], v[112:115], a[200:203], a[152:155]\n"
  "s_waitcnt lgkmcnt(8)\n"
  "v_exp_f32 v193, v193\n"
  "v_mfma_f32_16x16x32_bf16 a[156:159], v[112:115], a[216:219], a[156:159]\n"
  "s_waitcnt lgkmcnt(6)\n"
  "v_exp_f32 v194, v194\n"
  "v_mfma_f32_16x16x32_bf16 a[156:159], v[184:187], a[208:211], a[156:159]\n"
  "v_exp_f32 v195, v195\n"
  "v_smfmac_f32_16x16x64_bf16 a[236:239], a[240:243], v[104:111], v224\n"
  "s_waitcnt vmcnt(9) lgkmcnt(0)\n"
  "s_barrier\n"
  "v_mfma_f32_16x16x32_bf16 v[196:199], a[176:179], v[0:3], v[200:203]\n"
  "ds_read_b128 a[160:163], v228 offset:0\n"
  "v_cvt_pk_bf16_f32 v136, v188, v189\n"
  "v_mfma_f32_16x16x32_bf16 v[196:199], a[180:183], v[4:7], v[196:199]\n"
  "ds_read_b128 a[164:167], v229 offset:0\n"
  "v_cvt_pk_bf16_f32 v137, v190, v191\n"
  "v_mfma_f32_16x16x32_bf16 v[196:199], a[184:187], v[8:11], v[196:199]\n"
  "ds_read_b128 a[168:171], v230 offset:0\n"
  "v_cvt_pk_bf16_f32 v144, v192, v193\n"
  "v_mfma_f32_16x16x32_bf16 v[196:199], a[188:191], v[12:15], v[196:199]\n"
  "ds_read_b128 a[172:175], v231 offset:0\n"
  "v_cvt_pk_bf16_f32 v145, v194, v195\n"
  "v_mfma_f32_16x16x32_bf16 v[188:191], a[188:191], v[28:31], v[204:207]\n"
  "ds_read_b64_tr_b16 a[192:193], v244 offset:16384\n"
  "v_mfma_f32_16x16x32_bf16 v[188:191], a[184:187], v[24:27], v[188:191]\n"
  "ds_read_b64_tr_b16 a[194:195], v244 offset:18432\n"
  "v_mfma_f32_16x16x32_bf16 v[188:191], a[180:183], v[20:23], v[188:191]\n"
  "s_add_u32 m0, s67, 131072\n"
  "ds_read_b64_tr_b16 a[196:197], v248 offset:16384\n"
  "v_mfma_f32_16x16x32_bf16 v[188:191], a[176:179], v[16:19], v[188:191]\n"
  "buffer_load_dwordx4 v252, s[44:47], s71 offen nt lds\n"
  "v_mfma_f32_16x16x32_bf16 a[28:31], v[152:155], a[208:211], a[28:31]\n"
  "v_exp_f32 v196, v196\n"
  "v_mfma_f32_16x16x32_bf16 a[28:31], v[156:159], a[212:215], a[28:31]\n"
  "v_exp_f32 v197, v197\n"
  "v_mfma_f32_16x16x32_bf16 a[28:31], v[80:83], a[216:219], a[28:31]\n"
  "v_exp_f32 v198, v198\n"
  "v_mfma_f32_16x16x32_bf16 a[28:31], v[84:87], a[220:223], a[28:31]\n"
  "v_exp_f32 v199, v199\n"
  "v_mfma_f32_16x16x32_bf16 a[60:63], v[92:95], a[220:223], a[60:63]\n"
  "v_exp_f32 v188, v188\n"
  "v_mfma_f32_16x16x32_bf16 a[60:63], v[88:91], a[216:219], a[60:63]\n"
  "v_exp_f32 v189, v189\n"
  "v_mfma_f32_16x16x32_bf16 a[60:63], v[160:163], a[208:211], a[60:63]\n"
  "v_exp_f32 v190, v190\n"
  "v_mfma_f32_16x16x32_bf16 a[60:63], v[164:167], a[212:215], a[60:63]\n"
  "v_exp_f32 v191, v191\n"
  "v_mfma_f32_16x16x32_bf16 v[192:195], a[176:179], v[32:35], v[208:211]\n"
  "ds_read_b64_tr_b16 a[198:199], v248 offset:18432\n"
  "v_cvt_pk_bf16_f32 v122, v196, v197\n"
  "v_mfma_f32_16x16x32_bf16 v[192:195], a[180:183], v[36:39], v[192:195]\n"
  "ds_read_b64_tr_b16 a[200:201], v236 offset:0\n"
  "v_cvt_pk_bf16_f32 v123, v198, v199\n"
  "v_mfma_f32_16x16x32_bf16 v[192:195], a[184:187], v[40:43], v[192:195]\n"
  "ds_read_b64_tr_b16 a[202:203], v236 offset:2048\n"
  "v_cvt_pk_bf16_f32 v130, v188, v189\n"
  "v_mfma_f32_16x16x32_bf16 v[192:195], a[188:191], v[44:47], v[192:195]\n"
  "ds_read_b64_tr_b16 a[204:205], v240 offset:0\n"
  "v_cvt_pk_bf16_f32 v131, v190, v191\n"
  "v_mfma_f32_16x16x32_bf16 v[196:199], a[188:191], v[60:63], v[212:215]\n"
  "ds_read_b64_tr_b16 a[206:207], v240 offset:2048\n"
  "v_mfma_f32_16x16x32_bf16 v[196:199], a[184:187], v[56:59], v[196:199]\n"
  "ds_read_b128 a[188:191], v231 offset:2048\n"
  "v_mfma_f32_16x16x32_bf16 v[196:199], a[180:183], v[52:55], v[196:199]\n"
  "ds_read_b128 a[184:187], v230 offset:2048\n"
  "v_mfma_f32_16x16x32_bf16 v[196:199], a[176:179], v[48:51], v[196:199]\n"
  "buffer_load_dwordx4 v253, s[44:47], s71 offen offset:1024 nt lds\n"
  "v_mfma_f32_16x16x32_bf16 a[92:95], v[168:171], a[208:211], a[92:95]\n"
  "v_exp_f32 v192, v192\n"
  "v_mfma_f32_16x16x32_bf16 a[92:95], v[172:175], a[212:215], a[92:95]\n"
  "v_exp_f32 v193, v193\n"
  "v_mfma_f32_16x16x32_bf16 a[92:95], v[96:99], a[216:219], a[92:95]\n"
  "v_exp_f32 v194, v194\n"
  "v_mfma_f32_16x16x32_bf16 a[92:95], v[100:103], a[220:223], a[92:95]\n"
  "v_exp_f32 v195, v195\n"
  "v_mfma_f32_16x16x32_bf16 a[124:127], v[108:111], a[220:223], a[124:127]\n"
  "v_exp_f32 v196, v196\n"
  "v_mfma_f32_16x16x32_bf16 a[124:127], v[104:107], a[216:219], a[124:127]\n"
  "v_exp_f32 v197, v197\n"
  "v_mfma_f32_16x16x32_bf16 a[124:127], v[176:179], a[208:211], a[124:127]\n"
  "v_exp_f32 v198, v198\n"
  "v_mfma_f32_16x16x32_bf16 a[124:127], v[180:183], a[212:215], a[124:127]\n"
  "v_exp_f32 v199, v199\n"
  "s_waitcnt lgkmcnt(13)\n"
  "v_mfma_f32_16x16x32_bf16 v[188:191], a[160:163], v[64:67], v[216:219]\n"
  "s_waitcnt lgkmcnt(12)\n"
  "ds_read_b128 a[180:183], v229 offset:2048\n"
  "v_cvt_pk_bf16_f32 v138, v192, v193\n"
  "v_mfma_f32_16x16x32_bf16 v[188:191], a[164:167], v[68:71], v[188:191]\n"
  "s_waitcnt lgkmcnt(12)\n"
  "ds_read_b128 a[176:179], v228 offset:2048\n"
  "v_cvt_pk_bf16_f32 v139, v194, v195\n"
  "v_mfma_f32_16x16x32_bf16 v[188:191], a[168:171], v[72:75], v[188:191]\n"
  "s_waitcnt lgkmcnt(12)\n"
  "ds_read_b64_tr_b16 a[216:217], v237 offset:0\n"
  "v_cvt_pk_bf16_f32 v146, v196, v197\n"
  "v_mfma_f32_16x16x32_bf16 v[188:191], a[172:175], v[76:79], v[188:191]\n"
  "ds_read_b64_tr_b16 a[218:219], v237 offset:2048\n"
  "v_cvt_pk_bf16_f32 v147, v198, v199\n"
  "v_mfma_f32_16x16x32_bf16 v[192:195], a[172:175], v[12:15], v[200:203]\n"
  "ds_read_b64_tr_b16 a[208:209], v245 offset:16384\n"
  "v_mfma_f32_16x16x32_bf16 v[192:195], a[168:171], v[8:11], v[192:195]\n"
  "ds_read_b64_tr_b16 a[210:211], v245 offset:18432\n"
  "v_mfma_f32_16x16x32_bf16 v[192:195], a[164:167], v[4:7], v[192:195]\n"
  "ds_read_b64_tr_b16 a[212:213], v249 offset:16384\n"
  "v_mfma_f32_16x16x32_bf16 v[192:195], a[160:163], v[0:3], v[192:195]\n"
  "s_waitcnt lgkmcnt(14)\n"
  "buffer_load_dwordx4 v252, s[44:47], s71 offen offset:2048 nt lds\n"
  "v_mfma_f32_16x16x32_bf16 a[0:3], v[80:83], a[192:195], a[0:3]\n"
  "s_waitcnt lgkmcnt(13)\n"
  "v_exp_f32 v188, v188\n"
  "v_mfma_f32_16x16x32_bf16 a[0:3], v[84:87], a[196:199], a[0:3]\n"
  "s_waitcnt lgkmcnt(11)\n"
  "v_exp_f32 v189, v189\n"
  "v_mfma_f32_16x16x32_bf16 a[0:3], v[116:119], a[200:203], a[0:3]\n"
  "s_waitcnt lgkmcnt(9)\n"
  "v_exp_f32 v190, v190\n"
  "v_mfma_f32_16x16x32_bf16 a[0:3], v[120:123], a[204:207], a[0:3]\n"
  "v_exp_f32 v191, v191\n"
  "v_mfma_f32_16x16x32_bf16 a[32:35], v[128:131], a[204:207], a[32:35]\n"
  "v_exp_f32 v192, v192\n"
  "v_mfma_f32_16x16x32_bf16 a[32:35], v[124:127], a[200:203], a[32:35]\n"
  "v_exp_f32 v193, v193\n"
  "v_mfma_f32_16x16x32_bf16 a[32:35], v[88:91], a[192:195], a[32:35]\n"
  "v_exp_f32 v194, v194\n"
  "v_mfma_f32_16x16x32_bf16 a[32:35], v[92:95], a[196:199], a[32:35]\n"
  "v_exp_f32 v195, v195\n"
  "v_mfma_f32_16x16x32_bf16 v[196:199], a[160:163], v[16:19], v[204:207]\n"
  "v_cvt_pk_bf16_f32 v184, v188, v189\n"
  "v_cvt_pk_bf16_f32 v185, v190, v191\n"
  "v_mfma_f32_16x16x32_bf16 v[196:199], a[164:167], v[20:23], v[196:199]\n"
  "v_add_f32 v220, v188, v220\n"
  "v_add_f32 v221, v189, v221\n"
  "v_mfma_f32_16x16x32_bf16 v[196:199], a[168:171], v[24:27], v[196:199]\n"
  "ds_read_b64_tr_b16 a[214:215], v249 offset:18432\n"
  "v_add_f32 v220, v190, v220\n"
  "v_mfma_f32_16x16x32_bf16 v[196:199], a[172:175], v[28:31], v[196:199]\n"
  "ds_read_b64_tr_b16 a[220:221], v241 offset:0\n"
  "v_add_f32 v221, v191, v221\n"
  "v_mfma_f32_16x16x32_bf16 v[188:191], a[172:175], v[44:47], v[208:211]\n"
  "ds_read_b64_tr_b16 a[222:223], v241 offset:2048\n"
  "v_cvt_pk_bf16_f32 v152, v192, v193\n"
  "v_mfma_f32_16x16x32_bf16 v[188:191], a[168:171], v[40:43], v[188:191]\n"
  "v_cvt_pk_bf16_f32 v153, v194, v195\n"
  "v_mfma_f32_16x16x32_bf16 v[188:191], a[164:167], v[36:39], v[188:191]\n"
  "v_mfma_f32_16x16x32_bf16 v[188:191], a[160:163], v[32:35], v[188:191]\n"
  "buffer_load_dwordx4 v253, s[44:47], s71 offen offset:3072 nt lds\n"
  "v_mfma_f32_16x16x32_bf16 a[64:67], v[96:99], a[192:195], a[64:67]\n"
  "s_add_u32 s71, s71, s73\n"
  "v_exp_f32 v196, v196\n"
  "v_mfma_f32_16x16x32_bf16 a[64:67], v[100:103], a[196:199], a[64:67]\n"
  "s_and_b32 s71, s71, s8\n"
  "v_exp_f32 v197, v197\n"
  "v_mfma_f32_16x16x32_bf16 a[64:67], v[132:135], a[200:203], a[64:67]\n"
  "v_exp_f32 v198, v198\n"
  "v_mfma_f32_16x16x32_bf16 a[64:67], v[136:139], a[204:207], a[64:67]\n"
  "v_exp_f32 v199, v199\n"
  "v_mfma_f32_16x16x32_bf16 a[96:99], v[144:147], a[204:207], a[96:99]\n"
  "v_exp_f32 v188, v188\n"
  "v_mfma_f32_16x16x32_bf16 a[96:99], v[140:143], a[200:203], a[96:99]\n"
  "v_exp_f32 v189, v189\n"
  "v_mfma_f32_16x16x32_bf16 a[96:99], v[104:107], a[192:195], a[96:99]\n"
  "v_exp_f32 v190, v190\n"
  "v_mfma_f32_16x16x32_bf16 a[96:99], v[108:111], a[196:199], a[96:99]\n"
  "v_exp_f32 v191, v191\n"
  "v_smfmac_f32_16x16x64_bf16 a[224:227], a[240:243], v[116:123], v224\n"
  "v_cvt_pk_bf16_f32 v160, v196, v197\n"
  "v_mfma_f32_16x16x32_bf16 v[192:195], a[160:163], v[48:51], v[212:215]\n"
  "ds_read_b64_tr_b16 a[204:205], v242 offset:0\n"
  "v_cvt_pk_bf16_f32 v161, v198, v199\n"
  "v_mfma_f32_16x16x32_bf16 v[192:195], a[164:167], v[52:55], v[192:195]\n"
  "ds_read_b64_tr_b16 a[206:207], v242 offset:2048\n"
  "v_cvt_pk_bf16_f32 v168, v188, v189\n"
  "v_mfma_f32_16x16x32_bf16 v[192:195], a[168:171], v[56:59], v[192:195]\n"
  "s_add_u32 m0, s17, 0\n"
  "ds_read_b64_tr_b16 a[196:197], v250 offset:16384\n"
  "v_cvt_pk_bf16_f32 v169, v190, v191\n"
  "v_mfma_f32_16x16x32_bf16 v[192:195], a[172:175], v[60:63], v[192:195]\n"
  "s_waitcnt lgkmcnt(14)\n"
  "ds_read_b64_tr_b16 a[198:199], v250 offset:18432\n"
  "v_mfma_f32_16x16x32_bf16 v[196:199], a[188:191], v[60:63], v[212:215]\n"
  "s_waitcnt lgkmcnt(14)\n"
  "ds_read_b128 a[160:163], v232 offset:0\n"
  "v_mfma_f32_16x16x32_bf16 v[196:199], a[184:187], v[56:59], v[196:199]\n"
  "s_waitcnt lgkmcnt(14)\n"
  "ds_read_b128 a[164:167], v233 offset:0\n"
  "v_mfma_f32_16x16x32_bf16 v[196:199], a[180:183], v[52:55], v[196:199]\n"
  "s_waitcnt lgkmcnt(14)\n"
  "ds_read_b128 a[168:171], v234 offset:0\n"
  "v_mfma_f32_16x16x32_bf16 v[196:199], a[176:179], v[48:51], v[196:199]\n"
  "buffer_load_dwordx4 v254, s[48:51], s72 offen nt lds\n"
  "v_mfma_f32_16x16x32_bf16 a[128:131], v[112:115], a[192:195], a[128:131]\n"
  "v_exp_f32 v192, v192\n"
  "v_mfma_f32_16x16x32_bf16 a[128:131], v[148:151], a[200:203], a[128:131]\n"
  "s_waitcnt lgkmcnt(13)\n"
  "v_exp_f32 v193, v193\n"
  "v_mfma_f32_16x16x32_bf16 a[132:135], v[148:151], a[216:219], a[132:135]\n"
  "s_waitcnt lgkmcnt(11)\n"
  "v_exp_f32 v194, v194\n"
  "v_mfma_f32_16x16x32_bf16 a[132:135], v[112:115], a[208:211], a[132:135]\n"
  "v_exp_f32 v195, v195\n"
  "v_mfma_f32_16x16x32_bf16 a[4:7], v[80:83], a[208:211], a[4:7]\n"
  "v_exp_f32 v196, v196\n"
  "v_mfma_f32_16x16x32_bf16 a[4:7], v[116:119], a[216:219], a[4:7]\n"
  "s_waitcnt lgkmcnt(9)\n"
  "v_exp_f32 v197, v197\n"
  "v_mfma_f32_16x16x32_bf16 a[4:7], v[84:87], a[212:215], a[4:7]\n"
  "s_waitcnt lgkmcnt(7)\n"
  "v_exp_f32 v198, v198\n"
  "v_mfma_f32_16x16x32_bf16 a[4:7], v[120:123], a[220:223], a[4:7]\n"
  "v_exp_f32 v199, v199\n"
  "v_mfma_f32_16x16x32_bf16 v[188:191], a[176:179], v[32:35], v[208:211]\n"
  "ds_read_b64_tr_b16 a[200:201], v238 offset:0\n"
  "v_cvt_pk_bf16_f32 v176, v192, v193\n"
  "v_mfma_f32_16x16x32_bf16 v[188:191], a[180:183], v[36:39], v[188:191]\n"
  "ds_read_b64_tr_b16 a[202:203], v238 offset:2048\n"
  "v_cvt_pk_bf16_f32 v177, v194, v195\n"
  "v_mfma_f32_16x16x32_bf16 v[188:191], a[184:187], v[40:43], v[188:191]\n"
  "ds_read_b64_tr_b16 a[192:193], v246 offset:16384\n"
  "v_cvt_pk_bf16_f32 v178, v196, v197\n"
  "v_mfma_f32_16x16x32_bf16 v[188:191], a[188:191], v[44:47], v[188:191]\n"
  "ds_read_b64_tr_b16 a[194:195], v246 offset:18432\n"
  "v_cvt_pk_bf16_f32 v179, v198, v199\n"
  "v_mfma_f32_16x16x32_bf16 v[192:195], a[188:191], v[28:31], v[204:207]\n"
  "ds_read_b128 a[172:175], v235 offset:0\n"
  "v_mfma_f32_16x16x32_bf16 v[192:195], a[184:187], v[24:27], v[192:195]\n"
  "v_mfma_f32_16x16x32_bf16 v[192:195], a[180:183], v[20:23], v[192:195]\n"
  "s_cmp_eq_u32 s75, 4\n"
  "s_cbranch_scc1 L_side1_%=\n"
  "L_back1_%=:\n"
  "v_mfma_f32_16x16x32_bf16 v[192:195], a[176:179], v[16:19], v[192:195]\n"
  "buffer_load_dwordx4 v255, s[48:51], s72 offen offset:1024 nt lds\n"
  "v_mfma_f32_16x16x32_bf16 a[36:39], v[88:91], a[208:211], a[36:39]\n"
  "v_exp_f32 v188, v188\n"
  "v_mfma_f32_16x16x32_bf16 a[36:39], v[92:95], a[212:215], a[36:39]\n"
  "v_exp_f32 v189, v189\n"
  "v_mfma_f32_16x16x32_bf16 a[36:39], v[124:127], a[216:219], a[36:39]\n"
  "v_exp_f32 v190, v190\n"
  "v_mfma_f32_16x16x32_bf16 a[36:39], v[128:131], a[220:223], a[36:39]\n"
  "v_exp_f32 v191, v191\n"
  "v_mfma_f32_16x16x32_bf16 a[68:71], v[136:139], a[220:223], a[68:71]\n"
  "v_exp_f32 v192, v192\n"
  "v_mfma_f32_16x16x32_bf16 a[68:71], v[132:135], a[216:219], a[68:71]\n"
  "v_exp_f32 v193, v193\n"
  "v_mfma_f32_16x16x32_bf16 a[68:71], v[96:99], a[208:211], a[68:71]\n"
  "v_exp_f32 v194, v194\n"
  "v_mfma_f32_16x16x32_bf16 a[68:71], v[100:103], a[212:215], a[68:71]\n"
  "v_exp_f32 v195, v195\n"
  "v_smfmac_f32_16x16x64_bf16 a[228:231], a[240:243], v[124:131], v224\n"
  "v_cvt_pk_bf16_f32 v170, v188, v189\n"
  "v_mfma_f32_16x16x32_bf16 v[196:199], a[176:179], v[0:3], v[200:203]\n"
  "v_cvt_pk_bf16_f32 v171, v190, v191\n"
  "v_cvt_pk_bf16_f32 v162, v192, v193\n"
  "v_mfma_f32_16x16x32_bf16 v[196:199], a[180:183], v[4:7], v[196:199]\n"
  "v_cvt_pk_bf16_f32 v163, v194, v195\n"
  "v_mfma_f32_16x16x32_bf16 v[196:199], a[184:187], v[8:11], v[196:199]\n"
  "v_mfma_f32_16x16x32_bf16 v[196:199], a[188:191], v[12:15], v[196:199]\n"
  "v_mfma_f32_16x16x32_bf16 v[188:191], a[188:191], v[76:79], v[216:219]\n"
  "ds_read_b128 a[188:191], v235 offset:2048\n"
  "v_mfma_f32_16x16x32_bf16 v[188:191], a[184:187], v[72:75], v[188:191]\n"
  "ds_read_b128 a[184:187], v234 offset:2048\n"
  "v_mfma_f32_16x16x32_bf16 v[188:191], a[180:183], v[68:71], v[188:191]\n"
  "v_mfma_f32_16x16x32_bf16 v[188:191], a[176:179], v[64:67], v[188:191]\n"
  "buffer_load_dwordx4 v254, s[48:51], s72 offen offset:2048 nt lds\n"
  "v_mfma_f32_16x16x32_bf16 a[100:103], v[104:107], a[208:211], a[100:103]\n"
  "v_exp_f32 v196, v196\n"
  "v_mfma_f32_16x16x32_bf16 a[100:103], v[108:111], a[212:215], a[100:103]\n"
  "v_exp_f32 v197, v197\n"
  "v_mfma_f32_16x16x32_bf16 a[100:103], v[140:143], a[216:219], a[100:103]\n"
  "v_exp_f32 v198, v198\n"
  "v_mfma_f32_16x16x32_bf16 a[100:103], v[144:147], a[220:223], a[100:103]\n"
  "s_waitcnt lgkmcnt(12)\n"
  "v_exp_f32 v199, v199\n"
  "v_mfma_f32_16x16x32_bf16 a[104:107], v[144:147], a[204:207], a[104:107]\n"
  "s_waitcnt lgkmcnt(5)\n"
  "v_exp_f32 v188, v188\n"
  "v_mfma_f32_16x16x32_bf16 a[104:107], v[140:143], a[200:203], a[104:107]\n"
  "s_waitcnt lgkmcnt(3)\n"
  "v_exp_f32 v189, v189\n"
  "v_mfma_f32_16x16x32_bf16 a[104:107], v[104:107], a[192:195], a[104:107]\n"
  "v_exp_f32 v190, v190\n"
  "v_mfma_f32_16x16x32_bf16 a[104:107], v[108:111], a[196:199], a[104:107]\n"
  "v_exp_f32 v191, v191\n"
  "v_mfma_f32_16x16x32_bf16 v[192:195], a[160:163], v[0:3], v[200:203]\n"
  "v_cvt_pk_bf16_f32 v154, v196, v197\n"
  "v_cvt_pk_bf16_f32 v155, v198, v199\n"
  "v_mfma_f32_16x16x32_bf16 v[192:195], a[164:167], v[4:7], v[192:195]\n"
  "v_cvt_pk_bf16_f32 v186, v188, v189\n"
  "v_cvt_pk_bf16_f32 v187, v190, v191\n"
  "v_mfma_f32_16x16x32_bf16 v[192:195], a[168:171], v[8:11], v[192:195]\n"
  "s_waitcnt lgkmcnt(2)\n"
  "ds_read_b64_tr_b16 a[216:217], v239 offset:0\n"
  "v_add_f32 v220, v188, v220\n"
  "v_mfma_f32_16x16x32_bf16 v[192:195], a[172:175], v[12:15], v[192:195]\n"
  "ds_read_b64_tr_b16 a[218:219], v239 offset:2048\n"
  "v_add_f32 v221, v189, v221\n"
  "v_mfma_f32_16x16x32_bf16 v[196:199], a[172:175], v[28:31], v[204:207]\n"
  "ds_read_b64_tr_b16 a[208:209], v247 offset:16384\n"
  "v_add_f32 v220, v190, v220\n"
  "v_mfma_f32_16x16x32_bf16 v[196:199], a[168:171], v[24:27], v[196:199]\n"
  "ds_read_b64_tr_b16 a[210:211], v247 offset:18432\n"
  "v_add_f32 v221, v191, v221\n"
  "v_mfma_f32_16x16x32_bf16 v[196:199], a[164:167], v[20:23], v[196:199]\n"
  "ds_read_b128 a[176:179], v232 offset:2048\n"
  "v_mfma_f32_16x16x32_bf16 v[196:199], a[160:163], v[16:19], v[196:199]\n"
  "buffer_load_dwordx4 v255, s[48:51], s72 offen offset:3072 nt lds\n"
  "v_mfma_f32_16x16x32_bf16 a[72:75], v[96:99], a[192:195], a[72:75]\n"
  "s_add_u32 s72, s72, s74\n"
  "v_exp_f32 v192, v192\n"
  "v_mfma_f32_16x16x32_bf16 a[72:75], v[100:103], a[196:199], a[72:75]\n"
  "s_and_b32 s72, s72, s9\n"
  "v_exp_f32 v193, v193\n"
  "v_mfma_f32_16x16x32_bf16 a[72:75], v[132:135], a[200:203], a[72:75]\n"
  "v_exp_f32 v194, v194\n"
  "v_mfma_f32_16x16x32_bf16 a[72:75], v[136:139], a[204:207], a[72:75]\n"
  "v_exp_f32 v195, v195\n"
  "v_mfma_f32_16x16x32_bf16 a[40:43], v[128:131], a[204:207], a[40:43]\n"
  "v_exp_f32 v196, v196\n"
  "v_mfma_f32_16x16x32_bf16 a[40:43], v[124:127], a[200:203], a[40:43]\n"
  "v_exp_f32 v197, v197\n"
  "v_mfma_f32_16x16x32_bf16 a[40:43], v[88:91], a[192:195], a[40:43]\n"
  "v_exp_f32 v198, v198\n"
  "v_mfma_f32_16x16x32_bf16 a[40:43], v[92:95], a[196:199], a[40:43]\n"
  "v_exp_f32 v199, v199\n"
  "v_smfmac_f32_16x16x64_bf16 a[232:235], a[240:243], v[132:139], v224\n"
  "v_cvt_pk_bf16_f32 v156, v192, v193\n"
  "v_mfma_f32_16x16x32_bf16 v[188:191], a[160:163], v[32:35], v[208:211]\n"
  "ds_read_b128 a[180:183], v233 offset:2048\n"
  "v_cvt_pk_bf16_f32 v157, v194, v195\n"
  "v_mfma_f32_16x16x32_bf16 v[188:191], a[164:167], v[36:39], v[188:191]\n"
  "ds_read_b64_tr_b16 a[212:213], v251 offset:16384\n"
  "v_cvt_pk_bf16_f32 v164, v196, v197\n"
  "v_mfma_f32_16x16x32_bf16 v[188:191], a[168:171], v[40:43], v[188:191]\n"
  "ds_read_b64_tr_b16 a[214:215], v251 offset:18432\n"
  "v_cvt_pk_bf16_f32 v165, v198, v199\n"
  "v_mfma_f32_16x16x32_bf16 v[188:191], a[172:175], v[44:47], v[188:191]\n"
  "ds_read_b64_tr_b16 a[220:221], v243 offset:0\n"
  "v_mfma_f32_16x16x32_bf16 v[192:195], a[172:175], v[60:63], v[212:215]\n"
  "ds_read_b64_tr_b16 a[222:223], v243 offset:2048\n"
  "v_mfma_f32_16x16x32_bf16 v[192:195], a[168:171], v[56:59], v[192:195]\n"
  "v_mfma_f32_16x16x32_bf16 v[192:195], a[164:167], v[52:55], v[192:195]\n"
  "v_mfma_f32_16x16x32_bf16 v[192:195], a[160:163], v[48:51], v[192:195]\n"
  "v_mfma_f32_16x16x32_bf16 a[8:11], v[84:87], a[196:199], a[8:11]\n"
  "s_nop 0\n"
  "v_exp_f32 v188, v188\n"
  "v_mfma_f32_16x16x32_bf16 a[8:11], v[120:123], a[204:207], a[8:11]\n"
  "v_exp_f32 v189, v189\n"
  "v_mfma_f32_16x16x32_bf16 a[8:11], v[80:83], a[192:195], a[8:11]\n"
  "v_exp_f32 v190, v190\n"
  "v_mfma_f32_16x16x32_bf16 a[8:11], v[116:119], a[200:203], a[8:11]\n"
  "v_exp_f32 v191, v191\n"
  "v_mfma_f32_16x16x32_bf16 a[136:139], v[112:115], a[192:195], a[136:139]\n"
  "v_exp_f32 v192, v192\n"
  "v_mfma_f32_16x16x32_bf16 a[136:139], v[148:151], a[200:203], a[136:139]\n"
  "s_waitcnt lgkmcnt(8)\n"
  "v_exp_f32 v193, v193\n"
  "v_mfma_f32_16x16x32_bf16 a[140:143], v[148:151], a[216:219], a[140:143]\n"
  "s_waitcnt lgkmcnt(6)\n"
  "v_exp_f32 v194, v194\n"
  "v_mfma_f32_16x16x32_bf16 a[140:143], v[112:115], a[208:211], a[140:143]\n"
  "v_exp_f32 v195, v195\n"
  "v_smfmac_f32_16x16x64_bf16 a[236:239], a[240:243], v[140:147], v224\n"
  "s_waitcnt vmcnt(8) lgkmcnt(0)\n"
  "s_barrier\n"
  "v_mfma_f32_16x16x32_bf16 v[196:199], a[176:179], v[0:3], v[200:203]\n"
  "ds_read_b128 a[160:163], v228 offset:32768\n"
  "v_cvt_pk_bf16_f32 v172, v188, v189\n"
  "v_mfma_f32_16x16x32_bf16 v[196:199], a[180:183], v[4:7], v[196:199]\n"
  "ds_read_b128 a[164:167], v229 offset:32768\n"
  "v_cvt_pk_bf16_f32 v173, v190, v191\n"
  "v_mfma_f32_16x16x32_bf16 v[196:199], a[184:187], v[8:11], v[196:199]\n"
  "ds_read_b128 a[168:171], v230 offset:32768\n"
  "v_cvt_pk_bf16_f32 v180, v192, v193\n"
  "v_mfma_f32_16x16x32_bf16 v[196:199], a[188:191], v[12:15], v[196:199]\n"
  "ds_read_b128 a[172:175], v231 offset:32768\n"
  "v_cvt_pk_bf16_f32 v181, v194, v195\n"
  "v_mfma_f32_16x16x32_bf16 v[188:191], a[188:191], v[28:31], v[204:207]\n"
  "ds_read_b64_tr_b16 a[192:193], v236 offset:8192\n"
  "v_mfma_f32_16x16x32_bf16 v[188:191], a[184:187], v[24:27], v[188:191]\n"
  "ds_read_b64_tr_b16 a[194:195], v236 offset:10240\n"
  "v_mfma_f32_16x16x32_bf16 v[188:191], a[180:183], v[20:23], v[188:191]\n"
  "s_add_u32 m0, s67, 114688\n"
  "ds_read_b64_tr_b16 a[196:197], v240 offset:8192\n"
  "v_mfma_f32_16x16x32_bf16 v[188:191], a[176:179], v[16:19], v[188:191]\n"
  "buffer_load_dwordx4 v252, s[44:47], s71 offen nt lds\n"
  "v_mfma_f32_16x16x32_bf16 a[12:15], v[80:83], a[208:211], a[12:15]\n"
  "v_exp_f32 v196, v196\n"
  "v_mfma_f32_16x16x32_bf16 a[12:15], v[84:87], a[212:215], a[12:15]\n"
  "v_exp_f32 v197, v197\n"
  "v_mfma_f32_16x16x32_bf16 a[12:15], v[116:119], a[216:219], a[12:15]\n"
  "v_exp_f32 v198, v198\n"
  "v_mfma_f32_16x16x32_bf16 a[12:15], v[120:123], a[220:223], a[12:15]\n"
  "v_exp_f32 v199, v199\n"
  "v_mfma_f32_16x16x32_bf16 a[44:47], v[128:131], a[220:223], a[44:47]\n"
  "v_exp_f32 v188, v188\n"
  "v_mfma_f32_16x16x32_bf16 a[44:47], v[124:127], a[216:219], a[44:47]\n"
  "v_exp_f32 v189, v189\n"
  "v_mfma_f32_16x16x32_bf16 a[44:47], v[88:91], a[208:211], a[44:47]\n"
  "v_exp_f32 v190, v190\n"
  "v_mfma_f32_16x16x32_bf16 a[44:47], v[92:95], a[212:215], a[44:47]\n"
  "v_exp_f32 v191, v191\n"
  "v_mfma_f32_16x16x32_bf16 v[192:195], a[176:179], v[32:35], v[208:211]\n"
  "ds_read_b64_tr_b16 a[198:199], v240 offset:10240\n"
  "v_cvt_pk_bf16_f32 v158, v196, v197\n"
  "v_mfma_f32_16x16x32_bf16 v[192:195], a[180:183], v[36:39], v[192:195]\n"
  "ds_read_b64_tr_b16 a[200:201], v236 offset:24576\n"
  "v_cvt_pk_bf16_f32 v159, v198, v199\n"
  "v_mfma_f32_16x16x32_bf16 v[192:195], a[184:187], v[40:43], v[192:195]\n"
  "ds_read_b64_tr_b16 a[202:203], v236 offset:26624\n"
  "v_cvt_pk_bf16_f32 v166, v188, v189\n"
  "v_mfma_f32_16x16x32_bf16 v[192:195], a[188:191], v[44:47], v[192:195]\n"
  "ds_read_b64_tr_b16 a[204:205], v240 offset:24576\n"
  "v_cvt_pk_bf16_f32 v167, v190, v191\n"
  "v_mfma_f32_16x16x32_bf16 v[196:199], a[188:191], v[60:63], v[212:215]\n"
  "ds_read_b64_tr_b16 a[206:207], v240 offset:26624\n"
  "v_mfma_f32_16x16x32_bf16 v[196:199], a[184:187], v[56:59], v[196:199]\n"
  "ds_read_b128 a[188:191], v231 offset:34816\n"
  "v_mfma_f32_16x16x32_bf16 v[196:199], a[180:183], v[52:55], v[196:199]\n"
  "ds_read_b128 a[184:187], v230 offset:34816\n"
  "v_mfma_f32_16x16x32_bf16 v[196:199], a[176:179], v[48:51], v[196:199]\n"
  "buffer_load_dwordx4 v253, s[44:47], s71 offen offset:1024 nt lds\n"
  "v_mfma_f32_16x16x32_bf16 a[76:79], v[96:99], a[208:211], a[76:79]\n"
  "v_exp_f32 v192, v192\n"
  "v_mfma_f32_16x16x32_bf16 a[76:79], v[100:103], a[212:215], a[76:79]\n"
  "v_exp_f32 v193, v193\n"
  "v_mfma_f32_16x16x32_bf16 a[76:79], v[132:135], a[216:219], a[76:79]\n"
  "v_exp_f32 v194, v194\n"
  "v_mfma_f32_16x16x32_bf16 a[76:79], v[136:139], a[220:223], a[76:79]\n"
  "v_exp_f32 v195, v195\n"
  "v_mfma_f32_16x16x32_bf16 a[108:111], v[144:147], a[220:223], a[108:111]\n"
  "v_exp_f32 v196, v196\n"
  "v_mfma_f32_16x16x32_bf16 a[108:111], v[140:143], a[216:219], a[108:111]\n"
  "v_exp_f32 v197, v197\n"
  "v_mfma_f32_16x16x32_bf16 a[108:111], v[104:107], a[208:211], a[108:111]\n"
  "v_exp_f32 v198, v198\n"
  "v_mfma_f32_16x16x32_bf16 a[108:111], v[108:111], a[212:215], a[108:111]\n"
  "v_exp_f32 v199, v199\n"
  "s_waitcnt lgkmcnt(13)\n"
  "v_mfma_f32_16x16x32_bf16 v[188:191], a[160:163], v[64:67], v[216:219]\n"
  "s_waitcnt lgkmcnt(12)\n"
  "ds_read_b128 a[180:183], v229 offset:34816\n"
  "v_cvt_pk_bf16_f32 v174, v192, v193\n"
  "v_mfma_f32_16x16x32_bf16 v[188:191], a[164:167], v[68:71], v[188:191]\n"
  "s_waitcnt lgkmcnt(12)\n"
  "ds_read_b128 a[176:179], v228 offset:34816\n"
  "v_cvt_pk_bf16_f32 v175, v194, v195\n"
  "v_mfma_f32_16x16x32_bf16 v[188:191], a[168:171], v[72:75], v[188:191]\n"
  "s_waitcnt lgkmcnt(12)\n"
  "ds_read_b64_tr_b16 a[216:217], v237 offset:24576\n"
  "v_cvt_pk_bf16_f32 v182, v196, v197\n"
  "v_mfma_f32_16x16x32_bf16 v[188:191], a[172:175], v[76:79], v[188:191]\n"
  "ds_read_b64_tr_b16 a[218:219], v237 offset:26624\n"
  "v_cvt_pk_bf16_f32 v183, v198, v199\n"
  "v_mfma_f32_16x16x32_bf16 v[192:195], a[172:175], v[12:15], v[200:203]\n"
  "ds_read_b64_tr_b16 a[208:209], v237 offset:8192\n"
  "v_mfma_f32_16x16x32_bf16 v[192:195], a[168:171], v[8:11], v[192:195]\n"
  "ds_read_b64_tr_b16 a[210:211], v237 offset:10240\n"
  "v_mfma_f32_16x16x32_bf16 v[192:195], a[164:167], v[4:7], v[192:195]\n"
  "ds_read_b64_tr_b16 a[212:213], v241 offset:8192\n"
  "v_mfma_f32_16x16x32_bf16 v[192:195], a[160:163], v[0:3], v[192:195]\n"
  "s_waitcnt lgkmcnt(14)\n"
  "buffer_load_dwordx4 v252, s[44:47], s71 offen offset:2048 nt lds\n"
  "v_mfma_f32_16x16x32_bf16 a[16:19], v[116:119], a[192:195], a[16:19]\n"
  "s_waitcnt lgkmcnt(13)\n"
  "v_exp_f32 v188, v188\n"
  "v_mfma_f32_16x16x32_bf16 a[16:19], v[120:123], a[196:199], a[16:19]\n"
  "s_waitcnt lgkmcnt(11)\n"
  "v_exp_f32 v189, v189\n"
  "v_mfma_f32_16x16x32_bf16 a[16:19], v[152:155], a[200:203], a[16:19]\n"
  "s_waitcnt lgkmcnt(9)\n"
  "v_exp_f32 v190, v190\n"
  "v_mfma_f32_16x16x32_bf16 a[16:19], v[156:159], a[204:207], a[16:19]\n"
  "v_exp_f32 v191, v191\n"
  "v_mfma_f32_16x16x32_bf16 a[48:51], v[164:167], a[204:207], a[48:51]\n"
  "v_exp_f32 v192, v192\n"
  "v_mfma_f32_16x16x32_bf16 a[48:51], v[160:163], a[200:203], a[48:51]\n"
  "v_exp_f32 v193, v193\n"
  "v_mfma_f32_16x16x32_bf16 a[48:51], v[124:127], a[192:195], a[48:51]\n"
  "v_exp_f32 v194, v194\n"
  "v_mfma_f32_16x16x32_bf16 a[48:51], v[128:131], a[196:199], a[48:51]\n"
  "v_exp_f32 v195, v195\n"
  "v_mfma_f32_16x16x32_bf16 v[196:199], a[160:163], v[16:19], v[204:207]\n"
  "v_cvt_pk_bf16_f32 v112, v188, v189\n"
  "v_cvt_pk_bf16_f32 v113, v190, v191\n"
  "v_mfma_f32_16x16x32_bf16 v[196:199], a[164:167], v[20:23], v[196:199]\n"
  "v_add_f32 v220, v188, v220\n"
  "v_add_f32 v221, v189, v221\n"
  "v_mfma_f32_16x16x32_bf16 v[196:199], a[168:171], v[24:27], v[196:199]\n"
  "ds_read_b64_tr_b16 a[214:215], v241 offset:10240\n"
  "v_add_f32 v220, v190, v220\n"
  "v_mfma_f32_16x16x32_bf16 v[196:199], a[172:175], v[28:31], v[196:199]\n"
  "ds_read_b64_tr_b16 a[220:221], v241 offset:24576\n"
  "v_add_f32 v221, v191, v221\n"
  "v_mfma_f32_16x16x32_bf16 v[188:191], a[172:175], v[44:47], v[208:211]\n"
  "ds_read_b64_tr_b16 a[222:223], v241 offset:26624\n"
  "v_cvt_pk_bf16_f32 v80, v192, v193\n"
  "v_mfma_f32_16x16x32_bf16 v[188:191], a[168:171], v[40:43], v[188:191]\n"
  "v_cvt_pk_bf16_f32 v81, v194, v195\n"
  "v_mfma_f32_16x16x32_bf16 v[188:191], a[164:167], v[36:39], v[188:191]\n"
  "v_mfma_f32_16x16x32_bf16 v[188:191], a[160:163], v[32:35], v[188:191]\n"
  "buffer_load_dwordx4 v253, s[44:47], s71 offen offset:3072 nt lds\n"
  "v_mfma_f32_16x16x32_bf16 a[80:83], v[132:135], a[192:195], a[80:83]\n"
  "s_add_u32 s71, s71, s73\n"
  "v_exp_f32 v196, v196\n"
  "v_mfma_f32_16x16x32_bf16 a[80:83], v[136:139], a[196:199], a[80:83]\n"
  "s_and_b32 s71, s71, s8\n"
  "v_exp_f32 v197, v197\n"
  "v_mfma_f32_16x16x32_bf16 a[80:83], v[168:171], a[200:203], a[80:83]\n"
  "v_exp_f32 v198, v198\n"
  "v_mfma_f32_16x16x32_bf16 a[80:83], v[172:175], a[204:207], a[80:83]\n"
  "v_exp_f32 v199, v199\n"
  "v_mfma_f32_16x16x32_bf16 a[112:115], v[180:183], a[204:207], a[112:115]\n"
  "v_exp_f32 v188, v188\n"
  "v_mfma_f32_16x16x32_bf16 a[112:115], v[176:179], a[200:203], a[112:115]\n"
  "v_exp_f32 v189, v189\n"
  "v_mfma_f32_16x16x32_bf16 a[112:115], v[140:143], a[192:195], a[112:115]\n"
  "v_exp_f32 v190, v190\n"
  "v_mfma_f32_16x16x32_bf16 a[112:115], v[144:147], a[196:199], a[112:115]\n"
  "v_exp_f32 v191, v191\n"
  "v_smfmac_f32_16x16x64_bf16 a[224:227], a[240:243], v[152:159], v224\n"
  "v_cvt_pk_bf16_f32 v88, v196, v197\n"
  "v_mfma_f32_16x16x32_bf16 v[192:195], a[160:163], v[48:51], v[212:215]\n"
  "ds_read_b64_tr_b16 a[204:205], v242 offset:24576\n"
  "v_cvt_pk_bf16_f32 v89, v198, v199\n"
  "v_mfma_f32_16x16x32_bf16 v[192:195], a[164:167], v[52:55], v[192:195]\n"
  "ds_read_b64_tr_b16 a[206:207], v242 offset:26624\n"
  "v_cvt_pk_bf16_f32 v96, v188, v189\n"
  "v_mfma_f32_16x16x32_bf16 v[192:195], a[168:171], v[56:59], v[192:195]\n"
  "s_add_u32 m0, s17, 16384\n"
  "ds_read_b64_tr_b16 a[196:197], v242 offset:8192\n"
  "v_cvt_pk_bf16_f32 v97, v190, v191\n"
  "v_mfma_f32_16x16x32_bf16 v[192:195], a[172:175], v[60:63], v[192:195]\n"
  "s_waitcnt lgkmcnt(14)\n"
  "ds_read_b64_tr_b16 a[198:199], v242 offset:10240\n"
  "v_mfma_f32_16x16x32_bf16 v[196:199], a[188:191], v[60:63], v[212:215]\n"
  "s_waitcnt lgkmcnt(14)\n"
  "ds_read_b128 a[160:163], v232 offset:32768\n"
  "v_mfma_f32_16x16x32_bf16 v[196:199], a[184:187], v[56:59], v[196:199]\n"
  "s_waitcnt lgkmcnt(14)\n"
  "ds_read_b128 a[164:167], v233 offset:32768\n"
  "v_mfma_f32_16x16x32_bf16 v[196:199], a[180:183], v[52:55], v[196:199]\n"
  "s_waitcnt lgkmcnt(14)\n"
  "ds_read_b128 a[168:171], v234 offset:32768\n"
  "v_mfma_f32_16x16x32_bf16 v[196:199], a[176:179], v[48:51], v[196:199]\n"
  "buffer_load_dwordx4 v254, s[48:51], s72 offen nt lds\n"
  "v_mfma_f32_16x16x32_bf16 a[144:147], v[148:151], a[192:195], a[144:147]\n"
  "v_exp_f32 v192, v192\n"
  "v_mfma_f32_16x16x32_bf16 a[144:147], v[184:187], a[200:203], a[144:147]\n"
  "s_waitcnt lgkmcnt(13)\n"
  "v_exp_f32 v193, v193\n"
  "v_mfma_f32_16x16x32_bf16 a[148:151], v[184:187], a[216:219], a[148:151]\n"
  "s_waitcnt lgkmcnt(11)\n"
  "v_exp_f32 v194, v194\n"
  "v_mfma_f32_16x16x32_bf16 a[148:151], v[148:151], a[208:211], a[148:151]\n"
  "v_exp_f32 v195, v195\n"
  "v_mfma_f32_16x16x32_bf16 a[20:23], v[116:119], a[208:211], a[20:23]\n"
  "v_exp_f32 v196, v196\n"
  "v_mfma_f32_16x16x32_bf16 a[20:23], v[152:155], a[216:219], a[20:23]\n"
  "s_waitcnt lgkmcnt(9)\n"
  "v_exp_f32 v197, v197\n"
  "v_mfma_f32_16x16x32_bf16 a[20:23], v[120:123], a[212:215], a[20:23]\n"
  "s_waitcnt lgkmcnt(7)\n"
  "v_exp_f32 v198, v198\n"
  "v_mfma_f32_16x16x32_bf16 a[20:23], v[156:159], a[220:223], a[20:23]\n"
  "v_exp_f32 v199, v199\n"
  "v_mfma_f32_16x16x32_bf16 v[188:191], a[176:179], v[32:35], v[208:211]\n"
  "ds_read_b64_tr_b16 a[200:201], v238 offset:24576\n"
  "v_cvt_pk_bf16_f32 v104, v192, v193\n"
  "v_mfma_f32_16x16x32_bf16 v[188:191], a[180:183], v[36:39], v[188:191]\n"
  "ds_read_b64_tr_b16 a[202:203], v238 offset:26624\n"
  "v_cvt_pk_bf16_f32 v105, v194, v195\n"
  "v_mfma_f32_16x16x32_bf16 v[188:191], a[184:187], v[40:43], v[188:191]\n"
  "ds_read_b64_tr_b16 a[192:193], v238 offset:8192\n"
  "v_cvt_pk_bf16_f32 v106, v196, v197\n"
  "v_mfma_f32_16x16x32_bf16 v[188:191], a[188:191], v[44:47], v[188:191]\n"
  "ds_read_b64_tr_b16 a[194:195], v238 offset:10240\n"
  "v_cvt_pk_bf16_f32 v107, v198, v199\n"
  "v_mfma_f32_16x16x32_bf16 v[192:195], a[188:191], v[28:31], v[204:207]\n"
  "ds_read_b128 a[172:175], v235 offset:32768\n"
  "v_mfma_f32_16x16x32_bf16 v[192:195], a[184:187], v[24:27], v[192:195]\n"
  "v_mfma_f32_16x16x32_bf16 v[192:195], a[180:183], v[20:23], v[192:195]\n"
  "v_mfma_f32_16x16x32_bf16 v[192:195], a[176:179], v[16:19], v[192:195]\n"
  "buffer_load_dwordx4 v255, s[48:51], s72 offen offset:1024 nt lds\n"
  "v_mfma_f32_16x16x32_bf16 a[52:55], v[124:127], a[208:211], a[52:55]\n"
  "v_exp_f32 v188, v188\n"
  "v_mfma_f32_16x16x32_bf16 a[52:55], v[128:131], a[212:215], a[52:55]\n"
  "v_exp_f32 v189, v189\n"
  "v_mfma_f32_16x16x32_bf16 a[52:55], v[160:163], a[216:219], a[52:55]\n"
  "v_exp_f32 v190, v190\n"
  "v_mfma_f32_16x16x32_bf16 a[52:55], v[164:167], a[220:223], a[52:55]\n"
  "v_exp_f32 v191, v191\n"
  "v_mfma_f32_16x16x32_bf16 a[84:87], v[172:175], a[220:223], a[84:87]\n"
  "v_exp_f32 v192, v192\n"
  "v_mfma_f32_16x16x32_bf16 a[84:87], v[168:171], a[216:219], a[84:87]\n"
  "v_exp_f32 v193, v193\n"
  "v_mfma_f32_16x16x32_bf16 a[84:87], v[132:135], a[208:211], a[84:87]\n"
  "v_exp_f32 v194, v194\n"
  "v_mfma_f32_16x16x32_bf16 a[84:87], v[136:139], a[212:215], a[84:87]\n"
  "v_exp_f32 v195, v195\n"
  "v_smfmac_f32_16x16x64_bf16 a[228:231], a[240:243], v[160:167], v224\n"
  "v_cvt_pk_bf16_f32 v98, v188, v189\n"
  "v_mfma_f32_16x16x32_bf16 v[196:199], a[176:179], v[0:3], v[200:203]\n"
  "v_cvt_pk_bf16_f32 v99, v190, v191\n"
  "v_cvt_pk_bf16_f32 v90, v192, v193\n"
  "v_mfma_f32_16x16x32_bf16 v[196:199], a[180:183], v[4:7], v[196:199]\n"
  "v_cvt_pk_bf16_f32 v91, v194, v195\n"
  "v_mfma_f32_16x16x32_bf16 v[196:199], a[184:187], v[8:11], v[196:199]\n"
  "v_mfma_f32_16x16x32_bf16 v[196:199], a[188:191], v[12:15], v[196:199]\n"
  "v_mfma_f32_16x16x32_bf16 v[188:191], a[188:191], v[76:79], v[216:219]\n"
  "ds_read_b128 a[188:191], v235 offset:34816\n"
  "v_mfma_f32_16x16x32_bf16 v[188:191], a[184:187], v[72:75], v[188:191]\n"
  "ds_read_b128 a[184:187], v234 offset:34816\n"
  "v_mfma_f32_16x16x32_bf16 v[188:191], a[180:183], v[68:71], v[188:191]\n"
  "v_mfma_f32_16x16x32_bf16 v[188:191], a[176:179], v[64:67], v[188:191]\n"
  "buffer_load_dwordx4 v254, s[48:51], s72 offen offset:2048 nt lds\n"
  "v_mfma_f32_16x16x32_bf16 a[116:119], v[140:143], a[208:211], a[116:119]\n"
  "v_exp_f32 v196, v196\n"
  "v_mfma_f32_16x16x32_bf16 a[116:119], v[144:147], a[212:215], a[116:119]\n"
  "v_exp_f32 v197, v197\n"
  "v_mfma_f32_16x16x32_bf16 a[116:119], v[176:179], a[216:219], a[116:119]\n"
  "v_exp_f32 v198, v198\n"
  "v_mfma_f32_16x16x32_bf16 a[116:119], v[180:183], a[220:223], a[116:119]\n"
  "s_waitcnt lgkmcnt(12)\n"
  "v_exp_f32 v199, v199\n"
  "v_mfma_f32_16x16x32_bf16 a[120:123], v[180:183], a[204:207], a[120:123]\n"
  "s_waitcnt lgkmcnt(5)\n"
  "v_exp_f32 v188, v188\n"
  "v_mfma_f32_16x16x32_bf16 a[120:123], v[176:179], a[200:203], a[120:123]\n"
  "s_waitcnt lgkmcnt(3)\n"
  "v_exp_f32 v189, v189\n"
  "v_mfma_f32_16x16x32_bf16 a[120:123], v[140:143], a[192:195], a[120:123]\n"
  "v_exp_f32 v190, v190\n"
  "v_mfma_f32_16x16x32_bf16 a[120:123], v[144:147], a[196:199], a[120:123]\n"
  "v_exp_f32 v191, v191\n"
  "v_mfma_f32_16x16x32_bf16 v[192:195], a[160:163], v[0:3], v[200:203]\n"
  "v_cvt_pk_bf16_f32 v82, v196, v197\n"
  "v_cvt_pk_bf16_f32 v83, v198, v199\n"
  "v_mfma_f32_16x16x32_bf16 v[192:195], a[164:167], v[4:7], v[192:195]\n"
  "v_cvt_pk_bf16_f32 v114, v188, v189\n"
  "v_cvt_pk_bf16_f32 v115, v190, v191\n"
  "v_mfma_f32_16x16x32_bf16 v[192:195], a[168:171], v[8:11], v[192:195]\n"
  "s_waitcnt lgkmcnt(2)\n"
  "ds_read_b64_tr_b16 a[216:217], v239 offset:24576\n"
  "v_add_f32 v220, v188, v220\n"
  "v_mfma_f32_16x16x32_bf16 v[192:195], a[172:175], v[12:15], v[192:195]\n"
  "ds_read_b64_tr_b16 a[218:219], v239 offset:26624\n"
  "v_add_f32 v221, v189, v221\n"
  "v_mfma_f32_16x16x32_bf16 v[196:199], a[172:175], v[28:31], v[204:207]\n"
  "ds_read_b64_tr_b16 a[208:209], v239 offset:8192\n"
  "v_add_f32 v220, v190, v220\n"
  "v_mfma_f32_16x16x32_bf16 v[196:199], a[168:171], v[24:27], v[196:199]\n"
  "ds_read_b64_tr_b16 a[210:211], v239 offset:10240\n"
  "v_add_f32 v221, v191, v221\n"
  "v_mfma_f32_16x16x32_bf16 v[196:199], a[164:167], v[20:23], v[196:199]\n"
  "ds_read_b128 a[176:179], v232 offset:34816\n"
  "v_mfma_f32_16x16x32_bf16 v[196:199], a[160:163], v[16:19], v[196:199]\n"
  "buffer_load_dwordx4 v255, s[48:51], s72 offen offset:3072 nt lds\n"
  "v_mfma_f32_16x16x32_bf16 a[88:91], v[132:135], a[192:195], a[88:91]\n"
  "s_add_u32 s72, s72, s74\n"
  "v_exp_f32 v192, v192\n"
  "v_mfma_f32_16x16x32_bf16 a[88:91], v[136:139], a[196:199], a[88:91]\n"
  "s_and_b32 s72, s72, s9\n"
  "v_exp_f32 v193, v193\n"
  "v_mfma_f32_16x16x32_bf16 a[88:91], v[168:171], a[200:203], a[88:91]\n"
  "v_exp_f32 v194, v194\n"
  "v_mfma_f32_16x16x32_bf16 a[88:91], v[172:175], a[204:207], a[88:91]\n"
  "v_exp_f32 v195, v195\n"
  "v_mfma_f32_16x16x32_bf16 a[56:59], v[164:167], a[204:207], a[56:59]\n"
  "v_exp_f32 v196, v196\n"
  "v_mfma_f32_16x16x32_bf16 a[56:59], v[160:163], a[200:203], a[56:59]\n"
  "v_exp_f32 v197, v197\n"
  "v_mfma_f32_16x16x32_bf16 a[56:59], v[124:127], a[192:195], a[56:59]\n"
  "v_exp_f32 v198, v198\n"
  "v_mfma_f32_16x16x32_bf16 a[56:59], v[128:131], a[196:199], a[56:59]\n"
  "v_exp_f32 v199, v199\n"
  "v_smfmac_f32_16x16x64_bf16 a[232:235], a[240:243], v[168:175], v224\n"
  "v_cvt_pk_bf16_f32 v84, v192, v193\n"
  "v_mfma_f32_16x16x32_bf16 v[188:191], a[160:163], v[32:35], v[208:211]\n"
  "ds_read_b128 a[180:183], v233 offset:34816\n"
  "v_cvt_pk_bf16_f32 v85, v194, v195\n"
  "v_mfma_f32_16x16x32_bf16 v[188:191], a[164:167], v[36:39], v[188:191]\n"
  "ds_read_b64_tr_b16 a[212:213], v243 offset:8192\n"
  "v_cvt_pk_bf16_f32 v92, v196, v197\n"
  "v_mfma_f32_16x16x32_bf16 v[188:191], a[168:171], v[40:43], v[188:191]\n"
  "ds_read_b64_tr_b16 a[214:215], v243 offset:10240\n"
  "v_cvt_pk_bf16_f32 v93, v198, v199\n"
  "v_mfma_f32_16x16x32_bf16 v[188:191], a[172:175], v[44:47], v[188:191]\n"
  "ds_read_b64_tr_b16 a[220:221], v243 offset:24576\n"
  "v_mfma_f32_16x16x32_bf16 v[192:195], a[172:175], v[60:63], v[212:215]\n"
  "ds_read_b64_tr_b16 a[222:223], v243 offset:26624\n"
  "v_mfma_f32_16x16x32_bf16 v[192:195], a[168:171], v[56:59], v[192:195]\n"
  "v_mfma_f32_16x16x32_bf16 v[192:195], a[164:167], v[52:55], v[192:195]\n"
  "v_mfma_f32_16x16x32_bf16 v[192:195], a[160:163], v[48:51], v[192:195]\n"
  "s_cmp_eq_u32 s75, 4\n"
  "s_cbranch_scc1 L_side2_%=\n"
  "L_back2_%=:\n"
  "v_mfma_f32_16x16x32_bf16 a[24:27], v[120:123], a[196:199], a[24:27]\n"
  "v_exp_f32 v188, v188\n"
  "v_mfma_f32_16x16x32_bf16 a[24:27], v[156:159], a[204:207], a[24:27]\n"
  "v_exp_f32 v189, v189\n"
  "v_mfma_f32_16x16x32_bf16 a[24:27], v[116:119], a[192:195], a[24:27]\n"
  "v_exp_f32 v190, v190\n"
  "v_mfma_f32_16x16x32_bf16 a[24:27], v[152:155], a[200:203], a[24:27]\n"
  "v_exp_f32 v191, v191\n"
  "v_mfma_f32_16x16x32_bf16 a[152:155], v[148:151], a[192:195], a[152:155]\n"
  "v_exp_f32 v192, v192\n"
  "v_mfma_f32_16x16x32_bf16 a[152:155], v[184:187], a[200:203], a[152:155]\n"
  "s_waitcnt lgkmcnt(8)\n"
  "v_exp_f32 v193, v193\n"
  "v_mfma_f32_16x16x32_bf16 a[156:159], v[184:187], a[216:219], a[156:159]\n"
  "s_waitcnt lgkmcnt(6)\n"
  "v_exp_f32 v194, v194\n"
  "v_mfma_f32_16x16x32_bf16 a[156:159], v[148:151], a[208:211], a[156:159]\n"
  "v_exp_f32 v195, v195\n"
  "v_smfmac_f32_16x16x64_bf16 a[236:239], a[240:243], v[176:183], v224\n"
  "s_waitcnt vmcnt(8) lgkmcnt(0)\n"
  "s_barrier\n"
  "v_mfma_f32_16x16x32_bf16 v[196:199], a[176:179], v[0:3], v[200:203]\n"
  "ds_read_b128 a[160:163], v228 offset:16384\n"
  "v_cvt_pk_bf16_f32 v100, v188, v189\n"
  "v_mfma_f32_16x16x32_bf16 v[196:199], a[180:183], v[4:7], v[196:199]\n"
  "ds_read_b128 a[164:167], v229 offset:16384\n"
  "v_cvt_pk_bf16_f32 v101, v190, v191\n"
  "v_mfma_f32_16x16x32_bf16 v[196:199], a[184:187], v[8:11], v[196:199]\n"
  "ds_read_b128 a[168:171], v230 offset:16384\n"
  "v_cvt_pk_bf16_f32 v108, v192, v193\n"
  "v_mfma_f32_16x16x32_bf16 v[196:199], a[188:191], v[12:15], v[196:199]\n"
  "ds_read_b128 a[172:175], v231 offset:16384\n"
  "v_cvt_pk_bf16_f32 v109, v194, v195\n"
  "v_mfma_f32_16x16x32_bf16 v[188:191], a[188:191], v[28:31], v[204:207]\n"
  "ds_read_b64_tr_b16 a[192:193], v236 offset:16384\n"
  "v_mfma_f32_16x16x32_bf16 v[188:191], a[184:187], v[24:27], v[188:191]\n"
  "ds_read_b64_tr_b16 a[194:195], v236 offset:18432\n"
  "v_mfma_f32_16x16x32_bf16 v[188:191], a[180:183], v[20:23], v[188:191]\n"
  "s_add_u32 m0, s67, 147456\n"
  "ds_read_b64_tr_b16 a[196:197], v240 offset:16384\n"
  "v_mfma_f32_16x16x32_bf16 v[188:191], a[176:179], v[16:19], v[188:191]\n"
  "buffer_load_dwordx4 v252, s[44:47], s71 offen nt lds\n"
  "v_mfma_f32_16x16x32_bf16 a[28:31], v[116:119], a[208:211], a[28:31]\n"
  "v_exp_f32 v196, v196\n"
  "v_mfma_f32_16x16x32_bf16 a[28:31], v[120:123], a[212:215], a[28:31]\n"
  "v_exp_f32 v197, v197\n"
  "v_mfma_f32_16x16x32_bf16 a[28:31], v[152:155], a[216:219], a[28:31]\n"
  "v_exp_f32 v198, v198\n"
  "v_mfma_f32_16x16x32_bf16 a[28:31], v[156:159], a[220:223], a[28:31]\n"
  "v_exp_f32 v199, v199\n"
  "v_mfma_f32_16x16x32_bf16 a[60:63], v[164:167], a[220:223], a[60:63]\n"
  "v_exp_f32 v188, v188\n"
  "v_mfma_f32_16x16x32_bf16 a[60:63], v[160:163], a[216:219], a[60:63]\n"
  "v_exp_f32 v189, v189\n"
  "v_mfma_f32_16x16x32_bf16 a[60:63], v[124:127], a[208:211], a[60:63]\n"
  "v_exp_f32 v190, v190\n"
  "v_mfma_f32_16x16x32_bf16 a[60:63], v[128:131], a[212:215], a[60:63]\n"
  "v_exp_f32 v191, v191\n"
  "v_mfma_f32_16x16x32_bf16 v[192:195], a[176:179], v[32:35], v[208:211]\n"
  "ds_read_b64_tr_b16 a[198:199], v240 offset:18432\n"
  "v_cvt_pk_bf16_f32 v86, v196, v197\n"
  "v_mfma_f32_16x16x32_bf16 v[192:195], a[180:183], v[36:39], v[192:195]\n"
  "ds_read_b64_tr_b16 a[200:201], v244 offset:0\n"
  "v_cvt_pk_bf16_f32 v87, v198, v199\n"
  "v_mfma_f32_16x16x32_bf16 v[192:195], a[184:187], v[40:43], v[192:195]\n"
  "ds_read_b64_tr_b16 a[202:203], v244 offset:2048\n"
  "v_cvt_pk_bf16_f32 v94, v188, v189\n"
  "v_mfma_f32_16x16x32_bf16 v[192:195], a[188:191], v[44:47], v[192:195]\n"
  "ds_read_b64_tr_b16 a[204:205], v248 offset:0\n"
  "v_cvt_pk_bf16_f32 v95, v190, v191\n"
  "v_mfma_f32_16x16x32_bf16 v[196:199], a[188:191], v[60:63], v[212:215]\n"
  "ds_read_b64_tr_b16 a[206:207], v248 offset:2048\n"
  "v_mfma_f32_16x16x32_bf16 v[196:199], a[184:187], v[56:59], v[196:199]\n"
  "ds_read_b128 a[188:191], v231 offset:18432\n"
  "v_mfma_f32_16x16x32_bf16 v[196:199], a[180:183], v[52:55], v[196:199]\n"
  "ds_read_b128 a[184:187], v230 offset:18432\n"
  "v_mfma_f32_16x16x32_bf16 v[196:199], a[176:179], v[48:51], v[196:199]\n"
  "buffer_load_dwordx4 v253, s[44:47], s71 offen offset:1024 nt lds\n"
  "v_mfma_f32_16x16x32_bf16 a[92:95], v[132:135], a[208:211], a[92:95]\n"
  "v_exp_f32 v192, v192\n"
  "v_mfma_f32_16x16x32_bf16 a[92:95], v[136:139], a[212:215], a[92:95]\n"
  "v_exp_f32 v193, v193\n"
  "v_mfma_f32_16x16x32_bf16 a[92:95], v[168:171], a[216:219], a[92:95]\n"
  "v_exp_f32 v194, v194\n"
  "v_mfma_f32_16x16x32_bf16 a[92:95], v[172:175], a[220:223], a[92:95]\n"
  "v_exp_f32 v195, v195\n"
  "v_mfma_f32_16x16x32_bf16 a[124:127], v[180:183], a[220:223], a[124:127]\n"
  "v_exp_f32 v196, v196\n"
  "v_mfma_f32_16x16x32_bf16 a[124:127], v[176:179], a[216:219], a[124:127]\n"
  "v_exp_f32 v197, v197\n"
  "v_mfma_f32_16x16x32_bf16 a[124:127], v[140:143], a[208:211], a[124:127]\n"
  "v_exp_f32 v198, v198\n"
  "v_mfma_f32_16x16x32_bf16 a[124:127], v[144:147], a[212:215], a[124:127]\n"
  "v_exp_f32 v199, v199\n"
  "s_waitcnt lgkmcnt(13)\n"
  "v_mfma_f32_16x16x32_bf16 v[188:191], a[160:163], v[64:67], v[216:219]\n"
  "s_waitcnt lgkmcnt(12)\n"
  "ds_read_b128 a[180:183], v229 offset:18432\n"
  "v_cvt_pk_bf16_f32 v102, v192, v193\n"
  "v_mfma_f32_16x16x32_bf16 v[188:191], a[164:167], v[68:71], v[188:191]\n"
  "s_waitcnt lgkmcnt(12)\n"
  "ds_read_b128 a[176:179], v228 offset:18432\n"
  "v_cvt_pk_bf16_f32 v103, v194, v195\n"
  "v_mfma_f32_16x16x32_bf16 v[188:191], a[168:171], v[72:75], v[188:191]\n"
  "s_waitcnt lgkmcnt(12)\n"
  "ds_read_b64_tr_b16 a[216:217], v245 offset:0\n"
  "v_cvt_pk_bf16_f32 v110, v196, v197\n"
  "v_mfma_f32_16x16x32_bf16 v[188:191], a[172:175], v[76:79], v[188:191]\n"
  "ds_read_b64_tr_b16 a[218:219], v245 offset:2048\n"
  "v_cvt_pk_bf16_f32 v111, v198, v199\n"
  "v_mfma_f32_16x16x32_bf16 v[192:195], a[172:175], v[12:15], v[200:203]\n"
  "ds_read_b64_tr_b16 a[208:209], v237 offset:16384\n"
  "v_xor_b32 v236, 0x8000, v236\n"
  "v_mfma_f32_16x16x32_bf16 v[192:195], a[168:171], v[8:11], v[192:195]\n"
  "ds_read_b64_tr_b16 a[210:211], v237 offset:18432\n"
  "v_xor_b32 v240, 0x8000, v240\n"
  "v_mfma_f32_16x16x32_bf16 v[192:195], a[164:167], v[4:7], v[192:195]\n"
  "ds_read_b64_tr_b16 a[212:213], v241 offset:16384\n"
  "v_xor_b32 v245, 0x8000, v245\n"
  "v_mfma_f32_16x16x32_bf16 v[192:195], a[160:163], v[0:3], v[192:195]\n"
  "s_waitcnt lgkmcnt(14)\n"
  "buffer_load_dwordx4 v252, s[44:47], s71 offen offset:2048 nt lds\n"
  "v_mfma_f32_16x16x32_bf16 a[0:3], v[152:155], a[192:195], a[0:3]\n"
  "s_waitcnt lgkmcnt(13)\n"
  "v_exp_f32 v188, v188\n"
  "v_mfma_f32_16x16x32_bf16 a[0:3], v[156:159], a[196:199], a[0:3]\n"
  "s_waitcnt lgkmcnt(11)\n"
  "v_exp_f32 v189, v189\n"
  "v_mfma_f32_16x16x32_bf16 a[0:3], v[80:83], a[200:203], a[0:3]\n"
  "s_waitcnt lgkmcnt(9)\n"
  "v_exp_f32 v190, v190\n"
  "v_mfma_f32_16x16x32_bf16 a[0:3], v[84:87], a[204:207], a[0:3]\n"
  "v_exp_f32 v191, v191\n"
  "v_mfma_f32_16x16x32_bf16 a[32:35], v[92:95], a[204:207], a[32:35]\n"
  "v_exp_f32 v192, v192\n"
  "v_mfma_f32_16x16x32_bf16 a[32:35], v[88:91], a[200:203], a[32:35]\n"
  "v_exp_f32 v193, v193\n"
  "v_mfma_f32_16x16x32_bf16 a[32:35], v[160:163], a[192:195], a[32:35]\n"
  "v_exp_f32 v194, v194\n"
  "v_mfma_f32_16x16x32_bf16 a[32:35], v[164:167], a[196:199], a[32:35]\n"
  "v_exp_f32 v195, v195\n"
  "v_mfma_f32_16x16x32_bf16 v[196:199], a[160:163], v[16:19], v[204:207]\n"
  "v_cvt_pk_bf16_f32 v148, v188, v189\n"
  "v_cvt_pk_bf16_f32 v149, v190, v191\n"
  "v_mfma_f32_16x16x32_bf16 v[196:199], a[164:167], v[20:23], v[196:199]\n"
  "v_add_f32 v220, v188, v220\n"
  "v_add_f32 v221, v189, v221\n"
  "v_mfma_f32_16x16x32_bf16 v[196:199], a[168:171], v[24:27], v[196:199]\n"
  "ds_read_b64_tr_b16 a[214:215], v241 offset:18432\n"
  "v_add_f32 v220, v190, v220\n"
  "v_mfma_f32_16x16x32_bf16 v[196:199], a[172:175], v[28:31], v[196:199]\n"
  "ds_read_b64_tr_b16 a[220:221], v249 offset:0\n"
  "v_add_f32 v221, v191, v221\n"
  "v_mfma_f32_16x16x32_bf16 v[188:191], a[172:175], v[44:47], v[208:211]\n"
  "ds_read_b64_tr_b16 a[222:223], v249 offset:2048\n"
  "v_cvt_pk_bf16_f32 v116, v192, v193\n"
  "v_mfma_f32_16x16x32_bf16 v[188:191], a[168:171], v[40:43], v[188:191]\n"
  "v_cvt_pk_bf16_f32 v117, v194, v195\n"
  "v_xor_b32 v237, 0x8000, v237\n"
  "v_mfma_f32_16x16x32_bf16 v[188:191], a[164:167], v[36:39], v[188:191]\n"
  "v_xor_b32 v241, 0x8000, v241\n"
  "v_xor_b32 v249, 0x8000, v249\n"
  "v_mfma_f32_16x16x32_bf16 v[188:191], a[160:163], v[32:35], v[188:191]\n"
  "buffer_load_dwordx4 v253, s[44:47], s71 offen offset:3072 nt lds\n"
  "v_mfma_f32_16x16x32_bf16 a[64:67], v[168:171], a[192:195], a[64:67]\n"
  "s_add_u32 s71, s71, s73\n"
  "v_exp_f32 v196, v196\n"
  "v_mfma_f32_16x16x32_bf16 a[64:67], v[172:175], a[196:199], a[64:67]\n"
  "s_and_b32 s71, s71, s8\n"
  "v_exp_f32 v197, v197\n"
  "v_mfma_f32_16x16x32_bf16 a[64:67], v[96:99], a[200:203], a[64:67]\n"
  "v_exp_f32 v198, v198\n"
  "v_mfma_f32_16x16x32_bf16 a[64:67], v[100:103], a[204:207], a[64:67]\n"
  "v_exp_f32 v199, v199\n"
  "v_mfma_f32_16x16x32_bf16 a[96:99], v[108:111], a[204:207], a[96:99]\n"
  "v_exp_f32 v188, v188\n"
  "v_mfma_f32_16x16x32_bf16 a[96:99], v[104:107], a[200:203], a[96:99]\n"
  "v_exp_f32 v189, v189\n"
  "v_mfma_f32_16x16x32_bf16 a[96:99], v[176:179], a[192:195], a[96:99]\n"
  "v_exp_f32 v190, v190\n"
  "v_mfma_f32_16x16x32_bf16 a[96:99], v[180:183], a[196:199], a[96:99]\n"
  "v_exp_f32 v191, v191\n"
  "v_smfmac_f32_16x16x64_bf16 a[224:227], a[240:243], v[80:87], v224\n"
  "v_cvt_pk_bf16_f32 v124, v196, v197\n"
  "v_mfma_f32_16x16x32_bf16 v[192:195], a[160:163], v[48:51], v[212:215]\n"
  "ds_read_b64_tr_b16 a[204:205], v250 offset:0\n"
  "v_cvt_pk_bf16_f32 v125, v198, v199\n"
  "v_mfma_f32_16x16x32_bf16 v[192:195], a[164:167], v[52:55], v[192:195]\n"
  "ds_read_b64_tr_b16 a[206:207], v250 offset:2048\n"
  "v_cvt_pk_bf16_f32 v132, v188, v189\n"
  "v_mfma_f32_16x16x32_bf16 v[192:195], a[168:171], v[56:59], v[192:195]\n"
  "s_add_u32 m0, s16, 0\n"
  "ds_read_b64_tr_b16 a[196:197], v242 offset:16384\n"
  "v_cvt_pk_bf16_f32 v133, v190, v191\n"
  "v_mfma_f32_16x16x32_bf16 v[192:195], a[172:175], v[60:63], v[192:195]\n"
  "s_waitcnt lgkmcnt(14)\n"
  "ds_read_b64_tr_b16 a[198:199], v242 offset:18432\n"
  "v_xor_b32 v250, 0x8000, v250\n"
  "v_mfma_f32_16x16x32_bf16 v[196:199], a[188:191], v[60:63], v[212:215]\n"
  "s_waitcnt lgkmcnt(14)\n"
  "ds_read_b128 a[160:163], v232 offset:16384\n"
  "v_xor_b32 v242, 0x8000, v242\n"
  "v_mfma_f32_16x16x32_bf16 v[196:199], a[184:187], v[56:59], v[196:199]\n"
  "s_waitcnt lgkmcnt(14)\n"
  "ds_read_b128 a[164:167], v233 offset:16384\n"
  "v_mfma_f32_16x16x32_bf16 v[196:199], a[180:183], v[52:55], v[196:199]\n"
  "s_waitcnt lgkmcnt(14)\n"
  "ds_read_b128 a[168:171], v234 offset:16384\n"
  "v_mfma_f32_16x16x32_bf16 v[196:199], a[176:179], v[48:51], v[196:199]\n"
  "buffer_load_dwordx4 v254, s[48:51], s72 offen nt lds\n"
  "v_mfma_f32_16x16x32_bf16 a[128:131], v[184:187], a[192:195], a[128:131]\n"
  "v_exp_f32 v192, v192\n"
  "v_mfma_f32_16x16x32_bf16 a[128:131], v[112:115], a[200:203], a[128:131]\n"
  "s_waitcnt lgkmcnt(13)\n"
  "v_exp_f32 v193, v193\n"
  "v_mfma_f32_16x16x32_bf16 a[132:135], v[112:115], a[216:219], a[132:135]\n"
  "s_waitcnt lgkmcnt(11)\n"
  "v_exp_f32 v194, v194\n"
  "v_mfma_f32_16x16x32_bf16 a[132:135], v[184:187], a[208:211], a[132:135]\n"
  "v_exp_f32 v195, v195\n"
  "v_mfma_f32_16x16x32_bf16 a[4:7], v[152:155], a[208:211], a[4:7]\n"
  "v_exp_f32 v196, v196\n"
  "v_mfma_f32_16x16x32_bf16 a[4:7], v[80:83], a[216:219], a[4:7]\n"
  "s_waitcnt lgkmcnt(9)\n"
  "v_exp_f32 v197, v197\n"
  "v_mfma_f32_16x16x32_bf16 a[4:7], v[156:159], a[212:215], a[4:7]\n"
  "s_waitcnt lgkmcnt(7)\n"
  "v_exp_f32 v198, v198\n"
  "v_mfma_f32_16x16x32_bf16 a[4:7], v[84:87], a[220:223], a[4:7]\n"
  "v_exp_f32 v199, v199\n"
  "v_mfma_f32_16x16x32_bf16 v[188:191], a[176:179], v[32:35], v[208:211]\n"
  "ds_read_b64_tr_b16 a[200:201], v246 offset:0\n"
  "v_cvt_pk_bf16_f32 v140, v192, v193\n"
  "v_mfma_f32_16x16x32_bf16 v[188:191], a[180:183], v[36:39], v[188:191]\n"
  "ds_read_b64_tr_b16 a[202:203], v246 offset:2048\n"
  "v_cvt_pk_bf16_f32 v141, v194, v195\n"
  "v_mfma_f32_16x16x32_bf16 v[188:191], a[184:187], v[40:43], v[188:191]\n"
  "ds_read_b64_tr_b16 a[192:193], v238 offset:16384\n"
  "v_cvt_pk_bf16_f32 v142, v196, v197\n"
  "v_mfma_f32_16x16x32_bf16 v[188:191], a[188:191], v[44:47], v[188:191]\n"
  "ds_read_b64_tr_b16 a[194:195], v238 offset:18432\n"
  "v_cvt_pk_bf16_f32 v143, v198, v199\n"
  "v_mfma_f32_16x16x32_bf16 v[192:195], a[188:191], v[28:31], v[204:207]\n"
  "ds_read_b128 a[172:175], v235 offset:16384\n"
  "v_xor_b32 v246, 0x8000, v246\n"
  "v_mfma_f32_16x16x32_bf16 v[192:195], a[184:187], v[24:27], v[192:195]\n"
  "v_xor_b32 v238, 0x8000, v238\n"
  "v_mfma_f32_16x16x32_bf16 v[192:195], a[180:183], v[20:23], v[192:195]\n"
  "v_mfma_f32_16x16x32_bf16 v[192:195], a[176:179], v[16:19], v[192:195]\n"
  "buffer_load_dwordx4 v255, s[48:51], s72 offen offset:1024 nt lds\n"
  "v_mfma_f32_16x16x32_bf16 a[36:39], v[160:163], a[208:211], a[36:39]\n"
  "v_exp_f32 v188, v188\n"
  "v_mfma_f32_16x16x32_bf16 a[36:39], v[164:167], a[212:215], a[36:39]\n"
  "v_exp_f32 v189, v189\n"
  "v_mfma_f32_16x16x32_bf16 a[36:39], v[88:91], a[216:219], a[36:39]\n"
  "v_exp_f32 v190, v190\n"
  "v_mfma_f32_16x16x32_bf16 a[36:39], v[92:95], a[220:223], a[36:39]\n"
  "v_exp_f32 v191, v191\n"
  "v_mfma_f32_16x16x32_bf16 a[68:71], v[100:103], a[220:223], a[68:71]\n"
  "v_exp_f32 v192, v192\n"
  "v_mfma_f32_16x16x32_bf16 a[68:71], v[96:99], a[216:219], a[68:71]\n"
  "v_exp_f32 v193, v193\n"
  "v_mfma_f32_16x16x32_bf16 a[68:71], v[168:171], a[208:211], a[68:71]\n"
  "v_exp_f32 v194, v194\n"
  "v_mfma_f32_16x16x32_bf16 a[68:71], v[172:175], a[212:215], a[68:71]\n"
  "v_exp_f32 v195, v195\n"
  "v_smfmac_f32_16x16x64_bf16 a[228:231], a[240:243], v[88:95], v224\n"
  "v_cvt_pk_bf16_f32 v134, v188, v189\n"
  "v_mfma_f32_16x16x32_bf16 v[196:199], a[176:179], v[0:3], v[200:203]\n"
  "v_cvt_pk_bf16_f32 v135, v190, v191\n"
  "v_cvt_pk_bf16_f32 v126, v192, v193\n"
  "v_mfma_f32_16x16x32_bf16 v[196:199], a[180:183], v[4:7], v[196:199]\n"
  "v_cvt_pk_bf16_f32 v127, v194, v195\n"
  "v_mfma_f32_16x16x32_bf16 v[196:199], a[184:187], v[8:11], v[196:199]\n"
  "v_mfma_f32_16x16x32_bf16 v[196:199], a[188:191], v[12:15], v[196:199]\n"
  "v_mfma_f32_16x16x32_bf16 v[188:191], a[188:191], v[76:79], v[216:219]\n"
  "ds_read_b128 a[188:191], v235 offset:18432\n"
  "v_mfma_f32_16x16x32_bf16 v[188:191], a[184:187], v[72:75], v[188:191]\n"
  "ds_read_b128 a[184:187], v234 offset:18432\n"
  "v_mfma_f32_16x16x32_bf16 v[188:191], a[180:183], v[68:71], v[188:191]\n"
  "v_mfma_f32_16x16x32_bf16 v[188:191], a[176:179], v[64:67], v[188:191]\n"
  "buffer_load_dwordx4 v254, s[48:51], s72 offen offset:2048 nt lds\n"
  "v_mfma_f32_16x16x32_bf16 a[100:103], v[176:179], a[208:211], a[100:103]\n"
  "v_exp_f32 v196, v196\n"
  "v_mfma_f32_16x16x32_bf16 a[100:103], v[180:183], a[212:215], a[100:103]\n"
  "v_exp_f32 v197, v197\n"
  "v_mfma_f32_16x16x32_bf16 a[100:103], v[104:107], a[216:219], a[100:103]\n"
  "v_exp_f32 v198, v198\n"
  "v_mfma_f32_16x16x32_bf16 a[100:103], v[108:111], a[220:223], a[100:103]\n"
  "s_waitcnt lgkmcnt(12)\n"
  "v_exp_f32 v199, v199\n"
  "v_mfma_f32_16x16x32_bf16 a[104:107], v[108:111], a[204:207], a[104:107]\n"
  "s_waitcnt lgkmcnt(5)\n"
  "v_exp_f32 v188, v188\n"
  "v_mfma_f32_16x16x32_bf16 a[104:107], v[104:107], a[200:203], a[104:107]\n"
  "s_waitcnt lgkmcnt(3)\n"
  "v_exp_f32 v189, v189\n"
  "v_mfma_f32_16x16x32_bf16 a[104:107], v[176:179], a[192:195], a[104:107]\n"
  "v_exp_f32 v190, v190\n"
  "v_mfma_f32_16x16x32_bf16 a[104:107], v[180:183], a[196:199], a[104:107]\n"
  "v_exp_f32 v191, v191\n"
  "v_mfma_f32_16x16x32_bf16 v[192:195], a[160:163], v[0:3], v[200:203]\n"
  "v_cvt_pk_bf16_f32 v118, v196, v197\n"
  "v_cvt_pk_bf16_f32 v119, v198, v199\n"
  "v_mfma_f32_16x16x32_bf16 v[192:195], a[164:167], v[4:7], v[192:195]\n"
  "v_cvt_pk_bf16_f32 v150, v188, v189\n"
  "v_cvt_pk_bf16_f32 v151, v190, v191\n"
  "v_mfma_f32_16x16x32_bf16 v[192:195], a[168:171], v[8:11], v[192:195]\n"
  "s_waitcnt lgkmcnt(2)\n"
  "ds_read_b64_tr_b16 a[216:217], v247 offset:0\n"
  "v_add_f32 v220, v188, v220\n"
  "v_mfma_f32_16x16x32_bf16 v[192:195], a[172:175], v[12:15], v[192:195]\n"
  "ds_read_b64_tr_b16 a[218:219], v247 offset:2048\n"
  "v_add_f32 v221, v189, v221\n"
  "v_mfma_f32_16x16x32_bf16 v[196:199], a[172:175], v[28:31], v[204:207]\n"
  "ds_read_b64_tr_b16 a[208:209], v239 offset:16384\n"
  "v_add_f32 v220, v190, v220\n"
  "v_mfma_f32_16x16x32_bf16 v[196:199], a[168:171], v[24:27], v[196:199]\n"
  "ds_read_b64_tr_b16 a[210:211], v239 offset:18432\n"
  "v_add_f32 v221, v191, v221\n"
  "v_mfma_f32_16x16x32_bf16 v[196:199], a[164:167], v[20:23], v[196:199]\n"
  "ds_read_b128 a[176:179], v232 offset:18432\n"
  "v_xor_b32 v247, 0x8000, v247\n"
  "v_mfma_f32_16x16x32_bf16 v[196:199], a[160:163], v[16:19], v[196:199]\n"
  "buffer_load_dwordx4 v255, s[48:51], s72 offen offset:3072 nt lds\n"
  "v_mfma_f32_16x16x32_bf16 a[72:75], v[168:171], a[192:195], a[72:75]\n"
  "s_add_u32 s72, s72, s74\n"
  "v_exp_f32 v192, v192\n"
  "v_mfma_f32_16x16x32_bf16 a[72:75], v[172:175], a[196:199], a[72:75]\n"
  "s_and_b32 s72, s72, s9\n"
  "v_exp_f32 v193, v193\n"
  "v_mfma_f32_16x16x32_bf16 a[72:75], v[96:99], a[200:203], a[72:75]\n"
  "s_xor_b32 s16, s16, 0x8000\n"
  "v_exp_f32 v194, v194\n"
  "v_mfma_f32_16x16x32_bf16 a[72:75], v[100:103], a[204:207], a[72:75]\n"
  "s_xor_b32 s17, s17, 0x8000\n"
  "v_exp_f32 v195, v195\n"
  "v_mfma_f32_16x16x32_bf16 a[40:43], v[92:95], a[204:207], a[40:43]\n"
  "v_exp_f32 v196, v196\n"
  "v_mfma_f32_16x16x32_bf16 a[40:43], v[88:91], a[200:203], a[40:43]\n"
  "v_exp_f32 v197, v197\n"
  "v_mfma_f32_16x16x32_bf16 a[40:43], v[160:163], a[192:195], a[40:43]\n"
  "v_exp_f32 v198, v198\n"
  "v_mfma_f32_16x16x32_bf16 a[40:43], v[164:167], a[196:199], a[40:43]\n"
  "v_exp_f32 v199, v199\n"
  "v_smfmac_f32_16x16x64_bf16 a[232:235], a[240:243], v[96:103], v224\n"
  "v_cvt_pk_bf16_f32 v120, v192, v193\n"
  "v_mfma_f32_16x16x32_bf16 v[188:191], a[160:163], v[32:35], v[208:211]\n"
  "ds_read_b128 a[180:183], v233 offset:18432\n"
  "v_cvt_pk_bf16_f32 v121, v194, v195\n"
  "v_mfma_f32_16x16x32_bf16 v[188:191], a[164:167], v[36:39], v[188:191]\n"
  "ds_read_b64_tr_b16 a[212:213], v243 offset:16384\n"
  "v_cvt_pk_bf16_f32 v128, v196, v197\n"
  "v_mfma_f32_16x16x32_bf16 v[188:191], a[168:171], v[40:43], v[188:191]\n"
  "ds_read_b64_tr_b16 a[214:215], v243 offset:18432\n"
  "v_cvt_pk_bf16_f32 v129, v198, v199\n"
  "v_mfma_f32_16x16x32_bf16 v[188:191], a[172:175], v[44:47], v[188:191]\n"
  "ds_read_b64_tr_b16 a[220:221], v251 offset:0\n"
  "v_xor_b32 v239, 0x8000, v239\n"
  "v_mfma_f32_16x16x32_bf16 v[192:195], a[172:175], v[60:63], v[212:215]\n"
  "ds_read_b64_tr_b16 a[222:223], v251 offset:2048\n"
  "v_xor_b32 v243, 0x8000, v243\n"
  "v_mfma_f32_16x16x32_bf16 v[192:195], a[168:171], v[56:59], v[192:195]\n"
  "v_xor_b32 v251, 0x8000, v251\n"
  "v_mfma_f32_16x16x32_bf16 v[192:195], a[164:167], v[52:55], v[192:195]\n"
  "v_mfma_f32_16x16x32_bf16 v[192:195], a[160:163], v[48:51], v[192:195]\n"
  "v_mfma_f32_16x16x32_bf16 a[8:11], v[156:159], a[196:199], a[8:11]\n"
  "v_exp_f32 v188, v188\n"
  "v_mfma_f32_16x16x32_bf16 a[8:11], v[84:87], a[204:207], a[8:11]\n"
  "v_exp_f32 v189, v189\n"
  "v_mfma_f32_16x16x32_bf16 a[8:11], v[152:155], a[192:195], a[8:11]\n"
  "v_exp_f32 v190, v190\n"
  "v_mfma_f32_16x16x32_bf16 a[8:11], v[80:83], a[200:203], a[8:11]\n"
  "v_exp_f32 v191, v191\n"
  "v_mfma_f32_16x16x32_bf16 a[136:139], v[184:187], a[192:195], a[136:139]\n"
  "v_exp_f32 v192, v192\n"
  "v_mfma_f32_16x16x32_bf16 a[136:139], v[112:115], a[200:203], a[136:139]\n"
  "s_waitcnt lgkmcnt(8)\n"
  "v_exp_f32 v193, v193\n"
  "v_mfma_f32_16x16x32_bf16 a[140:143], v[112:115], a[216:219], a[140:143]\n"
  "s_waitcnt lgkmcnt(6)\n"
  "v_exp_f32 v194, v194\n"
  "v_mfma_f32_16x16x32_bf16 a[140:143], v[184:187], a[208:211], a[140:143]\n"
  "v_exp_f32 v195, v195\n"
  "v_smfmac_f32_16x16x64_bf16 a[236:239], a[240:243], v[104:111], v224\n"
  "s_waitcnt vmcnt(8) lgkmcnt(0)\n"
  "s_barrier\n"
  "v_mfma_f32_16x16x32_bf16 v[196:199], a[176:179], v[0:3], v[200:203]\n"
  "ds_read_b128 a[160:163], v228 offset:0\n"
  "v_cvt_pk_bf16_f32 v136, v188, v189\n"
  "v_mfma_f32_16x16x32_bf16 v[196:199], a[180:183], v[4:7], v[196:199]\n"
  "ds_read_b128 a[164:167], v229 offset:0\n"
  "v_cvt_pk_bf16_f32 v137, v190, v191\n"
  "v_mfma_f32_16x16x32_bf16 v[196:199], a[184:187], v[8:11], v[196:199]\n"
  "ds_read_b128 a[168:171], v230 offset:0\n"
  "v_cvt_pk_bf16_f32 v144, v192, v193\n"
  "v_mfma_f32_16x16x32_bf16 v[196:199], a[188:191], v[12:15], v[196:199]\n"
  "ds_read_b128 a[172:175], v231 offset:0\n"
  "v_cvt_pk_bf16_f32 v145, v194, v195\n"
  "v_mfma_f32_16x16x32_bf16 v[188:191], a[188:191], v[28:31], v[204:207]\n"
  "ds_read_b64_tr_b16 a[192:193], v244 offset:8192\n"
  "v_mfma_f32_16x16x32_bf16 v[188:191], a[184:187], v[24:27], v[188:191]\n"
  "ds_read_b64_tr_b16 a[194:195], v244 offset:10240\n"
  "v_mfma_f32_16x16x32_bf16 v[188:191], a[180:183], v[20:23], v[188:191]\n"
  "s_add_u32 m0, s67, 131072\n"
  "ds_read_b64_tr_b16 a[196:197], v248 offset:8192\n"
  "v_mfma_f32_16x16x32_bf16 v[188:191], a[176:179], v[16:19], v[188:191]\n"
  "buffer_load_dwordx4 v252, s[44:47], s71 offen nt lds\n"
  "v_mfma_f32_16x16x32_bf16 a[12:15], v[152:155], a[208:211], a[12:15]\n"
  "v_exp_f32 v196, v196\n"
  "v_mfma_f32_16x16x32_bf16 a[12:15], v[156:159], a[212:215], a[12:15]\n"
  "v_exp_f32 v197, v197\n"
  "v_mfma_f32_16x16x32_bf16 a[12:15], v[80:83], a[216:219], a[12:15]\n"
  "v_exp_f32 v198, v198\n"
  "v_mfma_f32_16x16x32_bf16 a[12:15], v[84:87], a[220:223], a[12:15]\n"
  "v_exp_f32 v199, v199\n"
  "v_mfma_f32_16x16x32_bf16 a[44:47], v[92:95], a[220:223], a[44:47]\n"
  "v_exp_f32 v188, v188\n"
  "v_mfma_f32_16x16x32_bf16 a[44:47], v[88:91], a[216:219], a[44:47]\n"
  "v_exp_f32 v189, v189\n"
  "v_mfma_f32_16x16x32_bf16 a[44:47], v[160:163], a[208:211], a[44:47]\n"
  "v_exp_f32 v190, v190\n"
  "v_mfma_f32_16x16x32_bf16 a[44:47], v[164:167], a[212:215], a[44:47]\n"
  "v_exp_f32 v191, v191\n"
  "v_mfma_f32_16x16x32_bf16 v[192:195], a[176:179], v[32:35], v[208:211]\n"
  "ds_read_b64_tr_b16 a[198:199], v248 offset:10240\n"
  "v_cvt_pk_bf16_f32 v122, v196, v197\n"
  "v_mfma_f32_16x16x32_bf16 v[192:195], a[180:183], v[36:39], v[192:195]\n"
  "ds_read_b64_tr_b16 a[200:201], v244 offset:24576\n"
  "v_cvt_pk_bf16_f32 v123, v198, v199\n"
  "v_mfma_f32_16x16x32_bf16 v[192:195], a[184:187], v[40:43], v[192:195]\n"
  "ds_read_b64_tr_b16 a[202:203], v244 offset:26624\n"
  "v_cvt_pk_bf16_f32 v130, v188, v189\n"
  "v_mfma_f32_16x16x32_bf16 v[192:195], a[188:191], v[44:47], v[192:195]\n"
  "ds_read_b64_tr_b16 a[204:205], v248 offset:24576\n"
  "v_cvt_pk_bf16_f32 v131, v190, v191\n"
  "v_mfma_f32_16x16x32_bf16 v[196:199], a[188:191], v[60:63], v[212:215]\n"
  "ds_read_b64_tr_b16 a[206:207], v248 offset:26624\n"
  "v_xor_b32 v244, 0x8000, v244\n"
  "v_mfma_f32_16x16x32_bf16 v[196:199], a[184:187], v[56:59], v[196:199]\n"
  "ds_read_b128 a[188:191], v231 offset:2048\n"
  "v_xor_b32 v248, 0x8000, v248\n"
  "v_mfma_f32_16x16x32_bf16 v[196:199], a[180:183], v[52:55], v[196:199]\n"
  "ds_read_b128 a[184:187], v230 offset:2048\n"
  "v_mfma_f32_16x16x32_bf16 v[196:199], a[176:179], v[48:51], v[196:199]\n"
  "buffer_load_dwordx4 v253, s[44:47], s71 offen offset:1024 nt lds\n"
  "v_mfma_f32_16x16x32_bf16 a[76:79], v[168:171], a[208:211], a[76:79]\n"
  "s_add_u32 s75, s75, 1\n"
  "v_exp_f32 v192, v192\n"
  "v_mfma_f32_16x16x32_bf16 a[76:79], v[172:175], a[212:215], a[76:79]\n"
  "v_exp_f32 v193, v193\n"
  "v_mfma_f32_16x16x32_bf16 a[76:79], v[96:99], a[216:219], a[76:79]\n"
  "v_exp_f32 v194, v194\n"
  "v_mfma_f32_16x16x32_bf16 a[76:79], v[100:103], a[220:223], a[76:79]\n"
  "s_cmp_lg_u32 s75, 5\n"
  "v_exp_f32 v195, v195\n"
  "v_mfma_f32_16x16x32_bf16 a[108:111], v[108:111], a[220:223], a[108:111]\n"
  "v_exp_f32 v196, v196\n"
  "v_mfma_f32_16x16x32_bf16 a[108:111], v[104:107], a[216:219], a[108:111]\n"
  "v_exp_f32 v197, v197\n"
  "v_mfma_f32_16x16x32_bf16 a[108:111], v[176:179], a[208:211], a[108:111]\n"
  "v_exp_f32 v198, v198\n"
  "v_mfma_f32_16x16x32_bf16 a[108:111], v[180:183], a[212:215], a[108:111]\n"
  "v_exp_f32 v199, v199\n"
  "s_cbranch_scc1 L_loop_%=\n"
  "L_tail_%=:\n"
  "s_waitcnt lgkmcnt(8)\n"
  "v_mfma_f32_16x16x32_bf16 a[16:19], v[80:83], a[192:195], a[16:19]\n"
  "s_waitcnt lgkmcnt(6)\n"
  "s_mov_b32 s70, 458752\n"
  "ds_read_b64_tr_b16 a[208:209], v237 offset:8192\n"
  "v_cvt_pk_bf16_f32 v138, v192, v193\n"
  "v_mfma_f32_16x16x32_bf16 a[16:19], v[84:87], a[196:199], a[16:19]\n"
  "s_waitcnt lgkmcnt(5)\n"
  "ds_read_b64_tr_b16 a[210:211], v237 offset:10240\n"
  "v_cvt_pk_bf16_f32 v139, v194, v195\n"
  "v_mfma_f32_16x16x32_bf16 a[16:19], v[116:119], a[200:203], a[16:19]\n"
  "s_waitcnt lgkmcnt(4)\n"
  "ds_read_b64_tr_b16 a[212:213], v241 offset:8192\n"
  "v_cvt_pk_bf16_f32 v146, v196, v197\n"
  "v_mfma_f32_16x16x32_bf16 a[16:19], v[120:123], a[204:207], a[16:19]\n"
  "ds_read_b64_tr_b16 a[214:215], v241 offset:10240\n"
  "v_cvt_pk_bf16_f32 v147, v198, v199\n"
  "v_mfma_f32_16x16x32_bf16 a[48:51], v[128:131], a[204:207], a[48:51]\n"
  "ds_read_b64_tr_b16 a[216:217], v237 offset:24576\n"
  "v_accvgpr_read_b32 v200, a244\n"
  "v_mfma_f32_16x16x32_bf16 a[48:51], v[124:127], a[200:203], a[48:51]\n"
  "ds_read_b64_tr_b16 a[218:219], v237 offset:26624\n"
  "v_xor_b32 v201, 32, v200\n"
  "v_mfma_f32_16x16x32_bf16 a[48:51], v[92:95], a[196:199], a[48:51]\n"
  "ds_read_b64_tr_b16 a[220:221], v241 offset:24576\n"
  "v_add_u32 v202, 65536, v200\n"
  "v_mfma_f32_16x16x32_bf16 a[48:51], v[88:91], a[192:195], a[48:51]\n"
  "ds_read_b64_tr_b16 a[222:223], v241 offset:26624\n"
  "v_add_u32 v203, 65536, v201\n"
  "v_mfma_f32_16x16x32_bf16 a[80:83], v[96:99], a[192:195], a[80:83]\n"
  "buffer_load_dwordx4 v252, s[44:47], s71 offen offset:2048 nt lds\n"
  "v_mfma_f32_16x16x32_bf16 a[80:83], v[100:103], a[196:199], a[80:83]\n"
  "ds_read_b64_tr_b16 v[0:1], v202 offset:0\n"
  "v_add_u32 v204, 131072, v200\n"
  "v_mfma_f32_16x16x32_bf16 a[80:83], v[132:135], a[200:203], a[80:83]\n"
  "ds_read_b64_tr_b16 v[4:5], v203 offset:0\n"
  "v_add_u32 v205, 131072, v201\n"
  "v_mfma_f32_16x16x32_bf16 a[80:83], v[136:139], a[204:207], a[80:83]\n"
  "ds_read_b64_tr_b16 v[8:9], v202 offset:256\n"
  "v_accvgpr_read_b32 v206, a245\n"
  "v_mfma_f32_16x16x32_bf16 a[112:115], v[144:147], a[204:207], a[112:115]\n"
  "ds_read_b64_tr_b16 v[12:13], v203 offset:256\n"
  "v_accvgpr_read_b32 v210, a246\n"
  "v_mfma_f32_16x16x32_bf16 a[112:115], v[140:143], a[200:203], a[112:115]\n"
  "buffer_load_dwordx4 v253, s[44:47], s71 offen offset:3072 nt lds\n"
  "s_add_u32 m0, s67, 16384\n"
  "v_mfma_f32_16x16x32_bf16 a[112:115], v[108:111], a[196:199], a[112:115]\n"
  "ds_read_b64_tr_b16 v[16:17], v202 offset:512\n"
  "v_accvgpr_read_b32 v214, a248\n"
  "v_mfma_f32_16x16x32_bf16 a[112:115], v[104:107], a[192:195], a[112:115]\n"
  "ds_read_b64_tr_b16 v[20:21], v203 offset:512\n"
  "v_accvgpr_read_b32 v215, a250\n"
  "v_mfma_f32_16x16x32_bf16 a[144:147], v[112:115], a[192:195], a[144:147]\n"
  "ds_read_b64_tr_b16 v[24:25], v202 offset:768\n"
  "v_accvgpr_read_b32 v216, a251\n"
  "v_mfma_f32_16x16x32_bf16 a[144:147], v[148:151], a[200:203], a[144:147]\n"
  "ds_read_b64_tr_b16 v[28:29], v203 offset:768\n"
  "v_xor_b32 v207, 16, v206\n"
  "v_smfmac_f32_16x16x64_bf16 a[224:227], a[240:243], v[116:123], v224\n"
  "v_xor_b32 v208, 32, v206\n"
  "v_smfmac_f32_16x16x64_bf16 a[228:231], a[240:243], v[124:131], v224\n"
  "v_xor_b32 v209, 48, v206\n"
  "v_smfmac_f32_16x16x64_bf16 a[232:235], a[240:243], v[132:139], v224\n"
  "v_lshrrev_b32 v211, 1, v214\n"
  "v_smfmac_f32_16x16x64_bf16 a[236:239], a[240:243], v[140:147], v224\n"
  "s_waitcnt lgkmcnt(14)\n"
  "s_add_u32 s77, s67, 32768\n"
  "v_add_u32 v211, s77, v211\n"
  "v_mfma_f32_16x16x32_bf16 a[20:23], v[80:83], a[208:211], a[20:23]\n"
  "s_waitcnt lgkmcnt(12)\n"
  "s_add_u32 s77, s66, 57344\n"
  "ds_read_b64_tr_b16 a[192:193], v238 offset:8192\n"
  "v_add_u32 v212, s77, v214\n"
  "v_mfma_f32_16x16x32_bf16 a[20:23], v[84:87], a[212:215], a[20:23]\n"
  "s_waitcnt lgkmcnt(11)\n"
  "s_lshl2_add_u32 s77, s64, 57344\n"
  "ds_read_b64_tr_b16 a[194:195], v238 offset:10240\n"
  "v_mov_b32 v213, s77\n"
  "v_mfma_f32_16x16x32_bf16 a[20:23], v[116:119], a[216:219], a[20:23]\n"
  "s_waitcnt lgkmcnt(10)\n"
  "ds_read_b64_tr_b16 a[196:197], v242 offset:8192\n"
  "v_xor_b32 v194, 4096, v211\n"
  "v_mfma_f32_16x16x32_bf16 a[20:23], v[120:123], a[220:223], a[20:23]\n"
  "ds_read_b64_tr_b16 a[198:199], v242 offset:10240\n"
  "v_xor_b32 v195, 1056, v212\n"
  "v_mfma_f32_16x16x32_bf16 a[52:55], v[128:131], a[220:223], a[52:55]\n"
  "buffer_load_dwordx4 v252, s[60:63], s70 offen lds\n"
  "v_mfma_f32_16x16x32_bf16 a[52:55], v[124:127], a[216:219], a[52:55]\n"
  "ds_read_b64_tr_b16 a[200:201], v238 offset:24576\n"
  "v_add_f32 v173, v220, v221\n"
  "v_mfma_f32_16x16x32_bf16 a[52:55], v[92:95], a[212:215], a[52:55]\n"
  "ds_read_b64_tr_b16 a[202:203], v238 offset:26624\n"
  "v_mov_b32 v174, v173\n"
  "v_mfma_f32_16x16x32_bf16 a[52:55], v[88:91], a[208:211], a[52:55]\n"
  "ds_read_b64_tr_b16 a[204:205], v242 offset:24576\n"
  "v_permlane32_swap_b32 v174, v173\n"
  "v_mfma_f32_16x16x32_bf16 a[84:87], v[96:99], a[208:211], a[84:87]\n"
  "ds_read_b64_tr_b16 a[206:207], v242 offset:26624\n"
  "v_add_f32 v173, v173, v174\n"
  "v_mfma_f32_16x16x32_bf16 a[84:87], v[100:103], a[212:215], a[84:87]\n"
  "buffer_load_dwordx4 v253, s[60:63], s70 offen offset:1024 lds\n"
  "v_mfma_f32_16x16x32_bf16 a[84:87], v[132:135], a[216:219], a[84:87]\n"
  "ds_read_b64_tr_b16 v[2:3], v202 offset:16384\n"
  "v_mov_b32 v174, v173\n"
  "v_mfma_f32_16x16x32_bf16 a[84:87], v[136:139], a[220:223], a[84:87]\n"
  "ds_read_b64_tr_b16 v[6:7], v203 offset:16384\n"
  "v_permlane16_swap_b32 v174, v173\n"
  "v_mfma_f32_16x16x32_bf16 a[116:119], v[144:147], a[220:223], a[116:119]\n"
  "ds_read_b64_tr_b16 v[10:11], v202 offset:16640\n"
  "v_add_f32 v173, v173, v174\n"
  "v_mfma_f32_16x16x32_bf16 a[116:119], v[140:143], a[216:219], a[116:119]\n"
  "ds_read_b64_tr_b16 v[14:15], v203 offset:16640\n"
  "v_accvgpr_read_b32 v169, a224\n"
  "v_mfma_f32_16x16x32_bf16 a[116:119], v[108:111], a[212:215], a[116:119]\n"
  "buffer_load_dwordx4 v252, s[60:63], s70 offen offset:2048 lds\n"
  "v_mfma_f32_16x16x32_bf16 a[116:119], v[104:107], a[208:211], a[116:119]\n"
  "ds_read_b64_tr_b16 v[18:19], v202 offset:16896\n"
  "v_accvgpr_read_b32 v174, a225\n"
  "v_mfma_f32_16x16x32_bf16 a[148:151], v[112:115], a[208:211], a[148:151]\n"
  "ds_read_b64_tr_b16 v[22:23], v203 offset:16896\n"
  "v_add_f32 v169, v169, v174\n"
  "v_mfma_f32_16x16x32_bf16 a[148:151], v[148:151], a[216:219], a[148:151]\n"
  "s_waitcnt lgkmcnt(12)\n"
  "buffer_load_dwordx4 v253, s[60:63], s70 offen offset:3072 lds\n"
  "s_add_u32 s70, s70, 65536\n"
  "v_mfma_f32_16x16x32_bf16 a[24:27], v[80:83], a[192:195], a[24:27]\n"
  "s_waitcnt lgkmcnt(10)\n"
  "s_add_u32 m0, s67, 65536\n"
  "ds_read_b64_tr_b16 a[208:209], v239 offset:8192\n"
  "v_accvgpr_read_b32 v170, a228\n"
  "v_mfma_f32_16x16x32_bf16 a[24:27], v[84:87], a[196:199], a[24:27]\n"
  "s_waitcnt lgkmcnt(9)\n"
  "ds_read_b64_tr_b16 a[210:211], v239 offset:10240\n"
  "v_accvgpr_read_b32 v174, a229\n"
  "v_mfma_f32_16x16x32_bf16 a[24:27], v[116:119], a[200:203], a[24:27]\n"
  "s_waitcnt lgkmcnt(8)\n"
  "ds_read_b64_tr_b16 a[212:213], v243 offset:8192\n"
  "v_add_f32 v170, v170, v174\n"
  "v_mfma_f32_16x16x32_bf16 a[24:27], v[120:123], a[204:207], a[24:27]\n"
  "ds_read_b64_tr_b16 a[214:215], v243 offset:10240\n"
  "v_accvgpr_read_b32 v171, a232\n"
  "v_mfma_f32_16x16x32_bf16 a[56:59], v[128:131], a[204:207], a[56:59]\n"
  "ds_read_b64_tr_b16 a[216:217], v239 offset:24576\n"
  "v_accvgpr_read_b32 v174, a233\n"
  "v_mfma_f32_16x16x32_bf16 a[56:59], v[124:127], a[200:203], a[56:59]\n"
  "ds_read_b64_tr_b16 a[218:219], v239 offset:26624\n"
  "v_add_f32 v171, v171, v174\n"
  "v_mfma_f32_16x16x32_bf16 a[56:59], v[92:95], a[196:199], a[56:59]\n"
  "ds_read_b64_tr_b16 a[220:221], v243 offset:24576\n"
  "v_accvgpr_read_b32 v172, a236\n"
  "v_mfma_f32_16x16x32_bf16 a[56:59], v[88:91], a[192:195], a[56:59]\n"
  "ds_read_b64_tr_b16 a[222:223], v243 offset:26624\n"
  "v_accvgpr_read_b32 v174, a237\n"
  "v_mfma_f32_16x16x32_bf16 a[88:91], v[96:99], a[192:195], a[88:91]\n"
  "ds_read_b64_tr_b16 v[26:27], v202 offset:17152\n"
  "v_add_f32 v172, v172, v174\n"
  "v_mfma_f32_16x16x32_bf16 a[88:91], v[100:103], a[196:199], a[88:91]\n"
  "ds_read_b64_tr_b16 v[30:31], v203 offset:17152\n"
  "v_add_f32 v193, v169, v170\n"
  "v_mfma_f32_16x16x32_bf16 a[88:91], v[132:135], a[200:203], a[88:91]\n"
  "v_rcp_f32 v188, v169\n"
  "v_mfma_f32_16x16x32_bf16 a[88:91], v[136:139], a[204:207], a[88:91]\n"
  "ds_read_b64_tr_b16 a[160:161], v236 offset:16384\n"
  "v_add_f32 v174, v171, v172\n"
  "v_mfma_f32_16x16x32_bf16 a[120:123], v[144:147], a[204:207], a[120:123]\n"
  "ds_read_b64_tr_b16 a[162:163], v236 offset:18432\n"
  "v_add_f32 v193, v193, v173\n"
  "v_mfma_f32_16x16x32_bf16 a[120:123], v[140:143], a[200:203], a[120:123]\n"
  "ds_read_b64_tr_b16 a[164:165], v240 offset:16384\n"
  "v_add_f32 v193, v193, v174\n"
  "v_mfma_f32_16x16x32_bf16 a[120:123], v[108:111], a[196:199], a[120:123]\n"
  "ds_read_b64_tr_b16 a[166:167], v240 offset:18432\n"
  "v_cmp_nge_f32 vcc, 0x57800000, v193\n"
  "v_mfma_f32_16x16x32_bf16 a[120:123], v[104:107], a[192:195], a[120:123]\n"
  "v_rcp_f32 v189, v170\n"
  "v_mfma_f32_16x16x32_bf16 a[152:155], v[112:115], a[192:195], a[152:155]\n"
  "s_waitcnt lgkmcnt(4)\n"
  "s_barrier\n"
  "v_mfma_f32_16x16x32_bf16 a[152:155], v[148:151], a[200:203], a[152:155]\n"
  "ds_read_b64_tr_b16 a[168:169], v237 offset:16384\n"
  "v_mov_b32 v219, 0x3fff0000\n"
  "v_mfma_f32_16x16x32_bf16 a[28:31], v[80:83], a[208:211], a[28:31]\n"
  "s_or_b32 s77, vcc_lo, vcc_hi\n"
  "ds_read_b64_tr_b16 a[170:171], v237 offset:18432\n"
  "v_mov_b32 v193, s77\n"
  "v_mfma_f32_16x16x32_bf16 a[28:31], v[84:87], a[212:215], a[28:31]\n"
  "buffer_load_dwordx4 v252, s[60:63], s70 offen lds\n"
  "v_mfma_f32_16x16x32_bf16 a[28:31], v[116:119], a[216:219], a[28:31]\n"
  "ds_read_b64_tr_b16 a[172:173], v241 offset:16384\n"
  "v_mfma_f32_16x16x32_bf16 a[28:31], v[120:123], a[220:223], a[28:31]\n"
  "ds_read_b64_tr_b16 a[174:175], v241 offset:18432\n"
  "v_mfma_f32_16x16x32_bf16 a[60:63], v[128:131], a[220:223], a[60:63]\n"
  "ds_read_b64_tr_b16 a[176:177], v238 offset:16384\n"
  "v_mfma_f32_16x16x32_bf16 a[60:63], v[124:127], a[216:219], a[60:63]\n"
  "buffer_load_dwordx4 v253, s[60:63], s70 offen offset:1024 lds\n"
  "v_mfma_f32_16x16x32_bf16 a[60:63], v[92:95], a[212:215], a[60:63]\n"
  "ds_read_b64_tr_b16 a[178:179], v238 offset:18432\n"
  "v_mfma_f32_16x16x32_bf16 a[60:63], v[88:91], a[208:211], a[60:63]\n"
  "ds_read_b64_tr_b16 a[180:181], v242 offset:16384\n"
  "v_mfma_f32_16x16x32_bf16 a[156:159], v[112:115], a[208:211], a[156:159]\n"
  "ds_read_b64_tr_b16 a[182:183], v242 offset:18432\n"
  "v_mfma_f32_16x16x32_bf16 a[156:159], v[148:151], a[216:219], a[156:159]\n"
  "s_waitcnt lgkmcnt(10)\n"
  "buffer_load_dwordx4 v252, s[60:63], s70 offen offset:2048 lds\n"
  "v_mfma_f32_16x16x32_bf16 a[0:3], v[116:119], a[160:163], a[0:3]\n"
  "s_waitcnt lgkmcnt(8)\n"
  "ds_read_b64_tr_b16 a[184:185], v239 offset:16384\n"
  "v_mfma_f32_16x16x32_bf16 a[0:3], v[120:123], a[164:167], a[0:3]\n"
  "s_waitcnt lgkmcnt(7)\n"
  "ds_read_b64_tr_b16 a[186:187], v239 offset:18432\n"
  "v_mfma_f32_16x16x32_bf16 a[4:7], v[116:119], a[168:171], a[4:7]\n"
  "s_waitcnt lgkmcnt(6)\n"
  "ds_read_b64_tr_b16 a[188:189], v243 offset:16384\n"
  "v_mfma_f32_16x16x32_bf16 a[4:7], v[120:123], a[172:175], a[4:7]\n"
  "s_waitcnt lgkmcnt(5)\n"
  "buffer_load_dwordx4 v253, s[60:63], s70 offen offset:3072 lds\n"
  "s_add_u32 s70, s70, 65536\n"
  "v_mfma_f32_16x16x32_bf16 a[8:11], v[116:119], a[176:179], a[8:11]\n"
  "s_waitcnt lgkmcnt(3)\n"
  "s_add_u32 m0, s67, 81920\n"
  "ds_read_b64_tr_b16 a[190:191], v243 offset:18432\n"
  "v_mfma_f32_16x16x32_bf16 a[8:11], v[120:123], a[180:183], a[8:11]\n"
  "s_waitcnt lgkmcnt(2)\n"
  "ds_bpermute_b32 v228, v210, v188 offset:0\n"
  "v_mfma_f32_16x16x32_bf16 a[12:15], v[116:119], a[184:187], a[12:15]\n"
  "s_waitcnt lgkmcnt(1)\n"
  "ds_bpermute_b32 v229, v210, v188 offset:4\n"
  "v_mfma_f32_16x16x32_bf16 a[12:15], v[120:123], a[188:191], a[12:15]\n"
  "buffer_load_dwordx4 v252, s[60:63], s70 offen lds\n"
  "v_mfma_f32_16x16x32_bf16 a[128:131], v[148:151], a[160:163], a[128:131]\n"
  "ds_bpermute_b32 v230, v210, v188 offset:8\n"
  "v_mfma_f32_16x16x32_bf16 a[132:135], v[148:151], a[168:171], a[132:135]\n"
  "ds_bpermute_b32 v231, v210, v188 offset:12\n"
  "v_mfma_f32_16x16x32_bf16 a[136:139], v[148:151], a[176:179], a[136:139]\n"
  "buffer_load_dwordx4 v253, s[60:63], s70 offen offset:1024 lds\n"
  "v_mfma_f32_16x16x32_bf16 a[140:143], v[148:151], a[184:187], a[140:143]\n"
  "buffer_load_dwordx4 v252, s[60:63], s70 offen offset:2048 lds\n"
  "v_mfma_f32_16x16x32_bf16 a[32:35], v[124:127], a[160:163], a[32:35]\n"
  "buffer_load_dwordx4 v253, s[60:63], s70 offen offset:3072 lds\n"
  "s_add_u32 s70, s70, 65536\n"
  "v_mfma_f32_16x16x32_bf16 a[32:35], v[128:131], a[164:167], a[32:35]\n"
  "ds_bpermute_b32 v232, v210, v189 offset:0\n"
  "v_accvgpr_read_b32 v152, a0\n"
  "v_mfma_f32_16x16x32_bf16 a[36:39], v[124:127], a[168:171], a[36:39]\n"
  "ds_bpermute_b32 v233, v210, v189 offset:4\n"
  "v_accvgpr_read_b32 v153, a4\n"
  "v_mfma_f32_16x16x32_bf16 a[36:39], v[128:131], a[172:175], a[36:39]\n"
  "ds_bpermute_b32 v234, v210, v189 offset:8\n"
  "v_accvgpr_read_b32 v154, a8\n"
  "v_mfma_f32_16x16x32_bf16 a[40:43], v[124:127], a[176:179], a[40:43]\n"
  "ds_bpermute_b32 v235, v210, v189 offset:12\n"
  "v_accvgpr_read_b32 v155, a12\n"
  "v_mfma_f32_16x16x32_bf16 a[40:43], v[128:131], a[180:183], a[40:43]\n"
  "ds_write_b32 v213, v193 offset:4096\n"
  "v_mfma_f32_16x16x32_bf16 a[44:47], v[124:127], a[184:187], a[44:47]\n"
  "ds_write_b32 v212, v173 offset:0\n"
  "v_mfma_f32_16x16x32_bf16 a[44:47], v[128:131], a[188:191], a[44:47]\n"
  "ds_write2st64_b64 v211, a[130:131], a[134:135] offset0:0 offset1:1\n"
  "v_accvgpr_read_b32 v156, a16\n"
  "ds_write2st64_b64 v211, a[138:139], a[142:143] offset0:2 offset1:3\n"
  "v_accvgpr_read_b32 v157, a20\n"
  "ds_write2st64_b64 v211, a[146:147], a[150:151] offset0:4 offset1:5\n"
  "v_accvgpr_read_b32 v158, a24\n"
  "ds_write2st64_b64 v211, a[154:155], a[158:159] offset0:6 offset1:7\n"
  "v_accvgpr_read_b32 v159, a28\n"
  "v_and_b32 v213, -16, v213\n"
  "v_rcp_f32 v190, v171\n"
  "v_rcp_f32 v191, v172\n"
  "s_waitcnt lgkmcnt(13)\n"
  "v_pk_mul_f32 v[152:153], v[152:153], v[228:229] op_sel:[0,0] op_sel_hi:[1,0]\n"
  "v_pk_mul_f32 v[154:155], v[154:155], v[228:229] op_sel:[0,0] op_sel_hi:[1,0]\n"
  "v_pk_mul_f32 v[156:157], v[156:157], v[228:229] op_sel:[0,0] op_sel_hi:[1,0]\n"
  "v_pk_mul_f32 v[158:159], v[158:159], v[228:229] op_sel:[0,0] op_sel_hi:[1,0]\n"
  "v_cvt_pk_bf16_f32 v160, v152, v153\n"
  "v_cvt_pk_bf16_f32 v161, v154, v155\n"
  "v_cvt_pk_bf16_f32 v162, v156, v157\n"
  "v_cvt_pk_bf16_f32 v163, v158, v159\n"
  "buffer_store_dwordx4 v[160:163], v206, s[52:55], s10 offen sc1\n"
  "v_accvgpr_read_b32 v176, a1\n"
  "v_accvgpr_read_b32 v177, a5\n"
  "v_accvgpr_read_b32 v178, a9\n"
  "v_accvgpr_read_b32 v179, a13\n"
  "v_accvgpr_read_b32 v180, a17\n"
  "v_accvgpr_read_b32 v181, a21\n"
  "v_accvgpr_read_b32 v182, a25\n"
  "v_accvgpr_read_b32 v183, a29\n"
  "s_waitcnt lgkmcnt(12)\n"
  "v_pk_mul_f32 v[176:177], v[176:177], v[228:229] op_sel:[0,1] op_sel_hi:[1,1]\n"
  "v_pk_mul_f32 v[178:179], v[178:179], v[228:229] op_sel:[0,1] op_sel_hi:[1,1]\n"
  "v_pk_mul_f32 v[180:181], v[180:181], v[228:229] op_sel:[0,1] op_sel_hi:[1,1]\n"
  "v_pk_mul_f32 v[182:183], v[182:183], v[228:229] op_sel:[0,1] op_sel_hi:[1,1]\n"
  "v_cvt_pk_bf16_f32 v184, v176, v177\n"
  "v_cvt_pk_bf16_f32 v185, v178, v179\n"
  "v_cvt_pk_bf16_f32 v186, v180, v181\n"
  "v_cvt_pk_bf16_f32 v187, v182, v183\n"
  "buffer_store_dwordx4 v[184:187], v207, s[52:55], s10 offen offset:1024 sc1\n"
  "v_accvgpr_read_b32 v152, a2\n"
  "v_accvgpr_read_b32 v153, a6\n"
  "v_accvgpr_read_b32 v154, a10\n"
  "v_accvgpr_read_b32 v155, a14\n"
  "v_accvgpr_read_b32 v156, a18\n"
  "v_accvgpr_read_b32 v157, a22\n"
  "v_accvgpr_read_b32 v158, a26\n"
  "v_accvgpr_read_b32 v159, a30\n"
  "s_waitcnt lgkmcnt(11)\n"
  "v_pk_mul_f32 v[152:153], v[152:153], v[230:231] op_sel:[0,0] op_sel_hi:[1,0]\n"
  "v_pk_mul_f32 v[154:155], v[154:155], v[230:231] op_sel:[0,0] op_sel_hi:[1,0]\n"
  "v_pk_mul_f32 v[156:157], v[156:157], v[230:231] op_sel:[0,0] op_sel_hi:[1,0]\n"
  "v_pk_mul_f32 v[158:159], v[158:159], v[230:231] op_sel:[0,0] op_sel_hi:[1,0]\n"
  "v_cvt_pk_bf16_f32 v160, v152, v153\n"
  "v_cvt_pk_bf16_f32 v161, v154, v155\n"
  "v_cvt_pk_bf16_f32 v162, v156, v157\n"
  "v_cvt_pk_bf16_f32 v163, v158, v159\n"
  "buffer_store_dwordx4 v[160:163], v208, s[52:55], s10 offen offset:2048 sc1\n"
  "v_accvgpr_read_b32 v176, a3\n"
  "v_accvgpr_read_b32 v177, a7\n"
  "v_accvgpr_read_b32 v178, a11\n"
  "v_accvgpr_read_b32 v179, a15\n"
  "v_accvgpr_read_b32 v180, a19\n"
  "v_accvgpr_read_b32 v181, a23\n"
  "v_accvgpr_read_b32 v182, a27\n"
  "v_accvgpr_read_b32 v183, a31\n"
  "s_waitcnt lgkmcnt(10)\n"
  "v_pk_mul_f32 v[176:177], v[176:177], v[230:231] op_sel:[0,1] op_sel_hi:[1,1]\n"
  "v_pk_mul_f32 v[178:179], v[178:179], v[230:231] op_sel:[0,1] op_sel_hi:[1,1]\n"
  "v_pk_mul_f32 v[180:181], v[180:181], v[230:231] op_sel:[0,1] op_sel_hi:[1,1]\n"
  "v_pk_mul_f32 v[182:183], v[182:183], v[230:231] op_sel:[0,1] op_sel_hi:[1,1]\n"
  "v_cvt_pk_bf16_f32 v184, v176, v177\n"
  "v_cvt_pk_bf16_f32 v185, v178, v179\n"
  "v_cvt_pk_bf16_f32 v186, v180, v181\n"
  "v_cvt_pk_bf16_f32 v187, v182, v183\n"
  "buffer_store_dwordx4 v[184:187], v209, s[52:55], s10 offen offset:3072 sc1\n"
  "s_waitcnt vmcnt(4) lgkmcnt(0)\n"
  "s_barrier\n"
  "ds_read_b128 v[164:167], v213 offset:4096\n"
  "ds_read_b32 v168, v195 offset:0\n"
  "ds_read2st64_b64 v[116:119], v194 offset0:0 offset1:1\n"
  "ds_read2st64_b64 v[120:123], v194 offset0:2 offset1:3\n"
  "ds_read2st64_b64 v[124:127], v194 offset0:4 offset1:5\n"
  "ds_read2st64_b64 v[128:131], v194 offset0:6 offset1:7\n"
  "v_accvgpr_read_b32 v152, a32\n"
  "v_accvgpr_read_b32 v153, a36\n"
  "v_accvgpr_read_b32 v154, a40\n"
  "v_accvgpr_read_b32 v155, a44\n"
  "v_accvgpr_read_b32 v156, a48\n"
  "v_accvgpr_read_b32 v157, a52\n"
  "v_accvgpr_read_b32 v158, a56\n"
  "v_accvgpr_read_b32 v159, a60\n"
  "v_pk_mul_f32 v[152:153], v[152:153], v[232:233] op_sel:[0,0] op_sel_hi:[1,0]\n"
  "v_pk_mul_f32 v[154:155], v[154:155], v[232:233] op_sel:[0,0] op_sel_hi:[1,0]\n"
  "v_pk_mul_f32 v[156:157], v[156:157], v[232:233] op_sel:[0,0] op_sel_hi:[1,0]\n"
  "v_pk_mul_f32 v[158:159], v[158:159], v[232:233] op_sel:[0,0] op_sel_hi:[1,0]\n"
  "v_cvt_pk_bf16_f32 v160, v152, v153\n"
  "v_cvt_pk_bf16_f32 v161, v154, v155\n"
  "v_cvt_pk_bf16_f32 v162, v156, v157\n"
  "v_cvt_pk_bf16_f32 v163, v158, v159\n"
  "buffer_store_dwordx4 v[160:163], v206, s[52:55], s11 offen sc1\n"
  "s_waitcnt lgkmcnt(5)\n"
  "v_or3_b32 v164, v164, v165, v166\n"
  "v_or_b32 v164, v164, v167\n"
  "s_waitcnt lgkmcnt(4)\n"
  "v_add_f32 v173, v173, v168\n"
  "v_readfirstlane_b32 s77, v164\n"
  "v_rcp_f32 v192, v173\n"
  "s_andn2_b32 s77, s77, s76\n"
  "s_cbranch_scc1 L_fb_%=\n"
  "s_mul_i32 s77, s65, 2080\n"
  "v_xor_b32 v174, s77, v206\n"
  "v_xor_b32 v175, 1040, v174\n"
  "ds_bpermute_b32 v244, v210, v192 offset:0\n"
  "ds_bpermute_b32 v245, v210, v192 offset:4\n"
  "ds_bpermute_b32 v236, v210, v190 offset:0\n"
  "ds_bpermute_b32 v237, v210, v190 offset:4\n"
  "ds_bpermute_b32 v238, v210, v190 offset:8\n"
  "ds_bpermute_b32 v239, v210, v190 offset:12\n"
  "ds_bpermute_b32 v240, v210, v191 offset:0\n"
  "ds_bpermute_b32 v241, v210, v191 offset:4\n"
  "ds_bpermute_b32 v242, v210, v191 offset:8\n"
  "ds_bpermute_b32 v243, v210, v191 offset:12\n"
  "v_accvgpr_read_b32 v176, a33\n"
  "v_accvgpr_read_b32 v177, a37\n"
  "v_accvgpr_read_b32 v178, a41\n"
  "v_accvgpr_read_b32 v179, a45\n"
  "v_accvgpr_read_b32 v180, a49\n"
  "v_accvgpr_read_b32 v181, a53\n"
  "v_accvgpr_read_b32 v182, a57\n"
  "v_accvgpr_read_b32 v183, a61\n"
  "v_pk_mul_f32 v[176:177], v[176:177], v[232:233] op_sel:[0,1] op_sel_hi:[1,1]\n"
  "v_pk_mul_f32 v[178:179], v[178:179], v[232:233] op_sel:[0,1] op_sel_hi:[1,1]\n"
  "v_pk_mul_f32 v[180:181], v[180:181], v[232:233] op_sel:[0,1] op_sel_hi:[1,1]\n"
  "v_pk_mul_f32 v[182:183], v[182:183], v[232:233] op_sel:[0,1] op_sel_hi:[1,1]\n"
  "v_cvt_pk_bf16_f32 v184, v176, v177\n"
  "v_cvt_pk_bf16_f32 v185, v178, v179\n"
  "v_cvt_pk_bf16_f32 v186, v180, v181\n"
  "v_cvt_pk_bf16_f32 v187, v182, v183\n"
  "buffer_store_dwordx4 v[184:187], v207, s[52:55], s11 offen offset:1024 sc1\n"
  "v_accvgpr_read_b32 v152, a34\n"
  "v_accvgpr_read_b32 v153, a38\n"
  "v_accvgpr_read_b32 v154, a42\n"
  "v_accvgpr_read_b32 v155, a46\n"
  "v_accvgpr_read_b32 v156, a50\n"
  "v_accvgpr_read_b32 v157, a54\n"
  "v_accvgpr_read_b32 v158, a58\n"
  "v_accvgpr_read_b32 v159, a62\n"
  "v_pk_mul_f32 v[152:153], v[152:153], v[234:235] op_sel:[0,0] op_sel_hi:[1,0]\n"
  "v_pk_mul_f32 v[154:155], v[154:155], v[234:235] op_sel:[0,0] op_sel_hi:[1,0]\n"
  "v_pk_mul_f32 v[156:157], v[156:157], v[234:235] op_sel:[0,0] op_sel_hi:[1,0]\n"
  "v_pk_mul_f32 v[158:159], v[158:159], v[234:235] op_sel:[0,0] op_sel_hi:[1,0]\n"
  "v_cvt_pk_bf16_f32 v160, v152, v153\n"
  "v_cvt_pk_bf16_f32 v161, v154, v155\n"
  "v_cvt_pk_bf16_f32 v162, v156, v157\n"
  "v_cvt_pk_bf16_f32 v163, v158, v159\n"
  "buffer_store_dwordx4 v[160:163], v208, s[52:55], s11 offen offset:2048 sc1\n"
  "v_accvgpr_read_b32 v176, a35\n"
  "v_accvgpr_read_b32 v177, a39\n"
  "v_accvgpr_read_b32 v178, a43\n"
  "v_accvgpr_read_b32 v179, a47\n"
  "v_accvgpr_read_b32 v180, a51\n"
  "v_accvgpr_read_b32 v181, a55\n"
  "v_accvgpr_read_b32 v182, a59\n"
  "v_accvgpr_read_b32 v183, a63\n"
  "v_pk_mul_f32 v[176:177], v[176:177], v[234:235] op_sel:[0,1] op_sel_hi:[1,1]\n"
  "v_pk_mul_f32 v[178:179], v[178:179], v[234:235] op_sel:[0,1] op_sel_hi:[1,1]\n"
  "v_pk_mul_f32 v[180:181], v[180:181], v[234:235] op_sel:[0,1] op_sel_hi:[1,1]\n"
  "v_pk_mul_f32 v[182:183], v[182:183], v[234:235] op_sel:[0,1] op_sel_hi:[1,1]\n"
  "v_cvt_pk_bf16_f32 v184, v176, v177\n"
  "v_cvt_pk_bf16_f32 v185, v178, v179\n"
  "v_cvt_pk_bf16_f32 v186, v180, v181\n"
  "v_cvt_pk_bf16_f32 v187, v182, v183\n"
  "buffer_store_dwordx4 v[184:187], v209, s[52:55], s11 offen offset:3072 sc1\n"
  "v_accvgpr_read_b32 v80, a128\n"
  "v_accvgpr_read_b32 v81, a129\n"
  "v_accvgpr_read_b32 v82, a132\n"
  "v_accvgpr_read_b32 v83, a133\n"
  "v_accvgpr_read_b32 v84, a136\n"
  "v_accvgpr_read_b32 v85, a137\n"
  "v_accvgpr_read_b32 v86, a140\n"
  "v_accvgpr_read_b32 v87, a141\n"
  "v_accvgpr_read_b32 v88, a144\n"
  "v_accvgpr_read_b32 v89, a145\n"
  "v_accvgpr_read_b32 v90, a148\n"
  "v_accvgpr_read_b32 v91, a149\n"
  "v_accvgpr_read_b32 v92, a152\n"
  "v_accvgpr_read_b32 v93, a153\n"
  "v_accvgpr_read_b32 v94, a156\n"
  "v_accvgpr_read_b32 v95, a157\n"
  "s_waitcnt lgkmcnt(13)\n"
  "v_pk_add_f32 v[80:81], v[80:81], v[116:117]\n"
  "v_pk_add_f32 v[82:83], v[82:83], v[118:119]\n"
  "s_waitcnt lgkmcnt(12)\n"
  "v_pk_add_f32 v[84:85], v[84:85], v[120:121]\n"
  "v_pk_add_f32 v[86:87], v[86:87], v[122:123]\n"
  "s_waitcnt lgkmcnt(11)\n"
  "v_pk_add_f32 v[88:89], v[88:89], v[124:125]\n"
  "v_pk_add_f32 v[90:91], v[90:91], v[126:127]\n"
  "s_waitcnt lgkmcnt(10)\n"
  "v_pk_add_f32 v[92:93], v[92:93], v[128:129]\n"
  "v_pk_add_f32 v[94:95], v[94:95], v[130:131]\n"
  "s_waitcnt lgkmcnt(8)\n"
  "v_pk_mul_f32 v[80:81], v[80:81], v[244:245]\n"
  "v_pk_mul_f32 v[82:83], v[82:83], v[244:245]\n"
  "v_pk_mul_f32 v[84:85], v[84:85], v[244:245]\n"
  "v_pk_mul_f32 v[86:87], v[86:87], v[244:245]\n"
  "v_pk_mul_f32 v[88:89], v[88:89], v[244:245]\n"
  "v_pk_mul_f32 v[90:91], v[90:91], v[244:245]\n"
  "v_pk_mul_f32 v[92:93], v[92:93], v[244:245]\n"
  "v_pk_mul_f32 v[94:95], v[94:95], v[244:245]\n"
  "v_cvt_pk_bf16_f32 v112, v80, v82\n"
  "v_cvt_pk_bf16_f32 v113, v84, v86\n"
  "v_cvt_pk_bf16_f32 v114, v88, v90\n"
  "v_cvt_pk_bf16_f32 v115, v92, v94\n"
  "v_cvt_pk_bf16_f32 v116, v81, v83\n"
  "v_cvt_pk_bf16_f32 v117, v85, v87\n"
  "v_cvt_pk_bf16_f32 v118, v89, v91\n"
  "v_cvt_pk_bf16_f32 v119, v93, v95\n"
  "buffer_store_dwordx4 v[112:115], v174, s[52:55], s14 offen sc1\n"
  "buffer_store_dwordx4 v[116:119], v175, s[52:55], s14 offen sc1\n"
  "v_mfma_f32_16x16x32_bf16 a[92:95], v[96:99], a[208:211], a[92:95]\n"
  "ds_read_b64_tr_b16 v[32:33], v202 offset:32768\n"
  "ds_read_b64_tr_b16 v[36:37], v203 offset:32768\n"
  "v_mfma_f32_16x16x32_bf16 a[92:95], v[100:103], a[212:215], a[92:95]\n"
  "ds_read_b64_tr_b16 v[40:41], v202 offset:33024\n"
  "ds_read_b64_tr_b16 v[44:45], v203 offset:33024\n"
  "v_mfma_f32_16x16x32_bf16 a[92:95], v[132:135], a[216:219], a[92:95]\n"
  "ds_read_b64_tr_b16 v[48:49], v202 offset:33280\n"
  "ds_read_b64_tr_b16 v[52:53], v203 offset:33280\n"
  "v_mfma_f32_16x16x32_bf16 a[92:95], v[136:139], a[220:223], a[92:95]\n"
  "ds_read_b64_tr_b16 v[56:57], v202 offset:33536\n"
  "ds_read_b64_tr_b16 v[60:61], v203 offset:33536\n"
  "v_mfma_f32_16x16x32_bf16 a[124:127], v[144:147], a[220:223], a[124:127]\n"
  "ds_read_b64_tr_b16 v[34:35], v202 offset:49152\n"
  "ds_read_b64_tr_b16 v[38:39], v203 offset:49152\n"
  "v_mfma_f32_16x16x32_bf16 a[124:127], v[140:143], a[216:219], a[124:127]\n"
  "ds_read_b64_tr_b16 v[42:43], v202 offset:49408\n"
  "ds_read_b64_tr_b16 v[46:47], v203 offset:49408\n"
  "v_mfma_f32_16x16x32_bf16 a[124:127], v[108:111], a[212:215], a[124:127]\n"
  "ds_read_b64_tr_b16 v[50:51], v202 offset:49664\n"
  "ds_read_b64_tr_b16 v[54:55], v203 offset:49664\n"
  "v_mfma_f32_16x16x32_bf16 a[124:127], v[104:107], a[208:211], a[124:127]\n"
  "ds_read_b64_tr_b16 v[58:59], v202 offset:49920\n"
  "ds_read_b64_tr_b16 v[62:63], v203 offset:49920\n"
  "v_mfma_f32_16x16x32_bf16 a[64:67], v[132:135], a[160:163], a[64:67]\n"
  "ds_read_b64_tr_b16 v[64:65], v204 offset:16384\n"
  "ds_read_b64_tr_b16 v[68:69], v205 offset:16384\n"
  "v_mfma_f32_16x16x32_bf16 a[64:67], v[136:139], a[164:167], a[64:67]\n"
  "ds_read_b64_tr_b16 v[72:73], v204 offset:16640\n"
  "ds_read_b64_tr_b16 v[76:77], v205 offset:16640\n"
  "v_mfma_f32_16x16x32_bf16 a[68:71], v[132:135], a[168:171], a[68:71]\n"
  "ds_read_b64_tr_b16 v[80:81], v204 offset:16896\n"
  "ds_read_b64_tr_b16 v[84:85], v205 offset:16896\n"
  "v_mfma_f32_16x16x32_bf16 a[68:71], v[136:139], a[172:175], a[68:71]\n"
  "ds_read_b64_tr_b16 v[88:89], v204 offset:17152\n"
  "ds_read_b64_tr_b16 v[92:93], v205 offset:17152\n"
  "v_mfma_f32_16x16x32_bf16 a[72:75], v[132:135], a[176:179], a[72:75]\n"
  "ds_read_b64_tr_b16 v[66:67], v204 offset:0\n"
  "ds_read_b64_tr_b16 v[70:71], v205 offset:0\n"
  "v_mfma_f32_16x16x32_bf16 a[72:75], v[136:139], a[180:183], a[72:75]\n"
  "ds_read_b64_tr_b16 v[74:75], v204 offset:256\n"
  "ds_read_b64_tr_b16 v[78:79], v205 offset:256\n"
  "v_mfma_f32_16x16x32_bf16 a[76:79], v[132:135], a[184:187], a[76:79]\n"
  "ds_read_b64_tr_b16 v[82:83], v204 offset:512\n"
  "ds_read_b64_tr_b16 v[86:87], v205 offset:512\n"
  "v_mfma_f32_16x16x32_bf16 a[76:79], v[136:139], a[188:191], a[76:79]\n"
  "ds_read_b64_tr_b16 v[90:91], v204 offset:768\n"
  "ds_read_b64_tr_b16 v[94:95], v205 offset:768\n"
  "v_mfma_f32_16x16x32_bf16 a[96:99], v[140:143], a[160:163], a[96:99]\n"
  "ds_read_b64_tr_b16 a[0:1], v202 offset:0\n"
  "ds_read_b64_tr_b16 a[4:5], v203 offset:0\n"
  "v_mfma_f32_16x16x32_bf16 a[96:99], v[144:147], a[164:167], a[96:99]\n"
  "ds_read_b64_tr_b16 a[8:9], v202 offset:256\n"
  "ds_read_b64_tr_b16 a[12:13], v203 offset:256\n"
  "v_mfma_f32_16x16x32_bf16 a[100:103], v[140:143], a[168:171], a[100:103]\n"
  "ds_read_b64_tr_b16 a[16:17], v202 offset:512\n"
  "ds_read_b64_tr_b16 a[20:21], v203 offset:512\n"
  "v_mfma_f32_16x16x32_bf16 a[100:103], v[144:147], a[172:175], a[100:103]\n"
  "ds_read_b64_tr_b16 a[24:25], v202 offset:768\n"
  "ds_read_b64_tr_b16 a[28:29], v203 offset:768\n"
  "v_mfma_f32_16x16x32_bf16 a[104:107], v[140:143], a[176:179], a[104:107]\n"
  "ds_read_b64_tr_b16 a[2:3], v202 offset:16384\n"
  "ds_read_b64_tr_b16 a[6:7], v203 offset:16384\n"
  "v_mfma_f32_16x16x32_bf16 a[104:107], v[144:147], a[180:183], a[104:107]\n"
  "ds_read_b64_tr_b16 a[10:11], v202 offset:16640\n"
  "ds_read_b64_tr_b16 a[14:15], v203 offset:16640\n"
  "v_mfma_f32_16x16x32_bf16 a[108:111], v[140:143], a[184:187], a[108:111]\n"
  "ds_read_b64_tr_b16 a[18:19], v202 offset:16896\n"
  "ds_read_b64_tr_b16 a[22:23], v203 offset:16896\n"
  "v_mfma_f32_16x16x32_bf16 a[108:111], v[144:147], a[188:191], a[108:111]\n"
  "ds_read_b64_tr_b16 a[26:27], v202 offset:17152\n"
  "ds_read_b64_tr_b16 a[30:31], v203 offset:17152\n"
  "ds_read_b64_tr_b16 a[192:193], v200 offset:0\n"
  "ds_read_b64_tr_b16 a[196:197], v201 offset:0\n"
  "ds_read_b64_tr_b16 a[200:201], v200 offset:256\n"
  "ds_read_b64_tr_b16 a[204:205], v201 offset:256\n"
  "ds_read_b64_tr_b16 a[208:209], v200 offset:512\n"
  "ds_read_b64_tr_b16 a[212:213], v201 offset:512\n"
  "ds_read_b64_tr_b16 a[216:217], v200 offset:768\n"
  "ds_read_b64_tr_b16 a[220:221], v201 offset:768\n"
  "ds_read_b64_tr_b16 a[194:195], v200 offset:16384\n"
  "ds_read_b64_tr_b16 a[198:199], v201 offset:16384\n"
  "ds_read_b64_tr_b16 a[202:203], v200 offset:16640\n"
  "ds_read_b64_tr_b16 a[206:207], v201 offset:16640\n"
  "ds_read_b64_tr_b16 a[210:211], v200 offset:16896\n"
  "ds_read_b64_tr_b16 a[214:215], v201 offset:16896\n"
  "ds_read_b64_tr_b16 a[218:219], v200 offset:17152\n"
  "ds_read_b64_tr_b16 a[222:223], v201 offset:17152\n"
  "v_cmp_gt_u32 vcc, 128, v214\n"
  "s_cmp_eq_u32 s64, 0\n"
  "s_cselect_b32 s74, 0xff, 0\n"
  "v_cndmask_b32 v218, v219, v215, vcc\n"
  "v_add_u32 v223, s68, v214\n"
  "s_waitcnt lgkmcnt(0)\n"
  "s_barrier\n"
  "v_accvgpr_read_b32 v152, a64\n"
  "v_accvgpr_read_b32 v153, a68\n"
  "v_accvgpr_read_b32 v154, a72\n"
  "v_accvgpr_read_b32 v155, a76\n"
  "s_add_u32 m0, s67, 0\n"
  "s_nop 0\n"
  "buffer_load_dwordx4 v252, s[60:63], s70 offen lds\n"
  "v_accvgpr_read_b32 v156, a80\n"
  "v_accvgpr_read_b32 v157, a84\n"
  "v_accvgpr_read_b32 v158, a88\n"
  "v_accvgpr_read_b32 v159, a92\n"
  "v_pk_mul_f32 v[152:153], v[152:153], v[236:237] op_sel:[0,0] op_sel_hi:[1,0]\n"
  "v_pk_mul_f32 v[154:155], v[154:155], v[236:237] op_sel:[0,0] op_sel_hi:[1,0]\n"
  "v_pk_mul_f32 v[156:157], v[156:157], v[236:237] op_sel:[0,0] op_sel_hi:[1,0]\n"
  "v_pk_mul_f32 v[158:159], v[158:159], v[236:237] op_sel:[0,0] op_sel_hi:[1,0]\n"
  "buffer_load_dwordx4 v253, s[60:63], s70 offen offset:1024 lds\n"
  "v_cvt_pk_bf16_f32 v120, v152, v153\n"
  "v_cvt_pk_bf16_f32 v121, v154, v155\n"
  "v_cvt_pk_bf16_f32 v122, v156, v157\n"
  "v_cvt_pk_bf16_f32 v123, v158, v159\n"
  "v_accvgpr_read_b32 v176, a65\n"
  "v_accvgpr_read_b32 v177, a69\n"
  "v_accvgpr_read_b32 v178, a73\n"
  "v_accvgpr_read_b32 v179, a77\n"
  "buffer_load_dwordx4 v252, s[60:63], s70 offen offset:2048 lds\n"
  "v_accvgpr_read_b32 v180, a81\n"
  "v_accvgpr_read_b32 v181, a85\n"
  "v_accvgpr_read_b32 v182, a89\n"
  "v_accvgpr_read_b32 v183, a93\n"
  "v_pk_mul_f32 v[176:177], v[176:177], v[236:237] op_sel:[0,1] op_sel_hi:[1,1]\n"
  "v_pk_mul_f32 v[178:179], v[178:179], v[236:237] op_sel:[0,1] op_sel_hi:[1,1]\n"
  "v_pk_mul_f32 v[180:181], v[180:181], v[236:237] op_sel:[0,1] op_sel_hi:[1,1]\n"
  "v_pk_mul_f32 v[182:183], v[182:183], v[236:237] op_sel:[0,1] op_sel_hi:[1,1]\n"
  "buffer_load_dwordx4 v253, s[60:63], s70 offen offset:3072 lds\n"
  "s_add_u32 s70, s70, 65536\n"
  "v_cvt_pk_bf16_f32 v124, v176, v177\n"
  "v_cvt_pk_bf16_f32 v125, v178, v179\n"
  "v_cvt_pk_bf16_f32 v126, v180, v181\n"
  "v_cvt_pk_bf16_f32 v127, v182, v183\n"
  "v_accvgpr_read_b32 v152, a66\n"
  "v_accvgpr_read_b32 v153, a70\n"
  "v_accvgpr_read_b32 v154, a74\n"
  "v_accvgpr_read_b32 v155, a78\n"
  "s_add_u32 m0, s67, 16384\n"
  "s_nop 0\n"
  "buffer_load_dwordx4 v252, s[60:63], s70 offen lds\n"
  "v_accvgpr_read_b32 v156, a82\n"
  "v_accvgpr_read_b32 v157, a86\n"
  "v_accvgpr_read_b32 v158, a90\n"
  "v_accvgpr_read_b32 v159, a94\n"
  "v_pk_mul_f32 v[152:153], v[152:153], v[238:239] op_sel:[0,0] op_sel_hi:[1,0]\n"
  "v_pk_mul_f32 v[154:155], v[154:155], v[238:239] op_sel:[0,0] op_sel_hi:[1,0]\n"
  "v_pk_mul_f32 v[156:157], v[156:157], v[238:239] op_sel:[0,0] op_sel_hi:[1,0]\n"
  "v_pk_mul_f32 v[158:159], v[158:159], v[238:239] op_sel:[0,0] op_sel_hi:[1,0]\n"
  "buffer_load_dwordx4 v253, s[60:63], s70 offen offset:1024 lds\n"
  "v_cvt_pk_bf16_f32 v128, v152, v153\n"
  "v_cvt_pk_bf16_f32 v129, v154, v155\n"
  "v_cvt_pk_bf16_f32 v130, v156, v157\n"
  "v_cvt_pk_bf16_f32 v131, v158, v159\n"
  "v_accvgpr_read_b32 v176, a67\n"
  "v_accvgpr_read_b32 v177, a71\n"
  "v_accvgpr_read_b32 v178, a75\n"
  "v_accvgpr_read_b32 v179, a79\n"
  "buffer_load_dwordx4 v252, s[60:63], s70 offen offset:2048 lds\n"
  "v_accvgpr_read_b32 v180, a83\n"
  "v_accvgpr_read_b32 v181, a87\n"
  "v_accvgpr_read_b32 v182, a91\n"
  "v_accvgpr_read_b32 v183, a95\n"
  "v_pk_mul_f32 v[176:177], v[176:177], v[238:239] op_sel:[0,1] op_sel_hi:[1,1]\n"
  "v_pk_mul_f32 v[178:179], v[178:179], v[238:239] op_sel:[0,1] op_sel_hi:[1,1]\n"
  "v_pk_mul_f32 v[180:181], v[180:181], v[238:239] op_sel:[0,1] op_sel_hi:[1,1]\n"
  "v_pk_mul_f32 v[182:183], v[182:183], v[238:239] op_sel:[0,1] op_sel_hi:[1,1]\n"
  "buffer_load_dwordx4 v253, s[60:63], s70 offen offset:3072 lds\n"
  "s_add_u32 s70, s70, 65536\n"
  "v_cvt_pk_bf16_f32 v132, v176, v177\n"
  "v_cvt_pk_bf16_f32 v133, v178, v179\n"
  "v_cvt_pk_bf16_f32 v134, v180, v181\n"
  "v_cvt_pk_bf16_f32 v135, v182, v183\n"
  "s_waitcnt vmcnt(8)\n"
  "s_barrier\n"
  "buffer_store_dword v222, v218, s[56:59], s6 offen offset:256 sc1\n"
  "v_accvgpr_read_b32 v152, a96\n"
  "v_accvgpr_read_b32 v153, a100\n"
  "s_add_u32 m0, s67, 32768\n"
  "s_nop 0\n"
  "buffer_load_dwordx4 v252, s[60:63], s70 offen lds\n"
  "v_accvgpr_read_b32 v154, a104\n"
  "v_accvgpr_read_b32 v155, a108\n"
  "v_accvgpr_read_b32 v156, a112\n"
  "v_accvgpr_read_b32 v157, a116\n"
  "buffer_load_dwordx4 v253, s[60:63], s70 offen offset:1024 lds\n"
  "v_accvgpr_read_b32 v158, a120\n"
  "v_accvgpr_read_b32 v159, a124\n"
  "v_pk_mul_f32 v[152:153], v[152:153], v[240:241] op_sel:[0,0] op_sel_hi:[1,0]\n"
  "v_pk_mul_f32 v[154:155], v[154:155], v[240:241] op_sel:[0,0] op_sel_hi:[1,0]\n"
  "buffer_load_dwordx4 v252, s[60:63], s70 offen offset:2048 lds\n"
  "v_pk_mul_f32 v[156:157], v[156:157], v[240:241] op_sel:[0,0] op_sel_hi:[1,0]\n"
  "v_pk_mul_f32 v[158:159], v[158:159], v[240:241] op_sel:[0,0] op_sel_hi:[1,0]\n"
  "v_cvt_pk_bf16_f32 v136, v152, v153\n"
  "buffer_load_dwordx4 v253, s[60:63], s70 offen offset:3072 lds\n"
  "s_add_u32 s70, s70, 65536\n"
  "v_cvt_pk_bf16_f32 v137, v154, v155\n"
  "v_cvt_pk_bf16_f32 v138, v156, v157\n"
  "v_cvt_pk_bf16_f32 v139, v158, v159\n"
  "v_accvgpr_read_b32 v176, a97\n"
  "s_add_u32 m0, s67, 49152\n"
  "s_nop 0\n"
  "buffer_load_dwordx4 v252, s[60:63], s70 offen lds\n"
  "v_accvgpr_read_b32 v177, a101\n"
  "v_accvgpr_read_b32 v178, a105\n"
  "v_accvgpr_read_b32 v179, a109\n"
  "v_accvgpr_read_b32 v180, a113\n"
  "buffer_load_dwordx4 v253, s[60:63], s70 offen offset:1024 lds\n"
  "v_accvgpr_read_b32 v181, a117\n"
  "v_accvgpr_read_b32 v182, a121\n"
  "v_accvgpr_read_b32 v183, a125\n"
  "v_pk_mul_f32 v[176:177], v[176:177], v[240:241] op_sel:[0,1] op_sel_hi:[1,1]\n"
  "buffer_load_dwordx4 v252, s[60:63], s70 offen offset:2048 lds\n"
  "v_pk_mul_f32 v[178:179], v[178:179], v[240:241] op_sel:[0,1] op_sel_hi:[1,1]\n"
  "v_pk_mul_f32 v[180:181], v[180:181], v[240:241] op_sel:[0,1] op_sel_hi:[1,1]\n"
  "v_pk_mul_f32 v[182:183], v[182:183], v[240:241] op_sel:[0,1] op_sel_hi:[1,1]\n"
  "v_cvt_pk_bf16_f32 v140, v176, v177\n"
  "buffer_load_dwordx4 v253, s[60:63], s70 offen offset:3072 lds\n"
  "s_add_u32 s70, s70, 65536\n"
  "v_cvt_pk_bf16_f32 v141, v178, v179\n"
  "v_cvt_pk_bf16_f32 v142, v180, v181\n"
  "v_cvt_pk_bf16_f32 v143, v182, v183\n"
  "s_add_u32 m0, s67, 98304\n"
  "s_nop 0\n"
  "buffer_load_dwordx4 v252, s[60:63], s70 offen lds\n"
  "v_accvgpr_read_b32 v152, a98\n"
  "v_accvgpr_read_b32 v153, a102\n"
  "v_accvgpr_read_b32 v154, a106\n"
  "v_accvgpr_read_b32 v155, a110\n"
  "buffer_load_dwordx4 v253, s[60:63], s70 offen offset:1024 lds\n"
  "v_accvgpr_read_b32 v156, a114\n"
  "v_accvgpr_read_b32 v157, a118\n"
  "v_accvgpr_read_b32 v158, a122\n"
  "v_accvgpr_read_b32 v159, a126\n"
  "buffer_load_dword v217, v216, s[56:59], s6 offen offset:256 sc1\n"
  "v_pk_mul_f32 v[152:153], v[152:153], v[242:243] op_sel:[0,0] op_sel_hi:[1,0]\n"
  "v_pk_mul_f32 v[154:155], v[154:155], v[242:243] op_sel:[0,0] op_sel_hi:[1,0]\n"
  "v_pk_mul_f32 v[156:157], v[156:157], v[242:243] op_sel:[0,0] op_sel_hi:[1,0]\n"
  "v_pk_mul_f32 v[158:159], v[158:159], v[242:243] op_sel:[0,0] op_sel_hi:[1,0]\n"
  "buffer_load_dwordx4 v252, s[60:63], s70 offen offset:2048 lds\n"
  "v_cvt_pk_bf16_f32 v144, v152, v153\n"
  "v_cvt_pk_bf16_f32 v145, v154, v155\n"
  "v_cvt_pk_bf16_f32 v146, v156, v157\n"
  "v_cvt_pk_bf16_f32 v147, v158, v159\n"
  "buffer_load_dwordx4 v253, s[60:63], s70 offen offset:3072 lds\n"
  "s_add_u32 s70, s70, 65536\n"
  "v_accvgpr_read_b32 v176, a99\n"
  "v_accvgpr_read_b32 v177, a103\n"
  "v_accvgpr_read_b32 v178, a107\n"
  "v_accvgpr_read_b32 v179, a111\n"
  "s_add_u32 m0, s67, 114688\n"
  "s_nop 0\n"
  "buffer_load_dwordx4 v252, s[60:63], s70 offen lds\n"
  "v_accvgpr_read_b32 v180, a115\n"
  "v_accvgpr_read_b32 v181, a119\n"
  "v_accvgpr_read_b32 v182, a123\n"
  "buffer_load_dwordx4 v253, s[60:63], s70 offen offset:1024 lds\n"
  "v_accvgpr_read_b32 v183, a127\n"
  "v_pk_mul_f32 v[176:177], v[176:177], v[242:243] op_sel:[0,1] op_sel_hi:[1,1]\n"
  "v_pk_mul_f32 v[178:179], v[178:179], v[242:243] op_sel:[0,1] op_sel_hi:[1,1]\n"
  "v_pk_mul_f32 v[180:181], v[180:181], v[242:243] op_sel:[0,1] op_sel_hi:[1,1]\n"
  "buffer_load_dwordx4 v252, s[60:63], s70 offen offset:2048 lds\n"
  "v_pk_mul_f32 v[182:183], v[182:183], v[242:243] op_sel:[0,1] op_sel_hi:[1,1]\n"
  "v_cvt_pk_bf16_f32 v148, v176, v177\n"
  "v_cvt_pk_bf16_f32 v149, v178, v179\n"
  "v_cvt_pk_bf16_f32 v150, v180, v181\n"
  "buffer_load_dwordx4 v253, s[60:63], s70 offen offset:3072 lds\n"
  "s_add_u32 s70, s70, 65536\n"
  "v_cvt_pk_bf16_f32 v151, v182, v183\n"
  "v_accvgpr_read_b32 v96, a247\n"
  "v_xor_b32 v97, 64, v96\n"
  "v_xor_b32 v98, 128, v96\n"
  "v_xor_b32 v99, 192, v96\n"
  "v_add_u32 v100, 65536, v96\n"
  "v_add_u32 v101, 65536, v97\n"
  "v_add_u32 v102, 65536, v98\n"
  "v_add_u32 v103, 65536, v99\n"
  "v_add_u32 v104, 131072, v96\n"
  "v_add_u32 v105, 131072, v97\n"
  "v_add_u32 v106, 131072, v98\n"
  "v_add_u32 v107, 131072, v99\n"
  "s_lshl_b32 s77, s5, 10\n"
  "s_add_u32 s77, s77, s67\n"
  "v_add_u32 v246, s77, v214\n"
  "v_xor_b32 v247, 1024, v246\n"
  "v_xor_b32 v248, 2048, v246\n"
  "v_xor_b32 v249, 3072, v246\n"
  "v_lshrrev_b32 v160, 4, v214\n"
  "v_cmp_gt_u32 vcc, 128, v214\n"
  "s_lshl_b32 s73, s64, 7\n"
  "s_nop 0\n"
  "v_cndmask_b32 v225, v215, v219, vcc\n"
  "v_and_b32 v161, 15, v160\n"
  "v_lshlrev_b32 v162, 10, v161\n"
  "v_bfe_u32 v163, v160, 4, 1\n"
  "v_lshl_add_u32 v162, v163, 5, v162\n"
  "v_lshrrev_b32 v163, 5, v160\n"
  "v_lshl_add_u32 v162, v163, 4, v162\n"
  "s_lshl_b32 s77, s64, 6\n"
  "v_add_u32 v226, s77, v162\n"
  "s_cmp_eq_u32 s64, s5\n"
  "s_cselect_b32 s71, 0, 0x3fff0000\n"
  "s_cselect_b32 s72, 0x3fff0000, 0\n"
  "s_lshl_b32 s75, 0x100, s5\n"
  "s_andn2_b32 s75, 0xf00, s75\n"
  "v_lshrrev_b32 v162, 3, v160\n"
  "v_and_b32 v162, 6, v162\n"
  "v_lshrrev_b32 v163, 1, v161\n"
  "v_sub_u32 v162, v163, v162\n"
  "v_and_b32 v163, 1, v160\n"
  "v_lshlrev_b32 v163, 4, v163\n"
  "v_mov_b32 v164, 0x3f80\n"
  "v_lshlrev_b32 v163, v163, v164\n"
  "v_cmp_eq_u32 vcc, 0, v162\n"
  "v_mov_b32 v152, 0\n"
  "v_mov_b32 v153, 0\n"
  "v_cndmask_b32 v154, 0, v163, vcc\n"
  "v_cmp_eq_u32 vcc, 1, v162\n"
  "v_mov_b32 v156, 0\n"
  "v_mov_b32 v157, 0\n"
  "v_cndmask_b32 v155, 0, v163, vcc\n"
  "s_mov_b64 s[44:45], s[18:19]\n"
  "s_mov_b32 s46, 294144\n"
  "s_mov_b32 s47, 0x20000\n"
  "s_xor_b32 s85, s5, 3\n"
  "s_lshl_b32 s85, s85, 14\n"
  "s_waitcnt vmcnt(6) lgkmcnt(0)\n"
  "v_cmp_ne_u32 vcc, s80, v217\n"
  "s_nop 0\n"
  "s_and_b32 s77, vcc_lo, s74\n"
  "s_cbranch_scc1 L_p1ac_%=\n"
  "L_p1ad_%=:\n"
  "s_barrier\n"
  "s_and_b32 s70, s33, 1\n"
  "s_lshl_b32 s70, s70, 3\n"
  "s_xor_b32 s84, s5, 3\n"
  "s_or_b32 s84, s84, s70\n"
  "s_lshl_b32 s84, s84, 17\n"
  "s_add_u32 m0, s68, 131072\n"
  "s_nop 0\n"
  "buffer_load_dwordx4 v223, s[36:39], s84 offen sc1 lds\n"
  "buffer_load_dwordx4 v223, s[36:39], s84 offen offset:1024 sc1 lds\n"
  "buffer_load_dwordx4 v223, s[36:39], s84 offen offset:2048 sc1 lds\n"
  "buffer_load_dwordx4 v223, s[36:39], s84 offen offset:3072 sc1 lds\n"
  "s_add_u32 s84, s84, 4096\n"
  "s_add_u32 m0, s68, 135168\n"
  "s_nop 0\n"
  "buffer_load_dwordx4 v223, s[36:39], s84 offen sc1 lds\n"
  "buffer_load_dwordx4 v223, s[36:39], s84 offen offset:1024 sc1 lds\n"
  "buffer_load_dwordx4 v223, s[36:39], s84 offen offset:2048 sc1 lds\n"
  "buffer_load_dwordx4 v223, s[36:39], s84 offen offset:3072 sc1 lds\n"
  "s_xor_b32 s84, s5, 7\n"
  "s_or_b32 s84, s84, s70\n"
  "s_lshl_b32 s84, s84, 17\n"
  "s_waitcnt vmcnt(8)\n"
  "s_barrier\n"
  "ds_read_b64_tr_b16 a[32:33], v200 offset:0\n"
  "ds_read_b64_tr_b16 a[36:37], v201 offset:0\n"
  "s_add_u32 m0, s68, 65536\n"
  "s_nop 0\n"
  "buffer_load_dwordx4 v223, s[36:39], s84 offen sc1 lds\n"
  "ds_read_b64_tr_b16 a[40:41], v200 offset:256\n"
  "ds_read_b64_tr_b16 a[44:45], v201 offset:256\n"
  "ds_read_b64_tr_b16 a[48:49], v200 offset:512\n"
  "ds_read_b64_tr_b16 a[52:53], v201 offset:512\n"
  "ds_read_b64_tr_b16 a[56:57], v200 offset:768\n"
  "ds_read_b64_tr_b16 a[60:61], v201 offset:768\n"
  "buffer_load_dwordx4 v223, s[36:39], s84 offen offset:1024 sc1 lds\n"
  "ds_read_b64_tr_b16 a[34:35], v200 offset:16384\n"
  "ds_read_b64_tr_b16 a[38:39], v201 offset:16384\n"
  "ds_read_b64_tr_b16 a[42:43], v200 offset:16640\n"
  "ds_read_b64_tr_b16 a[46:47], v201 offset:16640\n"
  "ds_read_b64_tr_b16 a[50:51], v200 offset:16896\n"
  "ds_read_b64_tr_b16 a[54:55], v201 offset:16896\n"
  "buffer_load_dwordx4 v223, s[36:39], s84 offen offset:2048 sc1 lds\n"
  "ds_read_b64_tr_b16 a[58:59], v200 offset:17152\n"
  "ds_read_b64_tr_b16 a[62:63], v201 offset:17152\n"
  "ds_read_b64_tr_b16 a[160:161], v200 offset:32768\n"
  "ds_read_b64_tr_b16 a[164:165], v201 offset:32768\n"
  "ds_read_b64_tr_b16 a[168:169], v200 offset:33024\n"
  "ds_read_b64_tr_b16 a[172:173], v201 offset:33024\n"
  "buffer_load_dwordx4 v223, s[36:39], s84 offen offset:3072 sc1 lds\n"
  "s_add_u32 s84, s84, 4096\n"
  "ds_read_b64_tr_b16 a[176:177], v200 offset:33280\n"
  "ds_read_b64_tr_b16 a[180:181], v201 offset:33280\n"
  "ds_read_b64_tr_b16 a[184:185], v200 offset:33536\n"
  "ds_read_b64_tr_b16 a[188:189], v201 offset:33536\n"
  "ds_read_b64_tr_b16 a[162:163], v200 offset:49152\n"
  "ds_read_b64_tr_b16 a[166:167], v201 offset:49152\n"
  "s_add_u32 m0, s68, 69632\n"
  "s_nop 0\n"
  "buffer_load_dwordx4 v223, s[36:39], s84 offen sc1 lds\n"
  "ds_read_b64_tr_b16 a[170:171], v200 offset:49408\n"
  "ds_read_b64_tr_b16 a[174:175], v201 offset:49408\n"
  "ds_read_b64_tr_b16 a[178:179], v200 offset:49664\n"
  "ds_read_b64_tr_b16 a[182:183], v201 offset:49664\n"
  "ds_read_b64_tr_b16 a[186:187], v200 offset:49920\n"
  "ds_read_b64_tr_b16 a[190:191], v201 offset:49920\n"
  "buffer_load_dwordx4 v223, s[36:39], s84 offen offset:1024 sc1 lds\n"
  "ds_read_b64_tr_b16 a[64:65], v202 offset:32768\n"
  "ds_read_b64_tr_b16 a[68:69], v203 offset:32768\n"
  "ds_read_b64_tr_b16 a[72:73], v202 offset:33024\n"
  "ds_read_b64_tr_b16 a[76:77], v203 offset:33024\n"
  "ds_read_b64_tr_b16 a[80:81], v202 offset:33280\n"
  "ds_read_b64_tr_b16 a[84:85], v203 offset:33280\n"
  "buffer_load_dwordx4 v223, s[36:39], s84 offen offset:2048 sc1 lds\n"
  "ds_read_b64_tr_b16 a[88:89], v202 offset:33536\n"
  "ds_read_b64_tr_b16 a[92:93], v203 offset:33536\n"
  "ds_read_b64_tr_b16 a[66:67], v202 offset:49152\n"
  "ds_read_b64_tr_b16 a[70:71], v203 offset:49152\n"
  "ds_read_b64_tr_b16 a[74:75], v202 offset:49408\n"
  "ds_read_b64_tr_b16 a[78:79], v203 offset:49408\n"
  "buffer_load_dwordx4 v223, s[36:39], s84 offen offset:3072 sc1 lds\n"
  "s_xor_b32 s84, s5, 2\n"
  "s_or_b32 s84, s84, s70\n"
  "s_lshl_b32 s84, s84, 17\n"
  "ds_read_b64_tr_b16 a[82:83], v202 offset:49664\n"
  "ds_read_b64_tr_b16 a[86:87], v203 offset:49664\n"
  "ds_read_b64_tr_b16 a[90:91], v202 offset:49920\n"
  "ds_read_b64_tr_b16 a[94:95], v203 offset:49920\n"
  "buffer_load_dword v222, v227, s[56:59], 0 offen sc1\n"
  "s_waitcnt vmcnt(9) lgkmcnt(0)\n"
  "s_barrier\n"
  "ds_read_b128 a[96:99], v104 offset:0\n"
  "ds_read_b128 a[100:103], v105 offset:0\n"
  "ds_read_b128 a[104:107], v106 offset:0\n"
  "ds_read_b128 a[108:111], v107 offset:0\n"
  "ds_read_b128 a[112:115], v104 offset:4096\n"
  "ds_read_b128 a[116:119], v105 offset:4096\n"
  "ds_read_b128 a[120:123], v106 offset:4096\n"
  "ds_read_b128 a[124:127], v107 offset:4096\n"
  "s_waitcnt lgkmcnt(7)\n"
  "v_mfma_f32_16x16x32_bf16 v[160:163], v[0:3], a[96:99], 0\n"
  "s_waitcnt lgkmcnt(6)\n"
  "ds_read_b128 a[128:131], v104 offset:8192\n"
  "v_mfma_f32_16x16x32_bf16 v[160:163], v[8:11], a[100:103], v[160:163]\n"
  "s_waitcnt lgkmcnt(6)\n"
  "ds_read_b128 a[132:135], v105 offset:8192\n"
  "v_mfma_f32_16x16x32_bf16 v[160:163], v[16:19], a[104:107], v[160:163]\n"
  "s_waitcnt lgkmcnt(6)\n"
  "ds_read_b128 a[136:139], v106 offset:8192\n"
  "v_mfma_f32_16x16x32_bf16 v[160:163], v[24:27], a[108:111], v[160:163]\n"
  "ds_read_b128 a[140:143], v107 offset:8192\n"
  "v_mfma_f32_16x16x32_bf16 v[164:167], v[28:31], a[108:111], 0\n"
  "buffer_store_dwordx4 v[120:123], v206, s[52:55], s12 offen sc1\n"
  "v_mfma_f32_16x16x32_bf16 v[164:167], v[20:23], a[104:107], v[164:167]\n"
  "v_mfma_f32_16x16x32_bf16 v[164:167], v[12:15], a[100:103], v[164:167]\n"
  "v_mfma_f32_16x16x32_bf16 v[164:167], v[4:7], a[96:99], v[164:167]\n"
  "s_waitcnt lgkmcnt(7)\n"
  "v_mfma_f32_16x16x32_bf16 v[164:167], v[36:39], a[112:115], v[164:167]\n"
  "s_waitcnt lgkmcnt(6)\n"
  "ds_read_b128 a[96:99], v104 offset:12288\n"
  "v_mfma_f32_16x16x32_bf16 v[164:167], v[44:47], a[116:119], v[164:167]\n"
  "s_waitcnt lgkmcnt(6)\n"
  "ds_read_b128 a[100:103], v105 offset:12288\n"
  "v_mfma_f32_16x16x32_bf16 v[164:167], v[52:55], a[120:123], v[164:167]\n"
  "s_waitcnt lgkmcnt(6)\n"
  "ds_read_b128 a[104:107], v106 offset:12288\n"
  "v_mfma_f32_16x16x32_bf16 v[164:167], v[60:63], a[124:127], v[164:167]\n"
  "ds_read_b128 a[108:111], v107 offset:12288\n"
  "v_mfma_f32_16x16x32_bf16 v[160:163], v[56:59], a[124:127], v[160:163]\n"
  "buffer_store_dwordx4 v[124:127], v207, s[52:55], s12 offen offset:1024 sc1\n"
  "v_mfma_f32_16x16x32_bf16 v[160:163], v[48:51], a[120:123], v[160:163]\n"
  "v_mfma_f32_16x16x32_bf16 v[160:163], v[40:43], a[116:119], v[160:163]\n"
  "v_mfma_f32_16x16x32_bf16 v[160:163], v[32:35], a[112:115], v[160:163]\n"
  "s_waitcnt lgkmcnt(7)\n"
  "v_mfma_f32_16x16x32_bf16 v[160:163], v[64:67], a[128:131], v[160:163]\n"
  "s_waitcnt lgkmcnt(6)\n"
  "ds_read_b128 a[112:115], v104 offset:16384\n"
  "v_mfma_f32_16x16x32_bf16 v[160:163], v[72:75], a[132:135], v[160:163]\n"
  "s_waitcnt lgkmcnt(6)\n"
  "ds_read_b128 a[116:119], v105 offset:16384\n"
  "v_mfma_f32_16x16x32_bf16 v[160:163], v[80:83], a[136:139], v[160:163]\n"
  "s_waitcnt lgkmcnt(6)\n"
  "ds_read_b128 a[120:123], v106 offset:16384\n"
  "v_mfma_f32_16x16x32_bf16 v[160:163], v[88:91], a[140:143], v[160:163]\n"
  "ds_read_b128 a[124:127], v107 offset:16384\n"
  "v_mfma_f32_16x16x32_bf16 v[164:167], v[92:95], a[140:143], v[164:167]\n"
  "buffer_store_dwordx4 v[128:131], v208, s[52:55], s12 offen offset:2048 sc1\n"
  "v_mfma_f32_16x16x32_bf16 v[164:167], v[84:87], a[136:139], v[164:167]\n"
  "v_mfma_f32_16x16x32_bf16 v[164:167], v[76:79], a[132:135], v[164:167]\n"
  "s_add_u32 m0, s68, 0\n"
  "v_mfma_f32_16x16x32_bf16 v[164:167], v[68:71], a[128:131], v[164:167]\n"
  "s_waitcnt lgkmcnt(7)\n"
  "v_mfma_f32_16x16x32_bf16 v[164:167], a[196:199], a[96:99], v[164:167]\n"
  "s_waitcnt lgkmcnt(6)\n"
  "ds_read_b128 a[128:131], v104 offset:20480\n"
  "v_mfma_f32_16x16x32_bf16 v[164:167], a[204:207], a[100:103], v[164:167]\n"
  "s_waitcnt lgkmcnt(6)\n"
  "ds_read_b128 a[132:135], v105 offset:20480\n"
  "v_mfma_f32_16x16x32_bf16 v[164:167], a[212:215], a[104:107], v[164:167]\n"
  "s_waitcnt lgkmcnt(6)\n"
  "buffer_load_dwordx4 v223, s[36:39], s84 offen sc1 lds\n"
  "v_mfma_f32_16x16x32_bf16 v[164:167], a[220:223], a[108:111], v[164:167]\n"
  "ds_read_b128 a[136:139], v106 offset:20480\n"
  "v_mfma_f32_16x16x32_bf16 v[160:163], a[216:219], a[108:111], v[160:163]\n"
  "buffer_store_dwordx4 v[132:135], v209, s[52:55], s12 offen offset:3072 sc1\n"
  "v_mfma_f32_16x16x32_bf16 v[160:163], a[208:211], a[104:107], v[160:163]\n"
  "ds_read_b128 a[140:143], v107 offset:20480\n"
  "v_mfma_f32_16x16x32_bf16 v[160:163], a[200:203], a[100:103], v[160:163]\n"
  "v_mfma_f32_16x16x32_bf16 v[160:163], a[192:195], a[96:99], v[160:163]\n"
  "s_waitcnt lgkmcnt(7)\n"
  "v_mfma_f32_16x16x32_bf16 v[160:163], a[0:3], a[112:115], v[160:163]\n"
  "s_waitcnt lgkmcnt(6)\n"
  "buffer_load_dwordx4 v223, s[36:39], s84 offen offset:1024 sc1 lds\n"
  "v_mfma_f32_16x16x32_bf16 v[160:163], a[8:11], a[116:119], v[160:163]\n"
  "s_waitcnt lgkmcnt(5)\n"
  "ds_read_b128 a[96:99], v104 offset:24576\n"
  "v_mfma_f32_16x16x32_bf16 v[160:163], a[16:19], a[120:123], v[160:163]\n"
  "s_waitcnt lgkmcnt(5)\n"
  "ds_read_b128 a[100:103], v105 offset:24576\n"
  "v_mfma_f32_16x16x32_bf16 v[160:163], a[24:27], a[124:127], v[160:163]\n"
  "ds_read_b128 a[104:107], v106 offset:24576\n"
  "v_mfma_f32_16x16x32_bf16 v[164:167], a[28:31], a[124:127], v[164:167]\n"
  "ds_read_b128 a[108:111], v107 offset:24576\n"
  "v_mfma_f32_16x16x32_bf16 v[164:167], a[20:23], a[120:123], v[164:167]\n"
  "buffer_load_dwordx4 v223, s[36:39], s84 offen offset:2048 sc1 lds\n"
  "v_mfma_f32_16x16x32_bf16 v[164:167], a[12:15], a[116:119], v[164:167]\n"
  "v_mfma_f32_16x16x32_bf16 v[164:167], a[4:7], a[112:115], v[164:167]\n"
  "s_waitcnt lgkmcnt(7)\n"
  "v_mfma_f32_16x16x32_bf16 v[164:167], a[36:39], a[128:131], v[164:167]\n"
  "s_waitcnt lgkmcnt(6)\n"
  "ds_read_b128 a[112:115], v104 offset:28672\n"
  "v_mfma_f32_16x16x32_bf16 v[164:167], a[44:47], a[132:135], v[164:167]\n"
  "s_waitcnt lgkmcnt(6)\n"
  "ds_read_b128 a[116:119], v105 offset:28672\n"
  "v_mfma_f32_16x16x32_bf16 v[164:167], a[52:55], a[136:139], v[164:167]\n"
  "s_waitcnt lgkmcnt(6)\n"
  "ds_read_b128 a[120:123], v106 offset:28672\n"
  "v_mfma_f32_16x16x32_bf16 v[164:167], a[60:63], a[140:143], v[164:167]\n"
  "buffer_load_dwordx4 v223, s[36:39], s84 offen offset:3072 sc1 lds\n"
  "v_mfma_f32_16x16x32_bf16 v[160:163], a[56:59], a[140:143], v[160:163]\n"
  "s_add_u32 s84, s84, 4096\n"
  "ds_read_b128 a[124:127], v107 offset:28672\n"
  "v_mfma_f32_16x16x32_bf16 v[160:163], a[48:51], a[136:139], v[160:163]\n"
  "v_mfma_f32_16x16x32_bf16 v[160:163], a[40:43], a[132:135], v[160:163]\n"
  "s_add_u32 m0, s68, 4096\n"
  "v_mfma_f32_16x16x32_bf16 v[160:163], a[32:35], a[128:131], v[160:163]\n"
  "s_waitcnt lgkmcnt(7)\n"
  "v_mfma_f32_16x16x32_bf16 v[160:163], a[160:163], a[96:99], v[160:163]\n"
  "s_waitcnt vmcnt(9)\n"
  "s_barrier\n"
  "s_waitcnt lgkmcnt(6)\n"
  "v_mfma_f32_16x16x32_bf16 v[160:163], a[168:171], a[100:103], v[160:163]\n"
  "s_waitcnt lgkmcnt(5)\n"
  "buffer_load_dwordx4 v223, s[36:39], s84 offen sc1 lds\n"
  "v_mfma_f32_16x16x32_bf16 v[160:163], a[176:179], a[104:107], v[160:163]\n"
  "s_waitcnt lgkmcnt(4)\n"
  "ds_read_b128 a[128:131], v100 offset:0\n"
  "v_mfma_f32_16x16x32_bf16 v[160:163], a[184:187], a[108:111], v[160:163]\n"
  "ds_read_b128 a[132:135], v101 offset:0\n"
  "v_mfma_f32_16x16x32_bf16 v[164:167], a[188:191], a[108:111], v[164:167]\n"
  "ds_read_b128 a[136:139], v102 offset:0\n"
  "v_mfma_f32_16x16x32_bf16 v[164:167], a[180:183], a[104:107], v[164:167]\n"
  "ds_read_b128 a[140:143], v103 offset:0\n"
  "v_mfma_f32_16x16x32_bf16 v[164:167], a[172:175], a[100:103], v[164:167]\n"
  "buffer_load_dwordx4 v223, s[36:39], s84 offen offset:1024 sc1 lds\n"
  "v_mfma_f32_16x16x32_bf16 v[164:167], a[164:167], a[96:99], v[164:167]\n"
  "s_waitcnt lgkmcnt(7)\n"
  "v_mfma_f32_16x16x32_bf16 v[164:167], a[68:71], a[112:115], v[164:167]\n"
  "s_waitcnt lgkmcnt(6)\n"
  "ds_read_b128 a[96:99], v100 offset:4096\n"
  "v_mfma_f32_16x16x32_bf16 v[164:167], a[76:79], a[116:119], v[164:167]\n"
  "s_waitcnt lgkmcnt(6)\n"
  "ds_read_b128 a[100:103], v101 offset:4096\n"
  "v_mfma_f32_16x16x32_bf16 v[164:167], a[84:87], a[120:123], v[164:167]\n"
  "s_waitcnt lgkmcnt(6)\n"
  "ds_read_b128 a[104:107], v102 offset:4096\n"
  "v_mfma_f32_16x16x32_bf16 v[164:167], a[92:95], a[124:127], v[164:167]\n"
  "buffer_load_dwordx4 v223, s[36:39], s84 offen offset:2048 sc1 lds\n"
  "v_mfma_f32_16x16x32_bf16 v[160:163], a[88:91], a[124:127], v[160:163]\n"
  "ds_read_b128 a[108:111], v103 offset:4096\n"
  "v_mfma_f32_16x16x32_bf16 v[160:163], a[80:83], a[120:123], v[160:163]\n"
  "v_mfma_f32_16x16x32_bf16 v[160:163], a[72:75], a[116:119], v[160:163]\n"
  "v_mfma_f32_16x16x32_bf16 v[160:163], a[64:67], a[112:115], v[160:163]\n"
  "s_waitcnt lgkmcnt(7)\n"
  "v_mfma_f32_16x16x32_bf16 v[168:171], v[0:3], a[128:131], 0\n"
  "s_waitcnt lgkmcnt(6)\n"
  "ds_read_b128 a[112:115], v100 offset:8192\n"
  "v_mfma_f32_16x16x32_bf16 v[168:171], v[8:11], a[132:135], v[168:171]\n"
  "s_waitcnt lgkmcnt(6)\n"
  "buffer_load_dwordx4 v223, s[36:39], s84 offen offset:3072 sc1 lds\n"
  "v_mfma_f32_16x16x32_bf16 v[168:171], v[16:19], a[136:139], v[168:171]\n"
  "s_waitcnt lgkmcnt(5)\n"
  "ds_read_b128 a[116:119], v101 offset:8192\n"
  "v_mfma_f32_16x16x32_bf16 v[168:171], v[24:27], a[140:143], v[168:171]\n"
  "s_xor_b32 s84, s5, 6\n"
  "ds_read_b128 a[120:123], v102 offset:8192\n"
  "v_mfma_f32_16x16x32_bf16 v[172:175], v[28:31], a[140:143], 0\n"
  "s_or_b32 s84, s84, s70\n"
  "v_cvt_pk_bf16_f32 v186, v164, v165\n"
  "v_cvt_pk_bf16_f32 v187, v166, v167\n"
  "v_mfma_f32_16x16x32_bf16 v[172:175], v[20:23], a[136:139], v[172:175]\n"
  "s_lshl_b32 s84, s84, 17\n"
  "v_cvt_pk_bf16_f32 v184, v160, v161\n"
  "v_cvt_pk_bf16_f32 v185, v162, v163\n"
  "v_mfma_f32_16x16x32_bf16 v[172:175], v[12:15], a[132:135], v[172:175]\n"
  "s_add_u32 m0, s68, 32768\n"
  "ds_read_b128 a[124:127], v103 offset:8192\n"
  "v_mfma_f32_16x16x32_bf16 v[172:175], v[4:7], a[128:131], v[172:175]\n"
  "s_waitcnt lgkmcnt(7)\n"
  "buffer_load_dwordx4 v223, s[36:39], s84 offen sc1 lds\n"
  "v_mfma_f32_16x16x32_bf16 v[172:175], v[36:39], a[96:99], v[172:175]\n"
  "s_waitcnt lgkmcnt(6)\n"
  "ds_read_b128 a[128:131], v100 offset:12288\n"
  "v_mfma_f32_16x16x32_bf16 v[172:175], v[44:47], a[100:103], v[172:175]\n"
  "s_waitcnt lgkmcnt(6)\n"
  "ds_read_b128 a[132:135], v101 offset:12288\n"
  "v_mfma_f32_16x16x32_bf16 v[172:175], v[52:55], a[104:107], v[172:175]\n"
  "s_waitcnt lgkmcnt(6)\n"
  "ds_read_b128 a[136:139], v102 offset:12288\n"
  "v_mfma_f32_16x16x32_bf16 v[172:175], v[60:63], a[108:111], v[172:175]\n"
  "ds_read_b128 a[140:143], v103 offset:12288\n"
  "v_mfma_f32_16x16x32_bf16 v[168:171], v[56:59], a[108:111], v[168:171]\n"
  "v_mfma_f32_16x16x32_bf16 v[168:171], v[48:51], a[104:107], v[168:171]\n"
  "v_mfma_f32_16x16x32_bf16 v[168:171], v[40:43], a[100:103], v[168:171]\n"
  "buffer_load_dwordx4 v223, s[36:39], s84 offen offset:1024 sc1 lds\n"
  "v_mfma_f32_16x16x32_bf16 v[168:171], v[32:35], a[96:99], v[168:171]\n"
  "s_waitcnt lgkmcnt(7)\n"
  "buffer_store_dwordx4 v[136:139], v206, s[52:55], s13 offen sc1\n"
  "v_mfma_f32_16x16x32_bf16 v[168:171], v[64:67], a[112:115], v[168:171]\n"
  "s_waitcnt lgkmcnt(6)\n"
  "ds_read_b128 a[96:99], v100 offset:16384\n"
  "v_mfma_f32_16x16x32_bf16 v[168:171], v[72:75], a[116:119], v[168:171]\n"
  "s_waitcnt lgkmcnt(6)\n"
  "ds_read_b128 a[100:103], v101 offset:16384\n"
  "v_mfma_f32_16x16x32_bf16 v[168:171], v[80:83], a[120:123], v[168:171]\n"
  "s_waitcnt lgkmcnt(6)\n"
  "ds_read_b128 a[104:107], v102 offset:16384\n"
  "v_mfma_f32_16x16x32_bf16 v[168:171], v[88:91], a[124:127], v[168:171]\n"
  "ds_read_b128 a[108:111], v103 offset:16384\n"
  "v_mfma_f32_16x16x32_bf16 v[172:175], v[92:95], a[124:127], v[172:175]\n"
  "v_mfma_f32_16x16x32_bf16 v[172:175], v[84:87], a[120:123], v[172:175]\n"
  "v_mfma_f32_16x16x32_bf16 v[172:175], v[76:79], a[116:119], v[172:175]\n"
  "buffer_load_dwordx4 v223, s[36:39], s84 offen offset:2048 sc1 lds\n"
  "v_mfma_f32_16x16x32_bf16 v[172:175], v[68:71], a[112:115], v[172:175]\n"
  "s_waitcnt lgkmcnt(7)\n"
  "v_mfma_f32_16x16x32_bf16 v[172:175], a[196:199], a[128:131], v[172:175]\n"
  "s_waitcnt lgkmcnt(6)\n"
  "ds_read_b128 a[112:115], v100 offset:20480\n"
  "v_mfma_f32_16x16x32_bf16 v[172:175], a[204:207], a[132:135], v[172:175]\n"
  "s_waitcnt lgkmcnt(6)\n"
  "ds_read_b128 a[116:119], v101 offset:20480\n"
  "v_mfma_f32_16x16x32_bf16 v[172:175], a[212:215], a[136:139], v[172:175]\n"
  "s_waitcnt lgkmcnt(6)\n"
  "ds_read_b128 a[120:123], v102 offset:20480\n"
  "v_mfma_f32_16x16x32_bf16 v[172:175], a[220:223], a[140:143], v[172:175]\n"
  "ds_read_b128 a[124:127], v103 offset:20480\n"
  "v_mfma_f32_16x16x32_bf16 v[168:171], a[216:219], a[140:143], v[168:171]\n"
  "v_mfma_f32_16x16x32_bf16 v[168:171], a[208:211], a[136:139], v[168:171]\n"
  "buffer_load_dwordx4 v223, s[36:39], s84 offen offset:3072 sc1 lds\n"
  "v_mfma_f32_16x16x32_bf16 v[168:171], a[200:203], a[132:135], v[168:171]\n"
  "s_add_u32 s84, s84, 4096\n"
  "buffer_store_dwordx4 v[140:143], v207, s[52:55], s13 offen offset:1024 sc1\n"
  "v_mfma_f32_16x16x32_bf16 v[168:171], a[192:195], a[128:131], v[168:171]\n"
  "s_waitcnt lgkmcnt(7)\n"
  "v_mfma_f32_16x16x32_bf16 v[168:171], a[0:3], a[96:99], v[168:171]\n"
  "s_waitcnt lgkmcnt(6)\n"
  "ds_read_b128 a[128:131], v100 offset:24576\n"
  "v_mfma_f32_16x16x32_bf16 v[168:171], a[8:11], a[100:103], v[168:171]\n"
  "s_waitcnt lgkmcnt(6)\n"
  "ds_read_b128 a[132:135], v101 offset:24576\n"
  "v_mfma_f32_16x16x32_bf16 v[168:171], a[16:19], a[104:107], v[168:171]\n"
  "s_waitcnt lgkmcnt(6)\n"
  "ds_read_b128 a[136:139], v102 offset:24576\n"
  "v_mfma_f32_16x16x32_bf16 v[168:171], a[24:27], a[108:111], v[168:171]\n"
  "s_add_u32 m0, s68, 36864\n"
  "ds_read_b128 a[140:143], v103 offset:24576\n"
  "v_mfma_f32_16x16x32_bf16 v[172:175], a[28:31], a[108:111], v[172:175]\n"
  "buffer_load_dwordx4 v223, s[36:39], s84 offen sc1 lds\n"
  "v_mfma_f32_16x16x32_bf16 v[172:175], a[20:23], a[104:107], v[172:175]\n"
  "v_mfma_f32_16x16x32_bf16 v[172:175], a[12:15], a[100:103], v[172:175]\n"
  "v_mfma_f32_16x16x32_bf16 v[172:175], a[4:7], a[96:99], v[172:175]\n"
  "s_waitcnt lgkmcnt(7)\n"
  "v_mfma_f32_16x16x32_bf16 v[172:175], a[36:39], a[112:115], v[172:175]\n"
  "s_waitcnt lgkmcnt(6)\n"
  "ds_read_b128 a[96:99], v100 offset:28672\n"
  "v_mfma_f32_16x16x32_bf16 v[172:175], a[44:47], a[116:119], v[172:175]\n"
  "s_waitcnt lgkmcnt(6)\n"
  "ds_read_b128 a[100:103], v101 offset:28672\n"
  "v_mfma_f32_16x16x32_bf16 v[172:175], a[52:55], a[120:123], v[172:175]\n"
  "s_waitcnt lgkmcnt(6)\n"
  "ds_read_b128 a[104:107], v102 offset:28672\n"
  "v_mfma_f32_16x16x32_bf16 v[172:175], a[60:63], a[124:127], v[172:175]\n"
  "buffer_load_dwordx4 v223, s[36:39], s84 offen offset:1024 sc1 lds\n"
  "v_mfma_f32_16x16x32_bf16 v[168:171], a[56:59], a[124:127], v[168:171]\n"
  "ds_read_b128 a[108:111], v103 offset:28672\n"
  "v_mfma_f32_16x16x32_bf16 v[168:171], a[48:51], a[120:123], v[168:171]\n"
  "buffer_store_dwordx4 v[144:147], v208, s[52:55], s13 offen offset:2048 sc1\n"
  "v_mfma_f32_16x16x32_bf16 v[168:171], a[40:43], a[116:119], v[168:171]\n"
  "v_mfma_f32_16x16x32_bf16 v[168:171], a[32:35], a[112:115], v[168:171]\n"
  "s_waitcnt lgkmcnt(7)\n"
  "v_mfma_f32_16x16x32_bf16 v[168:171], a[160:163], a[128:131], v[168:171]\n"
  "s_waitcnt vmcnt(9)\n"
  "s_barrier\n"
  "s_waitcnt lgkmcnt(6)\n"
  "v_mfma_f32_16x16x32_bf16 v[168:171], a[168:171], a[132:135], v[168:171]\n"
  "s_waitcnt lgkmcnt(5)\n"
  "buffer_store_dword v222, v218, s[56:59], s6 offen offset:384 sc1\n"
  "v_mfma_f32_16x16x32_bf16 v[168:171], a[176:179], a[136:139], v[168:171]\n"
  "s_waitcnt lgkmcnt(4)\n"
  "buffer_store_dword v222, v218, s[56:59], s6 offen offset:256 sc1\n"
  "v_mfma_f32_16x16x32_bf16 v[168:171], a[184:187], a[140:143], v[168:171]\n"
  "buffer_load_dwordx4 v223, s[36:39], s84 offen offset:2048 sc1 lds\n"
  "v_mfma_f32_16x16x32_bf16 v[172:175], a[188:191], a[140:143], v[172:175]\n"
  "ds_read_b128 a[112:115], v96 offset:0\n"
  "v_mfma_f32_16x16x32_bf16 v[172:175], a[180:183], a[136:139], v[172:175]\n"
  "ds_read_b128 a[116:119], v97 offset:0\n"
  "v_mfma_f32_16x16x32_bf16 v[172:175], a[172:175], a[132:135], v[172:175]\n"
  "ds_read_b128 a[120:123], v98 offset:0\n"
  "v_mfma_f32_16x16x32_bf16 v[172:175], a[164:167], a[128:131], v[172:175]\n"
  "s_waitcnt lgkmcnt(6)\n"
  "ds_read_b128 a[124:127], v99 offset:0\n"
  "v_mfma_f32_16x16x32_bf16 v[172:175], a[68:71], a[96:99], v[172:175]\n"
  "s_waitcnt lgkmcnt(6)\n"
  "ds_read_b128 a[128:131], v96 offset:4096\n"
  "v_mfma_f32_16x16x32_bf16 v[172:175], a[76:79], a[100:103], v[172:175]\n"
  "s_waitcnt lgkmcnt(6)\n"
  "ds_read_b128 a[132:135], v97 offset:4096\n"
  "v_mfma_f32_16x16x32_bf16 v[172:175], a[84:87], a[104:107], v[172:175]\n"
  "s_waitcnt lgkmcnt(6)\n"
  "buffer_load_dwordx4 v223, s[36:39], s84 offen offset:3072 sc1 lds\n"
  "v_mfma_f32_16x16x32_bf16 v[172:175], a[92:95], a[108:111], v[172:175]\n"
  "s_and_b32 s84, s33, 1\n"
  "ds_read_b128 a[136:139], v98 offset:4096\n"
  "v_mfma_f32_16x16x32_bf16 v[168:171], a[88:91], a[108:111], v[168:171]\n"
  "s_add_u32 s84, s84, 16\n"
  "s_lshl_b32 s84, s84, 17\n"
  "buffer_store_dwordx4 v[148:151], v209, s[52:55], s13 offen offset:3072 sc1\n"
  "v_mfma_f32_16x16x32_bf16 v[168:171], a[80:83], a[104:107], v[168:171]\n"
  "ds_read_b128 a[140:143], v99 offset:4096\n"
  "v_mfma_f32_16x16x32_bf16 v[168:171], a[72:75], a[100:103], v[168:171]\n"
  "s_add_u32 m0, s68, 98304\n"
  "v_mfma_f32_16x16x32_bf16 v[168:171], a[64:67], a[96:99], v[168:171]\n"
  "s_waitcnt lgkmcnt(7)\n"
  "v_mfma_f32_16x16x32_bf16 v[160:163], v[0:3], a[112:115], 0\n"
  "s_waitcnt lgkmcnt(6)\n"
  "ds_read_b128 a[96:99], v96 offset:8192\n"
  "v_mfma_f32_16x16x32_bf16 v[160:163], v[8:11], a[116:119], v[160:163]\n"
  "s_waitcnt lgkmcnt(6)\n"
  "buffer_load_dwordx4 v223, s[36:39], s84 offen sc1 lds\n"
  "v_mfma_f32_16x16x32_bf16 v[160:163], v[16:19], a[120:123], v[160:163]\n"
  "s_waitcnt lgkmcnt(5)\n"
  "ds_read_b128 a[100:103], v97 offset:8192\n"
  "v_mfma_f32_16x16x32_bf16 v[160:163], v[24:27], a[124:127], v[160:163]\n"
  "ds_read_b128 a[104:107], v98 offset:8192\n"
  "v_mfma_f32_16x16x32_bf16 v[164:167], v[28:31], a[124:127], 0\n"
  "v_cvt_pk_bf16_f32 v190, v172, v173\n"
  "v_cvt_pk_bf16_f32 v191, v174, v175\n"
  "v_mfma_f32_16x16x32_bf16 v[164:167], v[20:23], a[120:123], v[164:167]\n"
  "v_cvt_pk_bf16_f32 v188, v168, v169\n"
  "v_cvt_pk_bf16_f32 v189, v170, v171\n"
  "v_mfma_f32_16x16x32_bf16 v[164:167], v[12:15], a[116:119], v[164:167]\n"
  "ds_read_b128 a[108:111], v99 offset:8192\n"
  "v_mfma_f32_16x16x32_bf16 v[164:167], v[4:7], a[112:115], v[164:167]\n"
  "s_waitcnt lgkmcnt(7)\n"
  "v_mfma_f32_16x16x32_bf16 v[164:167], v[36:39], a[128:131], v[164:167]\n"
  "s_waitcnt lgkmcnt(6)\n"
  "ds_read_b128 a[112:115], v96 offset:12288\n"
  "v_mfma_f32_16x16x32_bf16 v[164:167], v[44:47], a[132:135], v[164:167]\n"
  "s_waitcnt lgkmcnt(6)\n"
  "buffer_load_dwordx4 v223, s[36:39], s84 offen offset:1024 sc1 lds\n"
  "v_mfma_f32_16x16x32_bf16 v[164:167], v[52:55], a[136:139], v[164:167]\n"
  "s_waitcnt lgkmcnt(5)\n"
  "ds_read_b128 a[116:119], v97 offset:12288\n"
  "v_mfma_f32_16x16x32_bf16 v[164:167], v[60:63], a[140:143], v[164:167]\n"
  "ds_read_b128 a[120:123], v98 offset:12288\n"
  "v_mfma_f32_16x16x32_bf16 v[160:163], v[56:59], a[140:143], v[160:163]\n"
  "ds_read_b128 a[124:127], v99 offset:12288\n"
  "v_mfma_f32_16x16x32_bf16 v[160:163], v[48:51], a[136:139], v[160:163]\n"
  "v_mfma_f32_16x16x32_bf16 v[160:163], v[40:43], a[132:135], v[160:163]\n"
  "v_mfma_f32_16x16x32_bf16 v[160:163], v[32:35], a[128:131], v[160:163]\n"
  "s_waitcnt lgkmcnt(7)\n"
  "v_mfma_f32_16x16x32_bf16 v[160:163], v[64:67], a[96:99], v[160:163]\n"
  "s_waitcnt lgkmcnt(6)\n"
  "buffer_load_dwordx4 v223, s[36:39], s84 offen offset:2048 sc1 lds\n"
  "v_mfma_f32_16x16x32_bf16 v[160:163], v[72:75], a[100:103], v[160:163]\n"
  "s_waitcnt lgkmcnt(5)\n"
  "ds_read_b128 a[128:131], v96 offset:16384\n"
  "v_mfma_f32_16x16x32_bf16 v[160:163], v[80:83], a[104:107], v[160:163]\n"
  "s_waitcnt lgkmcnt(5)\n"
  "ds_read_b128 a[132:135], v97 offset:16384\n"
  "v_mfma_f32_16x16x32_bf16 v[160:163], v[88:91], a[108:111], v[160:163]\n"
  "ds_read_b128 a[136:139], v98 offset:16384\n"
  "v_mfma_f32_16x16x32_bf16 v[164:167], v[92:95], a[108:111], v[164:167]\n"
  "ds_read_b128 a[140:143], v99 offset:16384\n"
  "v_mfma_f32_16x16x32_bf16 v[164:167], v[84:87], a[104:107], v[164:167]\n"
  "v_mfma_f32_16x16x32_bf16 v[164:167], v[76:79], a[100:103], v[164:167]\n"
  "v_mfma_f32_16x16x32_bf16 v[164:167], v[68:71], a[96:99], v[164:167]\n"
  "s_waitcnt lgkmcnt(7)\n"
  "v_mfma_f32_16x16x32_bf16 v[164:167], a[196:199], a[112:115], v[164:167]\n"
  "s_waitcnt lgkmcnt(6)\n"
  "buffer_load_dwordx4 v223, s[36:39], s84 offen offset:3072 sc1 lds\n"
  "v_mfma_f32_16x16x32_bf16 v[164:167], a[204:207], a[116:119], v[164:167]\n"
  "s_waitcnt lgkmcnt(5)\n"
  "ds_read_b128 a[96:99], v96 offset:20480\n"
  "v_mfma_f32_16x16x32_bf16 v[164:167], a[212:215], a[120:123], v[164:167]\n"
  "s_waitcnt lgkmcnt(5)\n"
  "ds_read_b128 a[100:103], v97 offset:20480\n"
  "v_mfma_f32_16x16x32_bf16 v[164:167], a[220:223], a[124:127], v[164:167]\n"
  "s_add_u32 s84, s84, 4096\n"
  "ds_read_b128 a[104:107], v98 offset:20480\n"
  "v_mfma_f32_16x16x32_bf16 v[160:163], a[216:219], a[124:127], v[160:163]\n"
  "ds_read_b128 a[108:111], v99 offset:20480\n"
  "v_mfma_f32_16x16x32_bf16 v[160:163], a[208:211], a[120:123], v[160:163]\n"
  "v_mfma_f32_16x16x32_bf16 v[160:163], a[200:203], a[116:119], v[160:163]\n"
  "s_add_u32 m0, s68, 102400\n"
  "v_mfma_f32_16x16x32_bf16 v[160:163], a[192:195], a[112:115], v[160:163]\n"
  "s_waitcnt lgkmcnt(7)\n"
  "buffer_load_dwordx4 v223, s[36:39], s84 offen sc1 lds\n"
  "v_mfma_f32_16x16x32_bf16 v[160:163], a[0:3], a[128:131], v[160:163]\n"
  "s_waitcnt lgkmcnt(6)\n"
  "ds_read_b128 a[112:115], v96 offset:24576\n"
  "v_mfma_f32_16x16x32_bf16 v[160:163], a[8:11], a[132:135], v[160:163]\n"
  "s_waitcnt lgkmcnt(6)\n"
  "ds_read_b128 a[116:119], v97 offset:24576\n"
  "v_mfma_f32_16x16x32_bf16 v[160:163], a[16:19], a[136:139], v[160:163]\n"
  "s_waitcnt lgkmcnt(6)\n"
  "ds_read_b128 a[120:123], v98 offset:24576\n"
  "v_mfma_f32_16x16x32_bf16 v[160:163], a[24:27], a[140:143], v[160:163]\n"
  "ds_read_b128 a[124:127], v99 offset:24576\n"
  "v_mfma_f32_16x16x32_bf16 v[164:167], a[28:31], a[140:143], v[164:167]\n"
  "v_mfma_f32_16x16x32_bf16 v[164:167], a[20:23], a[136:139], v[164:167]\n"
  "v_mfma_f32_16x16x32_bf16 v[164:167], a[12:15], a[132:135], v[164:167]\n"
  "v_mfma_f32_16x16x32_bf16 v[164:167], a[4:7], a[128:131], v[164:167]\n"
  "s_waitcnt lgkmcnt(7)\n"
  "buffer_load_dwordx4 v223, s[36:39], s84 offen offset:1024 sc1 lds\n"
  "v_mfma_f32_16x16x32_bf16 v[164:167], a[36:39], a[96:99], v[164:167]\n"
  "s_waitcnt lgkmcnt(6)\n"
  "ds_read_b128 a[128:131], v96 offset:28672\n"
  "v_mfma_f32_16x16x32_bf16 v[164:167], a[44:47], a[100:103], v[164:167]\n"
  "s_waitcnt lgkmcnt(6)\n"
  "ds_read_b128 a[132:135], v97 offset:28672\n"
  "v_mfma_f32_16x16x32_bf16 v[164:167], a[52:55], a[104:107], v[164:167]\n"
  "s_waitcnt lgkmcnt(6)\n"
  "ds_read_b128 a[136:139], v98 offset:28672\n"
  "v_mfma_f32_16x16x32_bf16 v[164:167], a[60:63], a[108:111], v[164:167]\n"
  "ds_read_b128 a[140:143], v99 offset:28672\n"
  "v_mfma_f32_16x16x32_bf16 v[160:163], a[56:59], a[108:111], v[160:163]\n"
  "v_mfma_f32_16x16x32_bf16 v[160:163], a[48:51], a[104:107], v[160:163]\n"
  "v_mfma_f32_16x16x32_bf16 v[160:163], a[40:43], a[100:103], v[160:163]\n"
  "buffer_load_dwordx4 v223, s[36:39], s84 offen offset:2048 sc1 lds\n"
  "v_mfma_f32_16x16x32_bf16 v[160:163], a[32:35], a[96:99], v[160:163]\n"
  "s_waitcnt lgkmcnt(7)\n"
  "v_mfma_f32_16x16x32_bf16 v[160:163], a[160:163], a[112:115], v[160:163]\n"
  "s_waitcnt vmcnt(8)\n"
  "s_barrier\n"
  "s_waitcnt lgkmcnt(6)\n"
  "v_mfma_f32_16x16x32_bf16 v[160:163], a[168:171], a[116:119], v[160:163]\n"
  "s_waitcnt lgkmcnt(5)\n"
  "ds_read_b128 a[96:99], v96 offset:32768\n"
  "v_mfma_f32_16x16x32_bf16 v[160:163], a[176:179], a[120:123], v[160:163]\n"
  "s_waitcnt lgkmcnt(5)\n"
  "ds_read_b128 a[100:103], v97 offset:32768\n"
  "v_mfma_f32_16x16x32_bf16 v[160:163], a[184:187], a[124:127], v[160:163]\n"
  "ds_read_b128 a[104:107], v98 offset:32768\n"
  "v_mfma_f32_16x16x32_bf16 v[164:167], a[188:191], a[124:127], v[164:167]\n"
  "ds_read_b128 a[108:111], v99 offset:32768\n"
  "v_mfma_f32_16x16x32_bf16 v[164:167], a[180:183], a[120:123], v[164:167]\n"
  "v_mfma_f32_16x16x32_bf16 v[164:167], a[172:175], a[116:119], v[164:167]\n"
  "buffer_load_dwordx4 v223, s[36:39], s84 offen offset:3072 sc1 lds\n"
  "v_mfma_f32_16x16x32_bf16 v[164:167], a[164:167], a[112:115], v[164:167]\n"
  "s_waitcnt lgkmcnt(7)\n"
  "v_mfma_f32_16x16x32_bf16 v[164:167], a[68:71], a[128:131], v[164:167]\n"
  "s_waitcnt lgkmcnt(6)\n"
  "ds_read_b128 a[112:115], v96 offset:36864\n"
  "v_mfma_f32_16x16x32_bf16 v[164:167], a[76:79], a[132:135], v[164:167]\n"
  "s_waitcnt lgkmcnt(6)\n"
  "ds_read_b128 a[116:119], v97 offset:36864\n"
  "v_mfma_f32_16x16x32_bf16 v[164:167], a[84:87], a[136:139], v[164:167]\n"
  "s_waitcnt lgkmcnt(6)\n"
  "ds_read_b128 a[120:123], v98 offset:36864\n"
  "v_mfma_f32_16x16x32_bf16 v[164:167], a[92:95], a[140:143], v[164:167]\n"
  "s_xor_b32 s84, s5, 1\n"
  "ds_read_b128 a[124:127], v99 offset:36864\n"
  "v_mfma_f32_16x16x32_bf16 v[160:163], a[88:91], a[140:143], v[160:163]\n"
  "s_or_b32 s84, s84, s70\n"
  "v_mfma_f32_16x16x32_bf16 v[160:163], a[80:83], a[136:139], v[160:163]\n"
  "s_lshl_b32 s84, s84, 17\n"
  "v_mfma_f32_16x16x32_bf16 v[160:163], a[72:75], a[132:135], v[160:163]\n"
  "v_mfma_f32_16x16x32_bf16 v[160:163], a[64:67], a[128:131], v[160:163]\n"
  "s_waitcnt lgkmcnt(7)\n"
  "buffer_load_dword v217, v216, s[56:59], 0 offen offset:384 sc1\n"
  "v_mfma_f32_16x16x32_bf16 v[168:171], v[0:3], a[96:99], 0\n"
  "s_waitcnt lgkmcnt(6)\n"
  "ds_read_b128 a[128:131], v96 offset:40960\n"
  "v_mfma_f32_16x16x32_bf16 v[168:171], v[8:11], a[100:103], v[168:171]\n"
  "s_waitcnt lgkmcnt(6)\n"
  "ds_read_b128 a[132:135], v97 offset:40960\n"
  "v_mfma_f32_16x16x32_bf16 v[168:171], v[16:19], a[104:107], v[168:171]\n"
  "s_waitcnt lgkmcnt(6)\n"
  "ds_read_b128 a[136:139], v98 offset:40960\n"
  "v_mfma_f32_16x16x32_bf16 v[168:171], v[24:27], a[108:111], v[168:171]\n"
  "ds_read_b128 a[140:143], v99 offset:40960\n"
  "v_mfma_f32_16x16x32_bf16 v[172:175], v[28:31], a[108:111], 0\n"
  "v_cvt_pk_bf16_f32 v114, v164, v165\n"
  "v_cvt_pk_bf16_f32 v115, v166, v167\n"
  "v_mfma_f32_16x16x32_bf16 v[172:175], v[20:23], a[104:107], v[172:175]\n"
  "v_cvt_pk_bf16_f32 v112, v160, v161\n"
  "v_cvt_pk_bf16_f32 v113, v162, v163\n"
  "v_mfma_f32_16x16x32_bf16 v[172:175], v[12:15], a[100:103], v[172:175]\n"
  "v_mfma_f32_16x16x32_bf16 v[172:175], v[4:7], a[96:99], v[172:175]\n"
  "s_waitcnt lgkmcnt(7)\n"
  "v_mfma_f32_16x16x32_bf16 v[172:175], v[36:39], a[112:115], v[172:175]\n"
  "s_waitcnt lgkmcnt(6)\n"
  "ds_read_b128 a[96:99], v96 offset:45056\n"
  "v_mfma_f32_16x16x32_bf16 v[172:175], v[44:47], a[116:119], v[172:175]\n"
  "s_waitcnt lgkmcnt(6)\n"
  "ds_read_b128 a[100:103], v97 offset:45056\n"
  "v_mfma_f32_16x16x32_bf16 v[172:175], v[52:55], a[120:123], v[172:175]\n"
  "s_waitcnt lgkmcnt(6)\n"
  "ds_read_b128 a[104:107], v98 offset:45056\n"
  "v_mfma_f32_16x16x32_bf16 v[172:175], v[60:63], a[124:127], v[172:175]\n"
  "ds_read_b128 a[108:111], v99 offset:45056\n"
  "v_mfma_f32_16x16x32_bf16 v[168:171], v[56:59], a[124:127], v[168:171]\n"
  "v_mfma_f32_16x16x32_bf16 v[168:171], v[48:51], a[120:123], v[168:171]\n"
  "v_mfma_f32_16x16x32_bf16 v[168:171], v[40:43], a[116:119], v[168:171]\n"
  "v_mfma_f32_16x16x32_bf16 v[168:171], v[32:35], a[112:115], v[168:171]\n"
  "s_waitcnt lgkmcnt(7)\n"
  "v_mfma_f32_16x16x32_bf16 v[168:171], v[64:67], a[128:131], v[168:171]\n"
  "s_waitcnt lgkmcnt(6)\n"
  "ds_read_b128 a[112:115], v96 offset:49152\n"
  "v_mfma_f32_16x16x32_bf16 v[168:171], v[72:75], a[132:135], v[168:171]\n"
  "s_waitcnt lgkmcnt(6)\n"
  "ds_read_b128 a[116:119], v97 offset:49152\n"
  "v_mfma_f32_16x16x32_bf16 v[168:171], v[80:83], a[136:139], v[168:171]\n"
  "s_waitcnt lgkmcnt(6)\n"
  "ds_read_b128 a[120:123], v98 offset:49152\n"
  "v_mfma_f32_16x16x32_bf16 v[168:171], v[88:91], a[140:143], v[168:171]\n"
  "ds_read_b128 a[124:127], v99 offset:49152\n"
  "v_mfma_f32_16x16x32_bf16 v[172:175], v[92:95], a[140:143], v[172:175]\n"
  "v_mfma_f32_16x16x32_bf16 v[172:175], v[84:87], a[136:139], v[172:175]\n"
  "v_mfma_f32_16x16x32_bf16 v[172:175], v[76:79], a[132:135], v[172:175]\n"
  "v_mfma_f32_16x16x32_bf16 v[172:175], v[68:71], a[128:131], v[172:175]\n"
  "s_waitcnt lgkmcnt(7)\n"
  "v_mfma_f32_16x16x32_bf16 v[172:175], a[196:199], a[96:99], v[172:175]\n"
  "s_waitcnt lgkmcnt(6)\n"
  "ds_read_b128 a[128:131], v96 offset:53248\n"
  "v_mfma_f32_16x16x32_bf16 v[172:175], a[204:207], a[100:103], v[172:175]\n"
  "s_waitcnt lgkmcnt(6)\n"
  "ds_read_b128 a[132:135], v97 offset:53248\n"
  "v_mfma_f32_16x16x32_bf16 v[172:175], a[212:215], a[104:107], v[172:175]\n"
  "s_waitcnt lgkmcnt(6)\n"
  "ds_read_b128 a[136:139], v98 offset:53248\n"
  "v_mfma_f32_16x16x32_bf16 v[172:175], a[220:223], a[108:111], v[172:175]\n"
  "s_waitcnt vmcnt(0)\n"
  "v_cmp_ne_u32 vcc, s80, v217\n"
  "s_nop 0\n"
  "s_and_b32 s77, vcc_lo, 0xff\n"
  "s_cbranch_scc1 L_p1bc_%=\n"
  "L_p1bd_%=:\n"
  "v_mfma_f32_16x16x32_bf16 v[168:171], a[216:219], a[108:111], v[168:171]\n"
  "ds_read_b128 a[140:143], v99 offset:53248\n"
  "v_mfma_f32_16x16x32_bf16 v[168:171], a[208:211], a[104:107], v[168:171]\n"
  "v_mfma_f32_16x16x32_bf16 v[168:171], a[200:203], a[100:103], v[168:171]\n"
  "s_add_u32 m0, s68, 131072\n"
  "v_mfma_f32_16x16x32_bf16 v[168:171], a[192:195], a[96:99], v[168:171]\n"
  "s_waitcnt lgkmcnt(7)\n"
  "buffer_load_dwordx4 v223, s[36:39], s84 offen sc1 lds\n"
  "v_mfma_f32_16x16x32_bf16 v[168:171], a[0:3], a[112:115], v[168:171]\n"
  "s_waitcnt lgkmcnt(6)\n"
  "ds_read_b128 a[96:99], v96 offset:57344\n"
  "v_mfma_f32_16x16x32_bf16 v[168:171], a[8:11], a[116:119], v[168:171]\n"
  "s_waitcnt lgkmcnt(6)\n"
  "ds_read_b128 a[100:103], v97 offset:57344\n"
  "v_mfma_f32_16x16x32_bf16 v[168:171], a[16:19], a[120:123], v[168:171]\n"
  "s_waitcnt lgkmcnt(6)\n"
  "ds_read_b128 a[104:107], v98 offset:57344\n"
  "v_mfma_f32_16x16x32_bf16 v[168:171], a[24:27], a[124:127], v[168:171]\n"
  "buffer_load_dwordx4 v223, s[36:39], s84 offen offset:1024 sc1 lds\n"
  "v_mfma_f32_16x16x32_bf16 v[172:175], a[28:31], a[124:127], v[172:175]\n"
  "ds_read_b128 a[108:111], v99 offset:57344\n"
  "v_mfma_f32_16x16x32_bf16 v[172:175], a[20:23], a[120:123], v[172:175]\n"
  "v_mfma_f32_16x16x32_bf16 v[172:175], a[12:15], a[116:119], v[172:175]\n"
  "v_mfma_f32_16x16x32_bf16 v[172:175], a[4:7], a[112:115], v[172:175]\n"
  "s_waitcnt lgkmcnt(7)\n"
  "v_mfma_f32_16x16x32_bf16 v[172:175], a[36:39], a[128:131], v[172:175]\n"
  "s_waitcnt lgkmcnt(6)\n"
  "buffer_load_dwordx4 v223, s[36:39], s84 offen offset:2048 sc1 lds\n"
  "v_mfma_f32_16x16x32_bf16 v[172:175], a[44:47], a[132:135], v[172:175]\n"
  "s_waitcnt lgkmcnt(5)\n"
  "ds_read_b128 a[112:115], v96 offset:61440\n"
  "v_mfma_f32_16x16x32_bf16 v[172:175], a[52:55], a[136:139], v[172:175]\n"
  "s_waitcnt lgkmcnt(5)\n"
  "ds_read_b128 a[116:119], v97 offset:61440\n"
  "v_mfma_f32_16x16x32_bf16 v[172:175], a[60:63], a[140:143], v[172:175]\n"
  "ds_read_b128 a[120:123], v98 offset:61440\n"
  "v_mfma_f32_16x16x32_bf16 v[168:171], a[56:59], a[140:143], v[168:171]\n"
  "buffer_load_dwordx4 v223, s[36:39], s84 offen offset:3072 sc1 lds\n"
  "v_mfma_f32_16x16x32_bf16 v[168:171], a[48:51], a[136:139], v[168:171]\n"
  "s_add_u32 s84, s84, 4096\n"
  "ds_read_b128 a[124:127], v99 offset:61440\n"
  "v_mfma_f32_16x16x32_bf16 v[168:171], a[40:43], a[132:135], v[168:171]\n"
  "v_mfma_f32_16x16x32_bf16 v[168:171], a[32:35], a[128:131], v[168:171]\n"
  "s_waitcnt lgkmcnt(7)\n"
  "v_mfma_f32_16x16x32_bf16 v[168:171], a[160:163], a[96:99], v[168:171]\n"
  "s_waitcnt vmcnt(5)\n"
  "s_barrier\n"
  "s_waitcnt lgkmcnt(6)\n"
  "v_mfma_f32_16x16x32_bf16 v[168:171], a[168:171], a[100:103], v[168:171]\n"
  "s_add_u32 m0, s68, 135168\n"
  "s_waitcnt lgkmcnt(5)\n"
  "buffer_store_dword v222, v218, s[56:59], s6 offen offset:320 sc1\n"
  "v_mfma_f32_16x16x32_bf16 v[168:171], a[176:179], a[104:107], v[168:171]\n"
  "s_waitcnt lgkmcnt(4)\n"
  "buffer_load_dwordx4 v223, s[36:39], s84 offen sc1 lds\n"
  "v_mfma_f32_16x16x32_bf16 v[168:171], a[184:187], a[108:111], v[168:171]\n"
  "ds_read_b128 a[128:131], v100 offset:32768\n"
  "v_mfma_f32_16x16x32_bf16 v[172:175], a[188:191], a[108:111], v[172:175]\n"
  "ds_read_b128 a[132:135], v101 offset:32768\n"
  "v_mfma_f32_16x16x32_bf16 v[172:175], a[180:183], a[104:107], v[172:175]\n"
  "buffer_load_dwordx4 v223, s[36:39], s84 offen offset:1024 sc1 lds\n"
  "v_mfma_f32_16x16x32_bf16 v[172:175], a[172:175], a[100:103], v[172:175]\n"
  "ds_read_b128 a[136:139], v102 offset:32768\n"
  "v_mfma_f32_16x16x32_bf16 v[172:175], a[164:167], a[96:99], v[172:175]\n"
  "s_waitcnt lgkmcnt(6)\n"
  "ds_read_b128 a[140:143], v103 offset:32768\n"
  "v_mfma_f32_16x16x32_bf16 v[172:175], a[68:71], a[112:115], v[172:175]\n"
  "s_waitcnt lgkmcnt(6)\n"
  "ds_read_b128 a[96:99], v100 offset:36864\n"
  "v_mfma_f32_16x16x32_bf16 v[172:175], a[76:79], a[116:119], v[172:175]\n"
  "s_waitcnt lgkmcnt(6)\n"
  "ds_read_b128 a[100:103], v101 offset:36864\n"
  "v_mfma_f32_16x16x32_bf16 v[172:175], a[84:87], a[120:123], v[172:175]\n"
  "s_waitcnt lgkmcnt(6)\n"
  "buffer_load_dwordx4 v223, s[36:39], s84 offen offset:2048 sc1 lds\n"
  "v_mfma_f32_16x16x32_bf16 v[172:175], a[92:95], a[124:127], v[172:175]\n"
  "ds_read_b128 a[104:107], v102 offset:36864\n"
  "v_mfma_f32_16x16x32_bf16 v[168:171], a[88:91], a[124:127], v[168:171]\n"
  "ds_read_b128 a[108:111], v103 offset:36864\n"
  "v_mfma_f32_16x16x32_bf16 v[168:171], a[80:83], a[120:123], v[168:171]\n"
  "v_mfma_f32_16x16x32_bf16 v[168:171], a[72:75], a[116:119], v[168:171]\n"
  "buffer_load_dwordx4 v223, s[36:39], s84 offen offset:3072 sc1 lds\n"
  "v_mfma_f32_16x16x32_bf16 v[168:171], a[64:67], a[112:115], v[168:171]\n"
  "s_waitcnt lgkmcnt(7)\n"
  "v_mfma_f32_16x16x32_bf16 v[176:179], v[0:3], a[128:131], 0\n"
  "s_waitcnt lgkmcnt(6)\n"
  "ds_read_b128 a[112:115], v100 offset:40960\n"
  "v_mfma_f32_16x16x32_bf16 v[176:179], v[8:11], a[132:135], v[176:179]\n"
  "s_waitcnt lgkmcnt(6)\n"
  "ds_read_b128 a[116:119], v101 offset:40960\n"
  "v_mfma_f32_16x16x32_bf16 v[176:179], v[16:19], a[136:139], v[176:179]\n"
  "s_waitcnt lgkmcnt(6)\n"
  "ds_read_b128 a[120:123], v102 offset:40960\n"
  "v_mfma_f32_16x16x32_bf16 v[176:179], v[24:27], a[140:143], v[176:179]\n"
  "s_xor_b32 s84, s5, 5\n"
  "ds_read_b128 a[124:127], v103 offset:40960\n"
  "v_mfma_f32_16x16x32_bf16 v[180:183], v[28:31], a[140:143], 0\n"
  "s_or_b32 s84, s84, s70\n"
  "v_cvt_pk_bf16_f32 v118, v172, v173\n"
  "v_cvt_pk_bf16_f32 v119, v174, v175\n"
  "v_mfma_f32_16x16x32_bf16 v[180:183], v[20:23], a[136:139], v[180:183]\n"
  "s_lshl_b32 s84, s84, 17\n"
  "v_cvt_pk_bf16_f32 v116, v168, v169\n"
  "v_cvt_pk_bf16_f32 v117, v170, v171\n"
  "v_mfma_f32_16x16x32_bf16 v[180:183], v[12:15], a[132:135], v[180:183]\n"
  "s_add_u32 m0, s68, 65536\n"
  "buffer_store_dwordx4 v[184:187], v246, s[40:43], s85 offen sc1\n"
  "v_mfma_f32_16x16x32_bf16 v[180:183], v[4:7], a[128:131], v[180:183]\n"
  "s_xor_b32 s85, s5, 7\n"
  "s_lshl_b32 s85, s85, 14\n"
  "s_waitcnt lgkmcnt(7)\n"
  "buffer_load_dwordx4 v223, s[36:39], s84 offen sc1 lds\n"
  "v_mfma_f32_16x16x32_bf16 v[180:183], v[36:39], a[96:99], v[180:183]\n"
  "s_waitcnt lgkmcnt(6)\n"
  "ds_read_b128 a[128:131], v100 offset:45056\n"
  "v_mfma_f32_16x16x32_bf16 v[180:183], v[44:47], a[100:103], v[180:183]\n"
  "s_waitcnt lgkmcnt(6)\n"
  "ds_read_b128 a[132:135], v101 offset:45056\n"
  "v_mfma_f32_16x16x32_bf16 v[180:183], v[52:55], a[104:107], v[180:183]\n"
  "s_waitcnt lgkmcnt(6)\n"
  "ds_read_b128 a[136:139], v102 offset:45056\n"
  "v_mfma_f32_16x16x32_bf16 v[180:183], v[60:63], a[108:111], v[180:183]\n"
  "ds_read_b128 a[140:143], v103 offset:45056\n"
  "v_mfma_f32_16x16x32_bf16 v[176:179], v[56:59], a[108:111], v[176:179]\n"
  "buffer_load_dwordx4 v223, s[36:39], s84 offen offset:1024 sc1 lds\n"
  "v_mfma_f32_16x16x32_bf16 v[176:179], v[48:51], a[104:107], v[176:179]\n"
  "v_mfma_f32_16x16x32_bf16 v[176:179], v[40:43], a[100:103], v[176:179]\n"
  "v_mfma_f32_16x16x32_bf16 v[176:179], v[32:35], a[96:99], v[176:179]\n"
  "s_waitcnt lgkmcnt(7)\n"
  "v_mfma_f32_16x16x32_bf16 v[176:179], v[64:67], a[112:115], v[176:179]\n"
  "s_waitcnt lgkmcnt(6)\n"
  "ds_read_b128 a[96:99], v100 offset:49152\n"
  "v_mfma_f32_16x16x32_bf16 v[176:179], v[72:75], a[116:119], v[176:179]\n"
  "s_waitcnt lgkmcnt(6)\n"
  "ds_read_b128 a[100:103], v101 offset:49152\n"
  "v_mfma_f32_16x16x32_bf16 v[176:179], v[80:83], a[120:123], v[176:179]\n"
  "s_waitcnt lgkmcnt(6)\n"
  "ds_read_b128 a[104:107], v102 offset:49152\n"
  "v_mfma_f32_16x16x32_bf16 v[176:179], v[88:91], a[124:127], v[176:179]\n"
  "buffer_load_dwordx4 v223, s[36:39], s84 offen offset:2048 sc1 lds\n"
  "v_mfma_f32_16x16x32_bf16 v[180:183], v[92:95], a[124:127], v[180:183]\n"
  "ds_read_b128 a[108:111], v103 offset:49152\n"
  "v_mfma_f32_16x16x32_bf16 v[180:183], v[84:87], a[120:123], v[180:183]\n"
  "v_mfma_f32_16x16x32_bf16 v[180:183], v[76:79], a[116:119], v[180:183]\n"
  "v_mfma_f32_16x16x32_bf16 v[180:183], v[68:71], a[112:115], v[180:183]\n"
  "s_waitcnt lgkmcnt(7)\n"
  "v_mfma_f32_16x16x32_bf16 v[180:183], a[196:199], a[128:131], v[180:183]\n"
  "s_waitcnt lgkmcnt(6)\n"
  "ds_read_b128 a[112:115], v100 offset:53248\n"
  "v_mfma_f32_16x16x32_bf16 v[180:183], a[204:207], a[132:135], v[180:183]\n"
  "s_waitcnt lgkmcnt(6)\n"
  "ds_read_b128 a[116:119], v101 offset:53248\n"
  "v_mfma_f32_16x16x32_bf16 v[180:183], a[212:215], a[136:139], v[180:183]\n"
  "s_waitcnt lgkmcnt(6)\n"
  "buffer_load_dwordx4 v223, s[36:39], s84 offen offset:3072 sc1 lds\n"
  "v_mfma_f32_16x16x32_bf16 v[180:183], a[220:223], a[140:143], v[180:183]\n"
  "s_add_u32 s84, s84, 4096\n"
  "ds_read_b128 a[120:123], v102 offset:53248\n"
  "v_mfma_f32_16x16x32_bf16 v[176:179], a[216:219], a[140:143], v[176:179]\n"
  "ds_read_b128 a[124:127], v103 offset:53248\n"
  "v_mfma_f32_16x16x32_bf16 v[176:179], a[208:211], a[136:139], v[176:179]\n"
  "v_mfma_f32_16x16x32_bf16 v[176:179], a[200:203], a[132:135], v[176:179]\n"
  "buffer_store_dwordx4 v[188:191], v246, s[40:43], s85 offen sc1\n"
  "v_mfma_f32_16x16x32_bf16 v[176:179], a[192:195], a[128:131], v[176:179]\n"
  "s_xor_b32 s85, s5, 2\n"
  "s_lshl_b32 s85, s85, 14\n"
  "s_waitcnt lgkmcnt(7)\n"
  "v_mfma_f32_16x16x32_bf16 v[176:179], a[0:3], a[96:99], v[176:179]\n"
  "s_waitcnt lgkmcnt(6)\n"
  "ds_read_b128 a[128:131], v100 offset:57344\n"
  "v_mfma_f32_16x16x32_bf16 v[176:179], a[8:11], a[100:103], v[176:179]\n"
  "s_add_u32 m0, s68, 69632\n"
  "s_waitcnt lgkmcnt(6)\n"
  "ds_read_b128 a[132:135], v101 offset:57344\n"
  "v_mfma_f32_16x16x32_bf16 v[176:179], a[16:19], a[104:107], v[176:179]\n"
  "s_waitcnt lgkmcnt(6)\n"
  "buffer_load_dwordx4 v223, s[36:39], s84 offen sc1 lds\n"
  "v_mfma_f32_16x16x32_bf16 v[176:179], a[24:27], a[108:111], v[176:179]\n"
  "ds_read_b128 a[136:139], v102 offset:57344\n"
  "v_mfma_f32_16x16x32_bf16 v[180:183], a[28:31], a[108:111], v[180:183]\n"
  "ds_read_b128 a[140:143], v103 offset:57344\n"
  "v_mfma_f32_16x16x32_bf16 v[180:183], a[20:23], a[104:107], v[180:183]\n"
  "v_mfma_f32_16x16x32_bf16 v[180:183], a[12:15], a[100:103], v[180:183]\n"
  "v_mfma_f32_16x16x32_bf16 v[180:183], a[4:7], a[96:99], v[180:183]\n"
  "s_waitcnt lgkmcnt(7)\n"
  "v_mfma_f32_16x16x32_bf16 v[180:183], a[36:39], a[112:115], v[180:183]\n"
  "s_waitcnt lgkmcnt(6)\n"
  "ds_read_b128 a[96:99], v100 offset:61440\n"
  "v_mfma_f32_16x16x32_bf16 v[180:183], a[44:47], a[116:119], v[180:183]\n"
  "s_waitcnt lgkmcnt(6)\n"
  "buffer_load_dwordx4 v223, s[36:39], s84 offen offset:1024 sc1 lds\n"
  "v_mfma_f32_16x16x32_bf16 v[180:183], a[52:55], a[120:123], v[180:183]\n"
  "s_waitcnt lgkmcnt(5)\n"
  "ds_read_b128 a[100:103], v101 offset:61440\n"
  "v_mfma_f32_16x16x32_bf16 v[180:183], a[60:63], a[124:127], v[180:183]\n"
  "ds_read_b128 a[104:107], v102 offset:61440\n"
  "v_mfma_f32_16x16x32_bf16 v[176:179], a[56:59], a[124:127], v[176:179]\n"
  "ds_read_b128 a[108:111], v103 offset:61440\n"
  "v_mfma_f32_16x16x32_bf16 v[176:179], a[48:51], a[120:123], v[176:179]\n"
  "v_mfma_f32_16x16x32_bf16 v[176:179], a[40:43], a[116:119], v[176:179]\n"
  "v_mfma_f32_16x16x32_bf16 v[176:179], a[32:35], a[112:115], v[176:179]\n"
  "s_waitcnt lgkmcnt(7)\n"
  "v_mfma_f32_16x16x32_bf16 v[176:179], a[160:163], a[128:131], v[176:179]\n"
  "s_waitcnt vmcnt(8)\n"
  "s_barrier\n"
  "s_waitcnt lgkmcnt(6)\n"
  "v_mfma_f32_16x16x32_bf16 v[176:179], a[168:171], a[132:135], v[176:179]\n"
  "s_waitcnt lgkmcnt(5)\n"
  "buffer_load_dwordx4 v223, s[36:39], s84 offen offset:2048 sc1 lds\n"
  "v_mfma_f32_16x16x32_bf16 v[176:179], a[176:179], a[136:139], v[176:179]\n"
  "s_waitcnt lgkmcnt(4)\n"
  "ds_read_b128 a[112:115], v104 offset:0\n"
  "v_mfma_f32_16x16x32_bf16 v[176:179], a[184:187], a[140:143], v[176:179]\n"
  "ds_read_b128 a[116:119], v105 offset:0\n"
  "v_mfma_f32_16x16x32_bf16 v[180:183], a[188:191], a[140:143], v[180:183]\n"
  "ds_read_b128 a[120:123], v106 offset:0\n"
  "v_mfma_f32_16x16x32_bf16 v[180:183], a[180:183], a[136:139], v[180:183]\n"
  "ds_read_b128 a[124:127], v107 offset:0\n"
  "v_mfma_f32_16x16x32_bf16 v[180:183], a[172:175], a[132:135], v[180:183]\n"
  "buffer_store_dwordx4 v[112:115], v246, s[40:43], s85 offen sc1\n"
  "v_mfma_f32_16x16x32_bf16 v[180:183], a[164:167], a[128:131], v[180:183]\n"
  "s_xor_b32 s85, s5, 6\n"
  "s_lshl_b32 s85, s85, 14\n"
  "s_waitcnt lgkmcnt(7)\n"
  "v_mfma_f32_16x16x32_bf16 v[180:183], a[68:71], a[96:99], v[180:183]\n"
  "s_waitcnt lgkmcnt(6)\n"
  "buffer_load_dwordx4 v223, s[36:39], s84 offen offset:3072 sc1 lds\n"
  "v_mfma_f32_16x16x32_bf16 v[180:183], a[76:79], a[100:103], v[180:183]\n"
  "s_waitcnt lgkmcnt(5)\n"
  "ds_read_b128 a[128:131], v104 offset:4096\n"
  "v_mfma_f32_16x16x32_bf16 v[180:183], a[84:87], a[104:107], v[180:183]\n"
  "s_waitcnt lgkmcnt(5)\n"
  "ds_read_b128 a[132:135], v105 offset:4096\n"
  "v_mfma_f32_16x16x32_bf16 v[180:183], a[92:95], a[108:111], v[180:183]\n"
  "s_xor_b32 s84, s5, 0\n"
  "ds_read_b128 a[136:139], v106 offset:4096\n"
  "v_mfma_f32_16x16x32_bf16 v[176:179], a[88:91], a[108:111], v[176:179]\n"
  "s_or_b32 s84, s84, s70\n"
  "ds_read_b128 a[140:143], v107 offset:4096\n"
  "v_mfma_f32_16x16x32_bf16 v[176:179], a[80:83], a[104:107], v[176:179]\n"
  "s_lshl_b32 s84, s84, 17\n"
  "v_mfma_f32_16x16x32_bf16 v[176:179], a[72:75], a[100:103], v[176:179]\n"
  "v_mfma_f32_16x16x32_bf16 v[176:179], a[64:67], a[96:99], v[176:179]\n"
  "s_waitcnt lgkmcnt(7)\n"
  "v_mfma_f32_16x16x32_bf16 v[168:171], v[0:3], a[112:115], 0\n"
  "s_waitcnt lgkmcnt(6)\n"
  "ds_read_b128 a[96:99], v104 offset:8192\n"
  "v_mfma_f32_16x16x32_bf16 v[168:171], v[8:11], a[116:119], v[168:171]\n"
  "s_waitcnt lgkmcnt(6)\n"
  "buffer_load_dword v217, v216, s[56:59], 0 offen offset:320 sc1\n"
  "v_mfma_f32_16x16x32_bf16 v[168:171], v[16:19], a[120:123], v[168:171]\n"
  "s_waitcnt lgkmcnt(5)\n"
  "ds_read_b128 a[100:103], v105 offset:8192\n"
  "v_mfma_f32_16x16x32_bf16 v[168:171], v[24:27], a[124:127], v[168:171]\n"
  "ds_read_b128 a[104:107], v106 offset:8192\n"
  "v_mfma_f32_16x16x32_bf16 v[172:175], v[28:31], a[124:127], 0\n"
  "v_cvt_pk_bf16_f32 v230, v180, v181\n"
  "v_cvt_pk_bf16_f32 v231, v182, v183\n"
  "v_mfma_f32_16x16x32_bf16 v[172:175], v[20:23], a[120:123], v[172:175]\n"
  "v_cvt_pk_bf16_f32 v228, v176, v177\n"
  "v_cvt_pk_bf16_f32 v229, v178, v179\n"
  "v_mfma_f32_16x16x32_bf16 v[172:175], v[12:15], a[116:119], v[172:175]\n"
  "ds_read_b128 a[108:111], v107 offset:8192\n"
  "v_mfma_f32_16x16x32_bf16 v[172:175], v[4:7], a[112:115], v[172:175]\n"
  "s_waitcnt lgkmcnt(7)\n"
  "v_mfma_f32_16x16x32_bf16 v[172:175], v[36:39], a[128:131], v[172:175]\n"
  "s_waitcnt lgkmcnt(6)\n"
  "ds_read_b128 a[112:115], v104 offset:12288\n"
  "v_mfma_f32_16x16x32_bf16 v[172:175], v[44:47], a[132:135], v[172:175]\n"
  "s_waitcnt lgkmcnt(6)\n"
  "ds_read_b128 a[116:119], v105 offset:12288\n"
  "v_mfma_f32_16x16x32_bf16 v[172:175], v[52:55], a[136:139], v[172:175]\n"
  "s_waitcnt lgkmcnt(6)\n"
  "ds_read_b128 a[120:123], v106 offset:12288\n"
  "v_mfma_f32_16x16x32_bf16 v[172:175], v[60:63], a[140:143], v[172:175]\n"
  "ds_read_b128 a[124:127], v107 offset:12288\n"
  "v_mfma_f32_16x16x32_bf16 v[168:171], v[56:59], a[140:143], v[168:171]\n"
  "v_mfma_f32_16x16x32_bf16 v[168:171], v[48:51], a[136:139], v[168:171]\n"
  "v_mfma_f32_16x16x32_bf16 v[168:171], v[40:43], a[132:135], v[168:171]\n"
  "v_mfma_f32_16x16x32_bf16 v[168:171], v[32:35], a[128:131], v[168:171]\n"
  "s_waitcnt lgkmcnt(7)\n"
  "v_mfma_f32_16x16x32_bf16 v[168:171], v[64:67], a[96:99], v[168:171]\n"
  "s_waitcnt lgkmcnt(6)\n"
  "ds_read_b128 a[128:131], v104 offset:16384\n"
  "v_mfma_f32_16x16x32_bf16 v[168:171], v[72:75], a[100:103], v[168:171]\n"
  "s_waitcnt lgkmcnt(6)\n"
  "ds_read_b128 a[132:135], v105 offset:16384\n"
  "v_mfma_f32_16x16x32_bf16 v[168:171], v[80:83], a[104:107], v[168:171]\n"
  "s_waitcnt lgkmcnt(6)\n"
  "ds_read_b128 a[136:139], v106 offset:16384\n"
  "v_mfma_f32_16x16x32_bf16 v[168:171], v[88:91], a[108:111], v[168:171]\n"
  "ds_read_b128 a[140:143], v107 offset:16384\n"
  "v_mfma_f32_16x16x32_bf16 v[172:175], v[92:95], a[108:111], v[172:175]\n"
  "v_mfma_f32_16x16x32_bf16 v[172:175], v[84:87], a[104:107], v[172:175]\n"
  "v_mfma_f32_16x16x32_bf16 v[172:175], v[76:79], a[100:103], v[172:175]\n"
  "v_mfma_f32_16x16x32_bf16 v[172:175], v[68:71], a[96:99], v[172:175]\n"
  "s_waitcnt lgkmcnt(7)\n"
  "v_mfma_f32_16x16x32_bf16 v[172:175], a[196:199], a[112:115], v[172:175]\n"
  "s_waitcnt lgkmcnt(6)\n"
  "ds_read_b128 a[96:99], v104 offset:20480\n"
  "v_mfma_f32_16x16x32_bf16 v[172:175], a[204:207], a[116:119], v[172:175]\n"
  "s_waitcnt lgkmcnt(6)\n"
  "ds_read_b128 a[100:103], v105 offset:20480\n"
  "v_mfma_f32_16x16x32_bf16 v[172:175], a[212:215], a[120:123], v[172:175]\n"
  "s_waitcnt lgkmcnt(6)\n"
  "ds_read_b128 a[104:107], v106 offset:20480\n"
  "v_mfma_f32_16x16x32_bf16 v[172:175], a[220:223], a[124:127], v[172:175]\n"
  "ds_read_b128 a[108:111], v107 offset:20480\n"
  "v_mfma_f32_16x16x32_bf16 v[168:171], a[216:219], a[124:127], v[168:171]\n"
  "v_mfma_f32_16x16x32_bf16 v[168:171], a[208:211], a[120:123], v[168:171]\n"
  "s_waitcnt vmcnt(0)\n"
  "v_cmp_ne_u32 vcc, s80, v217\n"
  "s_nop 0\n"
  "s_and_b32 s77, vcc_lo, 0xff\n"
  "s_cbranch_scc1 L_p1cc_%=\n"
  "L_p1cd_%=:\n"
  "v_mfma_f32_16x16x32_bf16 v[168:171], a[200:203], a[116:119], v[168:171]\n"
  "s_add_u32 m0, s68, 0\n"
  "v_mfma_f32_16x16x32_bf16 v[168:171], a[192:195], a[112:115], v[168:171]\n"
  "s_waitcnt lgkmcnt(7)\n"
  "v_mfma_f32_16x16x32_bf16 v[168:171], a[0:3], a[128:131], v[168:171]\n"
  "s_waitcnt lgkmcnt(6)\n"
  "ds_read_b128 a[112:115], v104 offset:24576\n"
  "v_mfma_f32_16x16x32_bf16 v[168:171], a[8:11], a[132:135], v[168:171]\n"
  "s_waitcnt lgkmcnt(6)\n"
  "buffer_load_dwordx4 v223, s[36:39], s84 offen sc1 lds\n"
  "v_mfma_f32_16x16x32_bf16 v[168:171], a[16:19], a[136:139], v[168:171]\n"
  "s_waitcnt lgkmcnt(5)\n"
  "buffer_store_dwordx4 v[116:119], v246, s[40:43], s85 offen sc1\n"
  "v_mfma_f32_16x16x32_bf16 v[168:171], a[24:27], a[140:143], v[168:171]\n"
  "s_or_b32 s85, s72, 131072\n"
  "ds_read_b128 a[116:119], v105 offset:24576\n"
  "v_mfma_f32_16x16x32_bf16 v[172:175], a[28:31], a[140:143], v[172:175]\n"
  "ds_read_b128 a[120:123], v106 offset:24576\n"
  "v_mfma_f32_16x16x32_bf16 v[172:175], a[20:23], a[136:139], v[172:175]\n"
  "buffer_load_dwordx4 v223, s[36:39], s84 offen offset:1024 sc1 lds\n"
  "v_mfma_f32_16x16x32_bf16 v[172:175], a[12:15], a[132:135], v[172:175]\n"
  "ds_read_b128 a[124:127], v107 offset:24576\n"
  "v_mfma_f32_16x16x32_bf16 v[172:175], a[4:7], a[128:131], v[172:175]\n"
  "s_waitcnt lgkmcnt(7)\n"
  "v_mfma_f32_16x16x32_bf16 v[172:175], a[36:39], a[96:99], v[172:175]\n"
  "s_waitcnt lgkmcnt(6)\n"
  "ds_read_b128 a[128:131], v104 offset:28672\n"
  "v_mfma_f32_16x16x32_bf16 v[172:175], a[44:47], a[100:103], v[172:175]\n"
  "s_waitcnt lgkmcnt(6)\n"
  "ds_read_b128 a[132:135], v105 offset:28672\n"
  "v_mfma_f32_16x16x32_bf16 v[172:175], a[52:55], a[104:107], v[172:175]\n"
  "s_waitcnt lgkmcnt(6)\n"
  "buffer_load_dwordx4 v223, s[36:39], s84 offen offset:2048 sc1 lds\n"
  "v_mfma_f32_16x16x32_bf16 v[172:175], a[60:63], a[108:111], v[172:175]\n"
  "ds_read_b128 a[136:139], v106 offset:28672\n"
  "v_mfma_f32_16x16x32_bf16 v[168:171], a[56:59], a[108:111], v[168:171]\n"
  "ds_read_b128 a[140:143], v107 offset:28672\n"
  "v_mfma_f32_16x16x32_bf16 v[168:171], a[48:51], a[104:107], v[168:171]\n"
  "v_mfma_f32_16x16x32_bf16 v[168:171], a[40:43], a[100:103], v[168:171]\n"
  "buffer_load_dwordx4 v223, s[36:39], s84 offen offset:3072 sc1 lds\n"
  "v_mfma_f32_16x16x32_bf16 v[168:171], a[32:35], a[96:99], v[168:171]\n"
  "s_waitcnt lgkmcnt(7)\n"
  "v_mfma_f32_16x16x32_bf16 v[168:171], a[160:163], a[112:115], v[168:171]\n"
  "s_waitcnt vmcnt(6)\n"
  "s_barrier\n"
  "s_waitcnt lgkmcnt(6)\n"
  "v_mfma_f32_16x16x32_bf16 v[168:171], a[168:171], a[116:119], v[168:171]\n"
  "s_waitcnt lgkmcnt(5)\n"
  "ds_read_b128 a[96:99], v100 offset:0\n"
  "v_mfma_f32_16x16x32_bf16 v[168:171], a[176:179], a[120:123], v[168:171]\n"
  "s_add_u32 m0, s68, 4096\n"
  "s_waitcnt lgkmcnt(5)\n"
  "ds_read_b128 a[100:103], v101 offset:0\n"
  "v_mfma_f32_16x16x32_bf16 v[168:171], a[184:187], a[124:127], v[168:171]\n"
  "s_add_u32 s84, s84, 4096\n"
  "buffer_load_dwordx4 v223, s[36:39], s84 offen sc1 lds\n"
  "v_mfma_f32_16x16x32_bf16 v[172:175], a[188:191], a[124:127], v[172:175]\n"
  "ds_read_b128 a[104:107], v102 offset:0\n"
  "v_mfma_f32_16x16x32_bf16 v[172:175], a[180:183], a[120:123], v[172:175]\n"
  "ds_read_b128 a[108:111], v103 offset:0\n"
  "v_mfma_f32_16x16x32_bf16 v[172:175], a[172:175], a[116:119], v[172:175]\n"
  "v_mfma_f32_16x16x32_bf16 v[172:175], a[164:167], a[112:115], v[172:175]\n"
  "s_waitcnt lgkmcnt(7)\n"
  "buffer_load_dwordx4 v223, s[36:39], s84 offen offset:1024 sc1 lds\n"
  "v_mfma_f32_16x16x32_bf16 v[172:175], a[68:71], a[128:131], v[172:175]\n"
  "s_waitcnt lgkmcnt(6)\n"
  "ds_read_b128 a[112:115], v100 offset:4096\n"
  "v_mfma_f32_16x16x32_bf16 v[172:175], a[76:79], a[132:135], v[172:175]\n"
  "s_waitcnt lgkmcnt(6)\n"
  "ds_read_b128 a[116:119], v101 offset:4096\n"
  "v_mfma_f32_16x16x32_bf16 v[172:175], a[84:87], a[136:139], v[172:175]\n"
  "s_waitcnt lgkmcnt(6)\n"
  "buffer_store_dwordx4 v[228:231], v246, s[40:43], s85 offen sc1\n"
  "v_mfma_f32_16x16x32_bf16 v[172:175], a[92:95], a[140:143], v[172:175]\n"
  "s_xor_b32 s85, s5, 1\n"
  "ds_read_b128 a[120:123], v102 offset:4096\n"
  "v_mfma_f32_16x16x32_bf16 v[168:171], a[88:91], a[140:143], v[168:171]\n"
  "s_lshl_b32 s85, s85, 14\n"
  "buffer_load_dwordx4 v223, s[36:39], s84 offen offset:2048 sc1 lds\n"
  "v_mfma_f32_16x16x32_bf16 v[168:171], a[80:83], a[136:139], v[168:171]\n"
  "ds_read_b128 a[124:127], v103 offset:4096\n"
  "v_mfma_f32_16x16x32_bf16 v[168:171], a[72:75], a[132:135], v[168:171]\n"
  "v_mfma_f32_16x16x32_bf16 v[168:171], a[64:67], a[128:131], v[168:171]\n"
  "s_waitcnt lgkmcnt(7)\n"
  "v_mfma_f32_16x16x32_bf16 v[160:163], v[0:3], a[96:99], 0\n"
  "s_waitcnt lgkmcnt(6)\n"
  "buffer_load_dwordx4 v223, s[36:39], s84 offen offset:3072 sc1 lds\n"
  "v_mfma_f32_16x16x32_bf16 v[160:163], v[8:11], a[100:103], v[160:163]\n"
  "s_waitcnt lgkmcnt(5)\n"
  "ds_read_b128 a[128:131], v100 offset:8192\n"
  "v_mfma_f32_16x16x32_bf16 v[160:163], v[16:19], a[104:107], v[160:163]\n"
  "s_waitcnt lgkmcnt(5)\n"
  "ds_read_b128 a[132:135], v101 offset:8192\n"
  "v_mfma_f32_16x16x32_bf16 v[160:163], v[24:27], a[108:111], v[160:163]\n"
  "s_xor_b32 s84, s5, 4\n"
  "ds_read_b128 a[136:139], v102 offset:8192\n"
  "v_mfma_f32_16x16x32_bf16 v[164:167], v[28:31], a[108:111], 0\n"
  "s_or_b32 s84, s84, s70\n"
  "v_cvt_pk_bf16_f32 v234, v172, v173\n"
  "v_cvt_pk_bf16_f32 v235, v174, v175\n"
  "v_mfma_f32_16x16x32_bf16 v[164:167], v[20:23], a[104:107], v[164:167]\n"
  "s_lshl_b32 s84, s84, 17\n"
  "s_add_u32 m0, s68, 32768\n"
  "v_cvt_pk_bf16_f32 v232, v168, v169\n"
  "v_cvt_pk_bf16_f32 v233, v170, v171\n"
  "v_mfma_f32_16x16x32_bf16 v[164:167], v[12:15], a[100:103], v[164:167]\n"
  "buffer_load_dwordx4 v223, s[36:39], s84 offen sc1 lds\n"
  "v_mfma_f32_16x16x32_bf16 v[164:167], v[4:7], a[96:99], v[164:167]\n"
  "s_waitcnt lgkmcnt(6)\n"
  "ds_read_b128 a[140:143], v103 offset:8192\n"
  "v_mfma_f32_16x16x32_bf16 v[164:167], v[36:39], a[112:115], v[164:167]\n"
  "s_waitcnt lgkmcnt(6)\n"
  "ds_read_b128 a[96:99], v100 offset:12288\n"
  "v_mfma_f32_16x16x32_bf16 v[164:167], v[44:47], a[116:119], v[164:167]\n"
  "s_waitcnt lgkmcnt(6)\n"
  "ds_read_b128 a[100:103], v101 offset:12288\n"
  "v_mfma_f32_16x16x32_bf16 v[164:167], v[52:55], a[120:123], v[164:167]\n"
  "s_waitcnt lgkmcnt(6)\n"
  "ds_read_b128 a[104:107], v102 offset:12288\n"
  "v_mfma_f32_16x16x32_bf16 v[164:167], v[60:63], a[124:127], v[164:167]\n"
  "ds_read_b128 a[108:111], v103 offset:12288\n"
  "v_mfma_f32_16x16x32_bf16 v[160:163], v[56:59], a[124:127], v[160:163]\n"
  "buffer_load_dwordx4 v223, s[36:39], s84 offen offset:1024 sc1 lds\n"
  "v_mfma_f32_16x16x32_bf16 v[160:163], v[48:51], a[120:123], v[160:163]\n"
  "v_mfma_f32_16x16x32_bf16 v[160:163], v[40:43], a[116:119], v[160:163]\n"
  "v_mfma_f32_16x16x32_bf16 v[160:163], v[32:35], a[112:115], v[160:163]\n"
  "s_waitcnt lgkmcnt(7)\n"
  "v_mfma_f32_16x16x32_bf16 v[160:163], v[64:67], a[128:131], v[160:163]\n"
  "s_waitcnt lgkmcnt(6)\n"
  "ds_read_b128 a[112:115], v100 offset:16384\n"
  "v_mfma_f32_16x16x32_bf16 v[160:163], v[72:75], a[132:135], v[160:163]\n"
  "s_waitcnt lgkmcnt(6)\n"
  "ds_read_b128 a[116:119], v101 offset:16384\n"
  "v_mfma_f32_16x16x32_bf16 v[160:163], v[80:83], a[136:139], v[160:163]\n"
  "s_waitcnt lgkmcnt(6)\n"
  "buffer_store_dwordx4 v[232:235], v246, s[40:43], s85 offen sc1\n"
  "v_mfma_f32_16x16x32_bf16 v[160:163], v[88:91], a[140:143], v[160:163]\n"
  "s_xor_b32 s85, s5, 5\n"
  "s_lshl_b32 s85, s85, 14\n"
  "buffer_load_dwordx4 v223, s[36:39], s84 offen offset:2048 sc1 lds\n"
  "v_mfma_f32_16x16x32_bf16 v[164:167], v[92:95], a[140:143], v[164:167]\n"
  "ds_read_b128 a[120:123], v102 offset:16384\n"
  "v_mfma_f32_16x16x32_bf16 v[164:167], v[84:87], a[136:139], v[164:167]\n"
  "ds_read_b128 a[124:127], v103 offset:16384\n"
  "v_mfma_f32_16x16x32_bf16 v[164:167], v[76:79], a[132:135], v[164:167]\n"
  "v_mfma_f32_16x16x32_bf16 v[164:167], v[68:71], a[128:131], v[164:167]\n"
  "s_waitcnt lgkmcnt(7)\n"
  "v_mfma_f32_16x16x32_bf16 v[164:167], a[196:199], a[96:99], v[164:167]\n"
  "s_waitcnt lgkmcnt(6)\n"
  "ds_read_b128 a[128:131], v100 offset:20480\n"
  "v_mfma_f32_16x16x32_bf16 v[164:167], a[204:207], a[100:103], v[164:167]\n"
  "s_waitcnt lgkmcnt(6)\n"
  "ds_read_b128 a[132:135], v101 offset:20480\n"
  "v_mfma_f32_16x16x32_bf16 v[164:167], a[212:215], a[104:107], v[164:167]\n"
  "s_waitcnt lgkmcnt(6)\n"
  "buffer_load_dwordx4 v223, s[36:39], s84 offen offset:3072 sc1 lds\n"
  "v_mfma_f32_16x16x32_bf16 v[164:167], a[220:223], a[108:111], v[164:167]\n"
  "s_add_u32 s84, s84, 4096\n"
  "ds_read_b128 a[136:139], v102 offset:20480\n"
  "v_mfma_f32_16x16x32_bf16 v[160:163], a[216:219], a[108:111], v[160:163]\n"
  "ds_read_b128 a[140:143], v103 offset:20480\n"
  "v_mfma_f32_16x16x32_bf16 v[160:163], a[208:211], a[104:107], v[160:163]\n"
  "v_mfma_f32_16x16x32_bf16 v[160:163], a[200:203], a[100:103], v[160:163]\n"
  "s_add_u32 m0, s68, 36864\n"
  "v_mfma_f32_16x16x32_bf16 v[160:163], a[192:195], a[96:99], v[160:163]\n"
  "s_waitcnt lgkmcnt(7)\n"
  "v_mfma_f32_16x16x32_bf16 v[160:163], a[0:3], a[112:115], v[160:163]\n"
  "s_waitcnt lgkmcnt(6)\n"
  "buffer_load_dwordx4 v223, s[36:39], s84 offen sc1 lds\n"
  "v_mfma_f32_16x16x32_bf16 v[160:163], a[8:11], a[116:119], v[160:163]\n"
  "s_waitcnt lgkmcnt(5)\n"
  "ds_read_b128 a[96:99], v100 offset:24576\n"
  "v_mfma_f32_16x16x32_bf16 v[160:163], a[16:19], a[120:123], v[160:163]\n"
  "s_waitcnt lgkmcnt(5)\n"
  "ds_read_b128 a[100:103], v101 offset:24576\n"
  "v_mfma_f32_16x16x32_bf16 v[160:163], a[24:27], a[124:127], v[160:163]\n"
  "ds_read_b128 a[104:107], v102 offset:24576\n"
  "v_mfma_f32_16x16x32_bf16 v[164:167], a[28:31], a[124:127], v[164:167]\n"
  "ds_read_b128 a[108:111], v103 offset:24576\n"
  "v_mfma_f32_16x16x32_bf16 v[164:167], a[20:23], a[120:123], v[164:167]\n"
  "v_mfma_f32_16x16x32_bf16 v[164:167], a[12:15], a[116:119], v[164:167]\n"
  "v_mfma_f32_16x16x32_bf16 v[164:167], a[4:7], a[112:115], v[164:167]\n"
  "s_waitcnt lgkmcnt(7)\n"
  "buffer_load_dwordx4 v223, s[36:39], s84 offen offset:1024 sc1 lds\n"
  "v_mfma_f32_16x16x32_bf16 v[164:167], a[36:39], a[128:131], v[164:167]\n"
  "s_waitcnt lgkmcnt(6)\n"
  "ds_read_b128 a[112:115], v100 offset:28672\n"
  "v_mfma_f32_16x16x32_bf16 v[164:167], a[44:47], a[132:135], v[164:167]\n"
  "s_waitcnt lgkmcnt(6)\n"
  "ds_read_b128 a[116:119], v101 offset:28672\n"
  "v_mfma_f32_16x16x32_bf16 v[164:167], a[52:55], a[136:139], v[164:167]\n"
  "s_waitcnt lgkmcnt(6)\n"
  "ds_read_b128 a[120:123], v102 offset:28672\n"
  "v_mfma_f32_16x16x32_bf16 v[164:167], a[60:63], a[140:143], v[164:167]\n"
  "ds_read_b128 a[124:127], v103 offset:28672\n"
  "v_mfma_f32_16x16x32_bf16 v[160:163], a[56:59], a[140:143], v[160:163]\n"
  "v_mfma_f32_16x16x32_bf16 v[160:163], a[48:51], a[136:139], v[160:163]\n"
  "v_mfma_f32_16x16x32_bf16 v[160:163], a[40:43], a[132:135], v[160:163]\n"
  "buffer_load_dwordx4 v223, s[36:39], s84 offen offset:2048 sc1 lds\n"
  "v_mfma_f32_16x16x32_bf16 v[160:163], a[32:35], a[128:131], v[160:163]\n"
  "s_waitcnt lgkmcnt(7)\n"
  "v_mfma_f32_16x16x32_bf16 v[160:163], a[160:163], a[96:99], v[160:163]\n"
  "s_waitcnt vmcnt(8)\n"
  "s_barrier\n"
  "s_waitcnt lgkmcnt(6)\n"
  "v_mfma_f32_16x16x32_bf16 v[160:163], a[168:171], a[100:103], v[160:163]\n"
  "s_waitcnt lgkmcnt(5)\n"
  "ds_read_b128 a[128:131], v96 offset:0\n"
  "v_mfma_f32_16x16x32_bf16 v[160:163], a[176:179], a[104:107], v[160:163]\n"
  "s_waitcnt lgkmcnt(5)\n"
  "ds_read_b128 a[132:135], v97 offset:0\n"
  "v_mfma_f32_16x16x32_bf16 v[160:163], a[184:187], a[108:111], v[160:163]\n"
  "ds_read_b128 a[136:139], v98 offset:0\n"
  "v_mfma_f32_16x16x32_bf16 v[164:167], a[188:191], a[108:111], v[164:167]\n"
  "buffer_load_dwordx4 v223, s[36:39], s84 offen offset:3072 sc1 lds\n"
  "v_mfma_f32_16x16x32_bf16 v[164:167], a[180:183], a[104:107], v[164:167]\n"
  "ds_read_b128 a[140:143], v99 offset:0\n"
  "v_mfma_f32_16x16x32_bf16 v[164:167], a[172:175], a[100:103], v[164:167]\n"
  "v_mfma_f32_16x16x32_bf16 v[164:167], a[164:167], a[96:99], v[164:167]\n"
  "s_waitcnt lgkmcnt(7)\n"
  "v_mfma_f32_16x16x32_bf16 v[164:167], a[68:71], a[112:115], v[164:167]\n"
  "s_waitcnt lgkmcnt(6)\n"
  "ds_read_b128 a[96:99], v96 offset:4096\n"
  "v_mfma_f32_16x16x32_bf16 v[164:167], a[76:79], a[116:119], v[164:167]\n"
  "s_waitcnt lgkmcnt(6)\n"
  "ds_read_b128 a[100:103], v97 offset:4096\n"
  "v_mfma_f32_16x16x32_bf16 v[164:167], a[84:87], a[120:123], v[164:167]\n"
  "s_waitcnt lgkmcnt(6)\n"
  "ds_read_b128 a[104:107], v98 offset:4096\n"
  "v_mfma_f32_16x16x32_bf16 v[164:167], a[92:95], a[124:127], v[164:167]\n"
  "ds_read_b128 a[108:111], v99 offset:4096\n"
  "v_mfma_f32_16x16x32_bf16 v[160:163], a[88:91], a[124:127], v[160:163]\n"
  "v_mfma_f32_16x16x32_bf16 v[160:163], a[80:83], a[120:123], v[160:163]\n"
  "v_mfma_f32_16x16x32_bf16 v[160:163], a[72:75], a[116:119], v[160:163]\n"
  "v_mfma_f32_16x16x32_bf16 v[160:163], a[64:67], a[112:115], v[160:163]\n"
  "s_waitcnt lgkmcnt(7)\n"
  "v_mfma_f32_16x16x32_bf16 v[168:171], v[0:3], a[128:131], 0\n"
  "s_waitcnt lgkmcnt(6)\n"
  "ds_read_b128 a[112:115], v96 offset:8192\n"
  "v_mfma_f32_16x16x32_bf16 v[168:171], v[8:11], a[132:135], v[168:171]\n"
  "s_waitcnt lgkmcnt(6)\n"
  "ds_read_b128 a[116:119], v97 offset:8192\n"
  "v_mfma_f32_16x16x32_bf16 v[168:171], v[16:19], a[136:139], v[168:171]\n"
  "s_waitcnt lgkmcnt(6)\n"
  "ds_read_b128 a[120:123], v98 offset:8192\n"
  "v_mfma_f32_16x16x32_bf16 v[168:171], v[24:27], a[140:143], v[168:171]\n"
  "ds_read_b128 a[124:127], v99 offset:8192\n"
  "v_mfma_f32_16x16x32_bf16 v[172:175], v[28:31], a[140:143], 0\n"
  "v_cvt_pk_bf16_f32 v238, v164, v165\n"
  "v_cvt_pk_bf16_f32 v239, v166, v167\n"
  "v_mfma_f32_16x16x32_bf16 v[172:175], v[20:23], a[136:139], v[172:175]\n"
  "v_cvt_pk_bf16_f32 v236, v160, v161\n"
  "v_cvt_pk_bf16_f32 v237, v162, v163\n"
  "v_mfma_f32_16x16x32_bf16 v[172:175], v[12:15], a[132:135], v[172:175]\n"
  "buffer_store_dwordx4 v[236:239], v246, s[40:43], s85 offen sc1\n"
  "v_mfma_f32_16x16x32_bf16 v[172:175], v[4:7], a[128:131], v[172:175]\n"
  "s_waitcnt lgkmcnt(7)\n"
  "v_mfma_f32_16x16x32_bf16 v[172:175], v[36:39], a[96:99], v[172:175]\n"
  "s_waitcnt lgkmcnt(6)\n"
  "ds_read_b128 a[128:131], v96 offset:12288\n"
  "v_mfma_f32_16x16x32_bf16 v[172:175], v[44:47], a[100:103], v[172:175]\n"
  "s_waitcnt lgkmcnt(6)\n"
  "ds_read_b128 a[132:135], v97 offset:12288\n"
  "v_mfma_f32_16x16x32_bf16 v[172:175], v[52:55], a[104:107], v[172:175]\n"
  "s_waitcnt lgkmcnt(6)\n"
  "ds_read_b128 a[136:139], v98 offset:12288\n"
  "v_mfma_f32_16x16x32_bf16 v[172:175], v[60:63], a[108:111], v[172:175]\n"
  "ds_read_b128 a[140:143], v99 offset:12288\n"
  "v_mfma_f32_16x16x32_bf16 v[168:171], v[56:59], a[108:111], v[168:171]\n"
  "v_mfma_f32_16x16x32_bf16 v[168:171], v[48:51], a[104:107], v[168:171]\n"
  "v_mfma_f32_16x16x32_bf16 v[168:171], v[40:43], a[100:103], v[168:171]\n"
  "v_mfma_f32_16x16x32_bf16 v[168:171], v[32:35], a[96:99], v[168:171]\n"
  "s_waitcnt lgkmcnt(7)\n"
  "v_mfma_f32_16x16x32_bf16 v[168:171], v[64:67], a[112:115], v[168:171]\n"
  "s_waitcnt lgkmcnt(6)\n"
  "ds_read_b128 a[96:99], v96 offset:16384\n"
  "v_mfma_f32_16x16x32_bf16 v[168:171], v[72:75], a[116:119], v[168:171]\n"
  "s_waitcnt lgkmcnt(6)\n"
  "ds_read_b128 a[100:103], v97 offset:16384\n"
  "v_mfma_f32_16x16x32_bf16 v[168:171], v[80:83], a[120:123], v[168:171]\n"
  "s_waitcnt lgkmcnt(6)\n"
  "ds_read_b128 a[104:107], v98 offset:16384\n"
  "v_mfma_f32_16x16x32_bf16 v[168:171], v[88:91], a[124:127], v[168:171]\n"
  "ds_read_b128 a[108:111], v99 offset:16384\n"
  "v_mfma_f32_16x16x32_bf16 v[172:175], v[92:95], a[124:127], v[172:175]\n"
  "v_mfma_f32_16x16x32_bf16 v[172:175], v[84:87], a[120:123], v[172:175]\n"
  "v_mfma_f32_16x16x32_bf16 v[172:175], v[76:79], a[116:119], v[172:175]\n"
  "v_mfma_f32_16x16x32_bf16 v[172:175], v[68:71], a[112:115], v[172:175]\n"
  "s_waitcnt lgkmcnt(7)\n"
  "v_mfma_f32_16x16x32_bf16 v[172:175], a[196:199], a[128:131], v[172:175]\n"
  "s_waitcnt lgkmcnt(6)\n"
  "ds_read_b128 a[112:115], v96 offset:20480\n"
  "v_mfma_f32_16x16x32_bf16 v[172:175], a[204:207], a[132:135], v[172:175]\n"
  "s_waitcnt lgkmcnt(6)\n"
  "ds_read_b128 a[116:119], v97 offset:20480\n"
  "v_mfma_f32_16x16x32_bf16 v[172:175], a[212:215], a[136:139], v[172:175]\n"
  "s_waitcnt lgkmcnt(6)\n"
  "ds_read_b128 a[120:123], v98 offset:20480\n"
  "v_mfma_f32_16x16x32_bf16 v[172:175], a[220:223], a[140:143], v[172:175]\n"
  "ds_read_b128 a[124:127], v99 offset:20480\n"
  "v_mfma_f32_16x16x32_bf16 v[168:171], a[216:219], a[140:143], v[168:171]\n"
  "v_mfma_f32_16x16x32_bf16 v[168:171], a[208:211], a[136:139], v[168:171]\n"
  "v_mfma_f32_16x16x32_bf16 v[168:171], a[200:203], a[132:135], v[168:171]\n"
  "v_mfma_f32_16x16x32_bf16 v[168:171], a[192:195], a[128:131], v[168:171]\n"
  "s_waitcnt lgkmcnt(7)\n"
  "v_mfma_f32_16x16x32_bf16 v[168:171], a[0:3], a[96:99], v[168:171]\n"
  "s_waitcnt lgkmcnt(6)\n"
  "ds_read_b128 a[128:131], v96 offset:24576\n"
  "v_mfma_f32_16x16x32_bf16 v[168:171], a[8:11], a[100:103], v[168:171]\n"
  "s_waitcnt lgkmcnt(6)\n"
  "ds_read_b128 a[132:135], v97 offset:24576\n"
  "v_mfma_f32_16x16x32_bf16 v[168:171], a[16:19], a[104:107], v[168:171]\n"
  "s_waitcnt lgkmcnt(6)\n"
  "ds_read_b128 a[136:139], v98 offset:24576\n"
  "v_mfma_f32_16x16x32_bf16 v[168:171], a[24:27], a[108:111], v[168:171]\n"
  "ds_read_b128 a[140:143], v99 offset:24576\n"
  "v_mfma_f32_16x16x32_bf16 v[172:175], a[28:31], a[108:111], v[172:175]\n"
  "v_mfma_f32_16x16x32_bf16 v[172:175], a[20:23], a[104:107], v[172:175]\n"
  "v_mfma_f32_16x16x32_bf16 v[172:175], a[12:15], a[100:103], v[172:175]\n"
  "v_mfma_f32_16x16x32_bf16 v[172:175], a[4:7], a[96:99], v[172:175]\n"
  "s_waitcnt lgkmcnt(7)\n"
  "v_mfma_f32_16x16x32_bf16 v[172:175], a[36:39], a[112:115], v[172:175]\n"
  "s_waitcnt lgkmcnt(6)\n"
  "ds_read_b128 a[96:99], v96 offset:28672\n"
  "v_mfma_f32_16x16x32_bf16 v[172:175], a[44:47], a[116:119], v[172:175]\n"
  "s_waitcnt lgkmcnt(6)\n"
  "ds_read_b128 a[100:103], v97 offset:28672\n"
  "v_mfma_f32_16x16x32_bf16 v[172:175], a[52:55], a[120:123], v[172:175]\n"
  "s_waitcnt lgkmcnt(6)\n"
  "ds_read_b128 a[104:107], v98 offset:28672\n"
  "v_mfma_f32_16x16x32_bf16 v[172:175], a[60:63], a[124:127], v[172:175]\n"
  "s_waitcnt vmcnt(0)\n"
  "buffer_store_dword v222, v225, s[56:59], s73 offen offset:512 sc1\n"
  "v_mfma_f32_16x16x32_bf16 v[168:171], a[56:59], a[124:127], v[168:171]\n"
  "ds_read_b128 a[108:111], v99 offset:28672\n"
  "v_mfma_f32_16x16x32_bf16 v[168:171], a[48:51], a[120:123], v[168:171]\n"
  "v_mfma_f32_16x16x32_bf16 v[168:171], a[40:43], a[116:119], v[168:171]\n"
  "v_mfma_f32_16x16x32_bf16 v[168:171], a[32:35], a[112:115], v[168:171]\n"
  "s_waitcnt lgkmcnt(7)\n"
  "v_mfma_f32_16x16x32_bf16 v[168:171], a[160:163], a[128:131], v[168:171]\n"
  "s_waitcnt vmcnt(2)\n"
  "s_barrier\n"
  "s_waitcnt lgkmcnt(6)\n"
  "v_mfma_f32_16x16x32_bf16 v[168:171], a[168:171], a[132:135], v[168:171]\n"
  "s_waitcnt lgkmcnt(5)\n"
  "ds_read_b128 a[112:115], v96 offset:32768\n"
  "v_mfma_f32_16x16x32_bf16 v[168:171], a[176:179], a[136:139], v[168:171]\n"
  "s_waitcnt lgkmcnt(5)\n"
  "ds_read_b128 a[116:119], v97 offset:32768\n"
  "v_mfma_f32_16x16x32_bf16 v[168:171], a[184:187], a[140:143], v[168:171]\n"
  "ds_read_b128 a[120:123], v98 offset:32768\n"
  "v_mfma_f32_16x16x32_bf16 v[172:175], a[188:191], a[140:143], v[172:175]\n"
  "ds_read_b128 a[124:127], v99 offset:32768\n"
  "v_mfma_f32_16x16x32_bf16 v[172:175], a[180:183], a[136:139], v[172:175]\n"
  "v_mfma_f32_16x16x32_bf16 v[172:175], a[172:175], a[132:135], v[172:175]\n"
  "v_mfma_f32_16x16x32_bf16 v[172:175], a[164:167], a[128:131], v[172:175]\n"
  "s_waitcnt lgkmcnt(7)\n"
  "v_mfma_f32_16x16x32_bf16 v[172:175], a[68:71], a[96:99], v[172:175]\n"
  "s_waitcnt lgkmcnt(6)\n"
  "ds_read_b128 a[128:131], v96 offset:36864\n"
  "v_mfma_f32_16x16x32_bf16 v[172:175], a[76:79], a[100:103], v[172:175]\n"
  "s_waitcnt lgkmcnt(6)\n"
  "ds_read_b128 a[132:135], v97 offset:36864\n"
  "v_mfma_f32_16x16x32_bf16 v[172:175], a[84:87], a[104:107], v[172:175]\n"
  "s_waitcnt lgkmcnt(6)\n"
  "ds_read_b128 a[136:139], v98 offset:36864\n"
  "v_mfma_f32_16x16x32_bf16 v[172:175], a[92:95], a[108:111], v[172:175]\n"
  "ds_read_b128 a[140:143], v99 offset:36864\n"
  "v_mfma_f32_16x16x32_bf16 v[168:171], a[88:91], a[108:111], v[168:171]\n"
  "v_mfma_f32_16x16x32_bf16 v[168:171], a[80:83], a[104:107], v[168:171]\n"
  "v_mfma_f32_16x16x32_bf16 v[168:171], a[72:75], a[100:103], v[168:171]\n"
  "v_mfma_f32_16x16x32_bf16 v[168:171], a[64:67], a[96:99], v[168:171]\n"
  "s_waitcnt lgkmcnt(7)\n"
  "v_mfma_f32_16x16x32_bf16 v[160:163], v[0:3], a[112:115], 0\n"
  "s_waitcnt lgkmcnt(6)\n"
  "ds_read_b128 a[96:99], v96 offset:40960\n"
  "v_mfma_f32_16x16x32_bf16 v[160:163], v[8:11], a[116:119], v[160:163]\n"
  "s_waitcnt lgkmcnt(6)\n"
  "ds_read_b128 a[100:103], v97 offset:40960\n"
  "v_mfma_f32_16x16x32_bf16 v[160:163], v[16:19], a[120:123], v[160:163]\n"
  "s_waitcnt lgkmcnt(6)\n"
  "ds_read_b128 a[104:107], v98 offset:40960\n"
  "v_mfma_f32_16x16x32_bf16 v[160:163], v[24:27], a[124:127], v[160:163]\n"
  "buffer_load_dword v217, v216, s[56:59], s73 offen offset:512 sc1\n"
  "v_mfma_f32_16x16x32_bf16 v[164:167], v[28:31], a[124:127], 0\n"
  "ds_read_b128 a[108:111], v99 offset:40960\n"
  "v_mfma_f32_16x16x32_bf16 v[164:167], v[20:23], a[120:123], v[164:167]\n"
  "v_mfma_f32_16x16x32_bf16 v[164:167], v[12:15], a[116:119], v[164:167]\n"
  "v_mfma_f32_16x16x32_bf16 v[164:167], v[4:7], a[112:115], v[164:167]\n"
  "s_waitcnt lgkmcnt(7)\n"
  "v_mfma_f32_16x16x32_bf16 v[164:167], v[36:39], a[128:131], v[164:167]\n"
  "s_waitcnt lgkmcnt(6)\n"
  "ds_read_b128 a[112:115], v96 offset:45056\n"
  "v_mfma_f32_16x16x32_bf16 v[164:167], v[44:47], a[132:135], v[164:167]\n"
  "s_waitcnt lgkmcnt(6)\n"
  "ds_read_b128 a[116:119], v97 offset:45056\n"
  "v_mfma_f32_16x16x32_bf16 v[164:167], v[52:55], a[136:139], v[164:167]\n"
  "s_waitcnt lgkmcnt(6)\n"
  "ds_read_b128 a[120:123], v98 offset:45056\n"
  "v_mfma_f32_16x16x32_bf16 v[164:167], v[60:63], a[140:143], v[164:167]\n"
  "ds_read_b128 a[124:127], v99 offset:45056\n"
  "v_mfma_f32_16x16x32_bf16 v[160:163], v[56:59], a[140:143], v[160:163]\n"
  "v_mfma_f32_16x16x32_bf16 v[160:163], v[48:51], a[136:139], v[160:163]\n"
  "v_mfma_f32_16x16x32_bf16 v[160:163], v[40:43], a[132:135], v[160:163]\n"
  "v_mfma_f32_16x16x32_bf16 v[160:163], v[32:35], a[128:131], v[160:163]\n"
  "s_waitcnt lgkmcnt(7)\n"
  "v_mfma_f32_16x16x32_bf16 v[160:163], v[64:67], a[96:99], v[160:163]\n"
  "s_waitcnt lgkmcnt(6)\n"
  "ds_read_b128 a[128:131], v96 offset:49152\n"
  "v_mfma_f32_16x16x32_bf16 v[160:163], v[72:75], a[100:103], v[160:163]\n"
  "s_waitcnt lgkmcnt(6)\n"
  "ds_read_b128 a[132:135], v97 offset:49152\n"
  "v_mfma_f32_16x16x32_bf16 v[160:163], v[80:83], a[104:107], v[160:163]\n"
  "s_waitcnt lgkmcnt(6)\n"
  "ds_read_b128 a[136:139], v98 offset:49152\n"
  "v_mfma_f32_16x16x32_bf16 v[160:163], v[88:91], a[108:111], v[160:163]\n"
  "ds_read_b128 a[140:143], v99 offset:49152\n"
  "v_mfma_f32_16x16x32_bf16 v[164:167], v[92:95], a[108:111], v[164:167]\n"
  "v_mfma_f32_16x16x32_bf16 v[164:167], v[84:87], a[104:107], v[164:167]\n"
  "v_mfma_f32_16x16x32_bf16 v[164:167], v[76:79], a[100:103], v[164:167]\n"
  "v_mfma_f32_16x16x32_bf16 v[164:167], v[68:71], a[96:99], v[164:167]\n"
  "s_waitcnt lgkmcnt(7)\n"
  "v_mfma_f32_16x16x32_bf16 v[164:167], a[196:199], a[112:115], v[164:167]\n"
  "s_waitcnt lgkmcnt(6)\n"
  "ds_read_b128 a[96:99], v96 offset:53248\n"
  "v_mfma_f32_16x16x32_bf16 v[164:167], a[204:207], a[116:119], v[164:167]\n"
  "s_waitcnt lgkmcnt(6)\n"
  "ds_read_b128 a[100:103], v97 offset:53248\n"
  "v_mfma_f32_16x16x32_bf16 v[164:167], a[212:215], a[120:123], v[164:167]\n"
  "s_waitcnt lgkmcnt(6)\n"
  "ds_read_b128 a[104:107], v98 offset:53248\n"
  "v_mfma_f32_16x16x32_bf16 v[164:167], a[220:223], a[124:127], v[164:167]\n"
  "ds_read_b128 a[108:111], v99 offset:53248\n"
  "v_mfma_f32_16x16x32_bf16 v[160:163], a[216:219], a[124:127], v[160:163]\n"
  "v_mfma_f32_16x16x32_bf16 v[160:163], a[208:211], a[120:123], v[160:163]\n"
  "v_mfma_f32_16x16x32_bf16 v[160:163], a[200:203], a[116:119], v[160:163]\n"
  "v_mfma_f32_16x16x32_bf16 v[160:163], a[192:195], a[112:115], v[160:163]\n"
  "s_waitcnt lgkmcnt(7)\n"
  "v_mfma_f32_16x16x32_bf16 v[160:163], a[0:3], a[128:131], v[160:163]\n"
  "s_waitcnt lgkmcnt(6)\n"
  "ds_read_b128 a[112:115], v96 offset:57344\n"
  "v_mfma_f32_16x16x32_bf16 v[160:163], a[8:11], a[132:135], v[160:163]\n"
  "s_waitcnt vmcnt(0)\n"
  "v_cmp_ne_u32 vcc, s80, v217\n"
  "s_nop 0\n"
  "s_and_b32 s77, vcc_lo, s75\n"
  "s_cbranch_scc1 L_p2c_%=\n"
  "L_p2d_%=:\n"
  "s_waitcnt lgkmcnt(6)\n"
  "v_mfma_f32_16x16x32_bf16 v[160:163], a[16:19], a[136:139], v[160:163]\n"
  "s_waitcnt lgkmcnt(5)\n"
  "ds_read_b128 a[116:119], v97 offset:57344\n"
  "v_mfma_f32_16x16x32_bf16 v[160:163], a[24:27], a[140:143], v[160:163]\n"
  "s_xor_b32 s85, s5, 0\n"
  "s_lshl_b32 s85, s85, 14\n"
  "buffer_load_dwordx4 v[112:115], v247, s[40:43], s85 offen sc1\n"
  "v_mfma_f32_16x16x32_bf16 v[164:167], a[28:31], a[140:143], v[164:167]\n"
  "ds_read_b128 a[120:123], v98 offset:57344\n"
  "v_mfma_f32_16x16x32_bf16 v[164:167], a[20:23], a[136:139], v[164:167]\n"
  "buffer_load_dwordx4 v[116:119], v248, s[40:43], s85 offen sc1\n"
  "v_mfma_f32_16x16x32_bf16 v[164:167], a[12:15], a[132:135], v[164:167]\n"
  "ds_read_b128 a[124:127], v99 offset:57344\n"
  "v_mfma_f32_16x16x32_bf16 v[164:167], a[4:7], a[128:131], v[164:167]\n"
  "s_waitcnt lgkmcnt(7)\n"
  "buffer_load_dwordx4 v[120:123], v249, s[40:43], s85 offen sc1\n"
  "v_mfma_f32_16x16x32_bf16 v[164:167], a[36:39], a[96:99], v[164:167]\n"
  "s_waitcnt lgkmcnt(6)\n"
  "ds_read_b128 a[128:131], v96 offset:61440\n"
  "v_mfma_f32_16x16x32_bf16 v[164:167], a[44:47], a[100:103], v[164:167]\n"
  "s_waitcnt lgkmcnt(6)\n"
  "s_or_b32 s85, s71, 131072\n"
  "buffer_load_dwordx4 v[124:127], v247, s[40:43], s85 offen sc1\n"
  "v_mfma_f32_16x16x32_bf16 v[164:167], a[52:55], a[104:107], v[164:167]\n"
  "s_waitcnt lgkmcnt(5)\n"
  "ds_read_b128 a[132:135], v97 offset:61440\n"
  "v_mfma_f32_16x16x32_bf16 v[164:167], a[60:63], a[108:111], v[164:167]\n"
  "buffer_load_dwordx4 v[128:131], v248, s[40:43], s85 offen sc1\n"
  "v_mfma_f32_16x16x32_bf16 v[160:163], a[56:59], a[108:111], v[160:163]\n"
  "ds_read_b128 a[136:139], v98 offset:61440\n"
  "v_mfma_f32_16x16x32_bf16 v[160:163], a[48:51], a[104:107], v[160:163]\n"
  "buffer_load_dwordx4 v[132:135], v249, s[40:43], s85 offen sc1\n"
  "v_mfma_f32_16x16x32_bf16 v[160:163], a[40:43], a[100:103], v[160:163]\n"
  "ds_read_b128 a[140:143], v99 offset:61440\n"
  "v_mfma_f32_16x16x32_bf16 v[160:163], a[32:35], a[96:99], v[160:163]\n"
  "s_waitcnt lgkmcnt(7)\n"
  "s_xor_b32 s85, s5, 4\n"
  "s_lshl_b32 s85, s85, 14\n"
  "buffer_load_dwordx4 v[136:139], v247, s[40:43], s85 offen sc1\n"
  "v_mfma_f32_16x16x32_bf16 v[160:163], a[160:163], a[112:115], v[160:163]\n"
  "s_waitcnt lgkmcnt(6)\n"
  "v_mfma_f32_16x16x32_bf16 v[160:163], a[168:171], a[116:119], v[160:163]\n"
  "s_waitcnt lgkmcnt(5)\n"
  "buffer_load_dwordx4 v[140:143], v248, s[40:43], s85 offen sc1\n"
  "v_mfma_f32_16x16x32_bf16 v[160:163], a[176:179], a[120:123], v[160:163]\n"
  "s_waitcnt lgkmcnt(4)\n"
  "v_mfma_f32_16x16x32_bf16 v[160:163], a[184:187], a[124:127], v[160:163]\n"
  "buffer_load_dwordx4 v[144:147], v249, s[40:43], s85 offen sc1\n"
  "v_mfma_f32_16x16x32_bf16 v[164:167], a[188:191], a[124:127], v[164:167]\n"
  "v_mfma_f32_16x16x32_bf16 v[164:167], a[180:183], a[120:123], v[164:167]\n"
  "buffer_load_dword v217, v227, s[56:59], 0 offen sc1\n"
  "v_mfma_f32_16x16x32_bf16 v[164:167], a[172:175], a[116:119], v[164:167]\n"
  "v_mfma_f32_16x16x32_bf16 v[164:167], a[164:167], a[112:115], v[164:167]\n"
  "s_waitcnt lgkmcnt(3)\n"
  "v_mfma_f32_16x16x32_bf16 v[164:167], a[68:71], a[128:131], v[164:167]\n"
  "s_waitcnt lgkmcnt(2)\n"
  "v_mfma_f32_16x16x32_bf16 v[164:167], a[76:79], a[132:135], v[164:167]\n"
  "s_waitcnt lgkmcnt(1)\n"
  "v_mfma_f32_16x16x32_bf16 v[164:167], a[84:87], a[136:139], v[164:167]\n"
  "s_waitcnt lgkmcnt(0)\n"
  "v_mfma_f32_16x16x32_bf16 v[164:167], a[92:95], a[140:143], v[164:167]\n"
  "v_mfma_f32_16x16x32_bf16 v[160:163], a[88:91], a[140:143], v[160:163]\n"
  "v_mfma_f32_16x16x32_bf16 v[160:163], a[80:83], a[136:139], v[160:163]\n"
  "v_mfma_f32_16x16x32_bf16 v[160:163], a[72:75], a[132:135], v[160:163]\n"
  "v_mfma_f32_16x16x32_bf16 v[160:163], a[64:67], a[128:131], v[160:163]\n"
  "s_waitcnt vmcnt(7)\n"
  "v_mfma_f32_16x16x32_bf16 v[168:171], v[154:157], v[112:115], v[168:171]\n"
  "v_mfma_f32_16x16x32_bf16 v[168:171], v[154:157], v[116:119], v[168:171]\n"
  "v_mfma_f32_16x16x32_bf16 v[168:171], v[154:157], v[120:123], v[168:171]\n"
  "v_mfma_f32_16x16x32_bf16 v[172:175], v[152:155], v[112:115], v[172:175]\n"
  "v_mfma_f32_16x16x32_bf16 v[172:175], v[152:155], v[116:119], v[172:175]\n"
  "v_mfma_f32_16x16x32_bf16 v[172:175], v[152:155], v[120:123], v[172:175]\n"
  "s_waitcnt vmcnt(4)\n"
  "v_mfma_f32_16x16x32_bf16 v[176:179], v[154:157], v[124:127], v[176:179]\n"
  "v_mfma_f32_16x16x32_bf16 v[176:179], v[154:157], v[128:131], v[176:179]\n"
  "v_mfma_f32_16x16x32_bf16 v[176:179], v[154:157], v[132:135], v[176:179]\n"
  "v_mfma_f32_16x16x32_bf16 v[180:183], v[152:155], v[124:127], v[180:183]\n"
  "s_xor_b32 s77, s5, 0\n"
  "s_or_b32 s77, s77, s70\n"
  "s_lshl_b32 s77, s77, 14\n"
  "v_cvt_pk_bf16_f32 v0, v168, v169\n"
  "v_cvt_pk_bf16_f32 v1, v170, v171\n"
  "v_cvt_pk_bf16_f32 v2, v172, v173\n"
  "v_cvt_pk_bf16_f32 v3, v174, v175\n"
  "s_nop 0\n"
  "v_permlane16_swap_b32 v0, v2\n"
  "v_permlane16_swap_b32 v1, v3\n"
  "buffer_store_dwordx4 v[0:3], v226, s[44:47], s77 offen sc1\n"
  "v_mfma_f32_16x16x32_bf16 v[180:183], v[152:155], v[128:131], v[180:183]\n"
  "v_mfma_f32_16x16x32_bf16 v[180:183], v[152:155], v[132:135], v[180:183]\n"
  "s_waitcnt vmcnt(2)\n"
  "v_mfma_f32_16x16x32_bf16 v[160:163], v[154:157], v[136:139], v[160:163]\n"
  "v_mfma_f32_16x16x32_bf16 v[160:163], v[154:157], v[140:143], v[160:163]\n"
  "v_mfma_f32_16x16x32_bf16 v[160:163], v[154:157], v[144:147], v[160:163]\n"
  "v_mfma_f32_16x16x32_bf16 v[164:167], v[152:155], v[136:139], v[164:167]\n"
  "s_lshr_b32 s77, s70, 3\n"
  "s_add_u32 s77, s77, 16\n"
  "s_lshl_b32 s77, s77, 14\n"
  "s_or_b32 s77, s77, s71\n"
  "v_cvt_pk_bf16_f32 v4, v176, v177\n"
  "v_cvt_pk_bf16_f32 v5, v178, v179\n"
  "v_cvt_pk_bf16_f32 v6, v180, v181\n"
  "v_cvt_pk_bf16_f32 v7, v182, v183\n"
  "s_nop 0\n"
  "v_permlane16_swap_b32 v4, v6\n"
  "v_permlane16_swap_b32 v5, v7\n"
  "buffer_store_dwordx4 v[4:7], v226, s[44:47], s77 offen sc1\n"
  "v_mfma_f32_16x16x32_bf16 v[164:167], v[152:155], v[140:143], v[164:167]\n"
  "v_mfma_f32_16x16x32_bf16 v[164:167], v[152:155], v[144:147], v[164:167]\n"
  "s_xor_b32 s77, s5, 4\n"
  "s_or_b32 s77, s77, s70\n"
  "s_lshl_b32 s77, s77, 14\n"
  "v_cvt_pk_bf16_f32 v8, v160, v161\n"
  "v_cvt_pk_bf16_f32 v9, v162, v163\n"
  "s_nop 2\n"
  "v_cvt_pk_bf16_f32 v10, v164, v165\n"
  "v_cvt_pk_bf16_f32 v11, v166, v167\n"
  "s_nop 0\n"
  "v_permlane16_swap_b32 v8, v10\n"
  "v_permlane16_swap_b32 v9, v11\n"
  "buffer_store_dwordx4 v[8:11], v226, s[44:47], s77 offen sc1\n"
  "s_waitcnt vmcnt(3)\n"
  "v_cmp_ne_u32 vcc, v217, v222\n"
  "v_mov_b32 v222, v217\n"
  "s_and_b32 s77, vcc_lo, 0xf00\n"
  "s_cbranch_scc0 L_fin_%=\n"
  "buffer_store_dword v222, v225, s[56:59], s73 offen offset:512 sc1\n"
  "L_fin_%=:\n"
  "s_branch L_end_%=\n"
  "L_p1ac_%=:\n"
  "s_mov_b32 s87, 0\n"
  "L_p1a_%=:\n"
  "buffer_load_dword v217, v216, s[56:59], s6 offen offset:256 sc1\n"
  "s_waitcnt vmcnt(0)\n"
  "v_cmp_ne_u32 vcc, s80, v217\n"
  "s_add_u32 s87, s87, 1\n"
  "s_lshr_b32 s86, s87, 16\n"
  "s_cbranch_scc1 L_p1ax_%=\n"
  "s_and_b32 s77, vcc_lo, s74\n"
  "s_cbranch_scc0 L_p1ax_%=\n"
  "s_sleep 2\n"
  "s_and_b32 s77, s87, 15\n"
  "s_cbranch_scc1 L_p1a_%=\n"
  "buffer_load_dword v222, v227, s[56:59], 0 offen sc1\n"
  "s_waitcnt vmcnt(0)\n"
  "buffer_store_dword v222, v218, s[56:59], s6 offen offset:256 sc1\n"
  "s_branch L_p1a_%=\n"
  "L_p1ax_%=:\n"
  "s_branch L_p1ad_%=\n"
  "L_p1bc_%=:\n"
  "s_mov_b32 s87, 0\n"
  "L_p1b_%=:\n"
  "buffer_load_dword v217, v216, s[56:59], 0 offen offset:384 sc1\n"
  "s_waitcnt vmcnt(0)\n"
  "v_cmp_ne_u32 vcc, s80, v217\n"
  "s_add_u32 s87, s87, 1\n"
  "s_lshr_b32 s86, s87, 16\n"
  "s_cbranch_scc1 L_p1bx_%=\n"
  "s_and_b32 s77, vcc_lo, 0xff\n"
  "s_cbranch_scc0 L_p1bx_%=\n"
  "s_sleep 2\n"
  "s_branch L_p1b_%=\n"
  "L_p1bx_%=:\n"
  "s_branch L_p1bd_%=\n"
  "L_p1cc_%=:\n"
  "s_mov_b32 s87, 0\n"
  "L_p1c_%=:\n"
  "buffer_load_dword v217, v216, s[56:59], 0 offen offset:320 sc1\n"
  "s_waitcnt vmcnt(0)\n"
  "v_cmp_ne_u32 vcc, s80, v217\n"
  "s_add_u32 s87, s87, 1\n"
  "s_lshr_b32 s86, s87, 16\n"
  "s_cbranch_scc1 L_p1cx_%=\n"
  "s_and_b32 s77, vcc_lo, 0xff\n"
  "s_cbranch_scc0 L_p1cx_%=\n"
  "s_sleep 2\n"
  "s_branch L_p1c_%=\n"
  "L_p1cx_%=:\n"
  "s_branch L_p1cd_%=\n"
  "L_p2c_%=:\n"
  "s_mov_b32 s87, 0\n"
  "L_p2_%=:\n"
  "buffer_load_dword v217, v216, s[56:59], s73 offen offset:512 sc1\n"
  "s_waitcnt vmcnt(0)\n"
  "v_cmp_ne_u32 vcc, s80, v217\n"
  "s_add_u32 s87, s87, 1\n"
  "s_lshr_b32 s86, s87, 16\n"
  "s_cbranch_scc1 L_p2x_%=\n"
  "s_and_b32 s77, vcc_lo, s75\n"
  "s_cbranch_scc0 L_p2x_%=\n"
  "s_sleep 2\n"
  "s_and_b32 s77, s87, 15\n"
  "s_cbranch_scc1 L_p2_%=\n"
  "buffer_load_dword v222, v227, s[56:59], 0 offen sc1\n"
  "s_waitcnt vmcnt(0)\n"
  "buffer_store_dword v222, v225, s[56:59], s73 offen offset:512 sc1\n"
  "s_branch L_p2_%=\n"
  "L_p2x_%=:\n"
  "s_branch L_p2d_%=\n"
  "L_fb_%=:\n"
  "s_mov_b32 s76, -1\n"
  "s_waitcnt vmcnt(0) lgkmcnt(0)\n"
  "s_barrier\n"
  "s_branch L_top_%=\n"
  "L_fbm_%=:\n"
  "s_mov_b32 s71, 0\n"
  "v_mov_b32 v100, 0xff800000\n"
  "v_mov_b32 v101, 0xff800000\n"
  "v_mov_b32 v102, 0xff800000\n"
  "v_mov_b32 v103, 0xff800000\n"
  "v_mov_b32 v104, 0xff800000\n"
  "L_fbl_%=:\n"
  "s_waitcnt vmcnt(0) lgkmcnt(0)\n"
  "s_barrier\n"
  "s_add_u32 m0, s67, 147456\n"
  "s_nop 0\n"
  "buffer_load_dwordx4 v252, s[44:47], s71 offen nt lds\n"
  "buffer_load_dwordx4 v253, s[44:47], s71 offen offset:1024 nt lds\n"
  "buffer_load_dwordx4 v252, s[44:47], s71 offen offset:2048 nt lds\n"
  "buffer_load_dwordx4 v253, s[44:47], s71 offen offset:3072 nt lds\n"
  "s_add_u32 s71, s71, s73\n"
  "s_and_b32 s71, s71, s8\n"
  "s_waitcnt vmcnt(0)\n"
  "s_barrier\n"
  "ds_read_b128 a[160:163], v228 offset:32768\n"
  "ds_read_b128 a[164:167], v229 offset:32768\n"
  "ds_read_b128 a[168:171], v230 offset:32768\n"
  "ds_read_b128 a[172:175], v231 offset:32768\n"
  "s_waitcnt lgkmcnt(3)\n"
  "v_mfma_f32_16x16x32_bf16 v[80:83], a[160:163], v[0:3], 0\n"
  "s_waitcnt lgkmcnt(2)\n"
  "v_mfma_f32_16x16x32_bf16 v[80:83], a[164:167], v[4:7], v[80:83]\n"
  "s_waitcnt lgkmcnt(1)\n"
  "v_mfma_f32_16x16x32_bf16 v[80:83], a[168:171], v[8:11], v[80:83]\n"
  "s_waitcnt lgkmcnt(0)\n"
  "v_mfma_f32_16x16x32_bf16 v[80:83], a[172:175], v[12:15], v[80:83]\n"
  "v_mfma_f32_16x16x32_bf16 v[84:87], a[160:163], v[16:19], 0\n"
  "v_mfma_f32_16x16x32_bf16 v[84:87], a[164:167], v[20:23], v[84:87]\n"
  "v_mfma_f32_16x16x32_bf16 v[84:87], a[168:171], v[24:27], v[84:87]\n"
  "v_mfma_f32_16x16x32_bf16 v[84:87], a[172:175], v[28:31], v[84:87]\n"
  "v_mfma_f32_16x16x32_bf16 v[88:91], a[160:163], v[32:35], 0\n"
  "v_mfma_f32_16x16x32_bf16 v[88:91], a[164:167], v[36:39], v[88:91]\n"
  "v_mfma_f32_16x16x32_bf16 v[88:91], a[168:171], v[40:43], v[88:91]\n"
  "v_mfma_f32_16x16x32_bf16 v[88:91], a[172:175], v[44:47], v[88:91]\n"
  "v_mfma_f32_16x16x32_bf16 v[92:95], a[160:163], v[48:51], 0\n"
  "v_mfma_f32_16x16x32_bf16 v[92:95], a[164:167], v[52:55], v[92:95]\n"
  "v_mfma_f32_16x16x32_bf16 v[92:95], a[168:171], v[56:59], v[92:95]\n"
  "v_mfma_f32_16x16x32_bf16 v[92:95], a[172:175], v[60:63], v[92:95]\n"
  "v_mfma_f32_16x16x32_bf16 v[96:99], a[160:163], v[64:67], 0\n"
  "v_mfma_f32_16x16x32_bf16 v[96:99], a[164:167], v[68:71], v[96:99]\n"
  "v_mfma_f32_16x16x32_bf16 v[96:99], a[168:171], v[72:75], v[96:99]\n"
  "v_mfma_f32_16x16x32_bf16 v[96:99], a[172:175], v[76:79], v[96:99]\n"
  "v_max3_f32 v100, v80, v81, v100\n"
  "v_max3_f32 v100, v82, v83, v100\n"
  "v_max3_f32 v101, v84, v85, v101\n"
  "v_max3_f32 v101, v86, v87, v101\n"
  "v_max3_f32 v102, v88, v89, v102\n"
  "v_max3_f32 v102, v90, v91, v102\n"
  "v_max3_f32 v103, v92, v93, v103\n"
  "v_max3_f32 v103, v94, v95, v103\n"
  "v_max3_f32 v104, v96, v97, v104\n"
  "v_max3_f32 v104, v98, v99, v104\n"
  "ds_read_b128 a[176:179], v228 offset:34816\n"
  "ds_read_b128 a[180:183], v229 offset:34816\n"
  "ds_read_b128 a[184:187], v230 offset:34816\n"
  "ds_read_b128 a[188:191], v231 offset:34816\n"
  "s_waitcnt lgkmcnt(3)\n"
  "v_mfma_f32_16x16x32_bf16 v[80:83], a[176:179], v[0:3], 0\n"
  "s_waitcnt lgkmcnt(2)\n"
  "v_mfma_f32_16x16x32_bf16 v[80:83], a[180:183], v[4:7], v[80:83]\n"
  "s_waitcnt lgkmcnt(1)\n"
  "v_mfma_f32_16x16x32_bf16 v[80:83], a[184:187], v[8:11], v[80:83]\n"
  "s_waitcnt lgkmcnt(0)\n"
  "v_mfma_f32_16x16x32_bf16 v[80:83], a[188:191], v[12:15], v[80:83]\n"
  "v_mfma_f32_16x16x32_bf16 v[84:87], a[176:179], v[16:19], 0\n"
  "v_mfma_f32_16x16x32_bf16 v[84:87], a[180:183], v[20:23], v[84:87]\n"
  "v_mfma_f32_16x16x32_bf16 v[84:87], a[184:187], v[24:27], v[84:87]\n"
  "v_mfma_f32_16x16x32_bf16 v[84:87], a[188:191], v[28:31], v[84:87]\n"
  "v_mfma_f32_16x16x32_bf16 v[88:91], a[176:179], v[32:35], 0\n"
  "v_mfma_f32_16x16x32_bf16 v[88:91], a[180:183], v[36:39], v[88:91]\n"
  "v_mfma_f32_16x16x32_bf16 v[88:91], a[184:187], v[40:43], v[88:91]\n"
  "v_mfma_f32_16x16x32_bf16 v[88:91], a[188:191], v[44:47], v[88:91]\n"
  "v_mfma_f32_16x16x32_bf16 v[92:95], a[176:179], v[48:51], 0\n"
  "v_mfma_f32_16x16x32_bf16 v[92:95], a[180:183], v[52:55], v[92:95]\n"
  "v_mfma_f32_16x16x32_bf16 v[92:95], a[184:187], v[56:59], v[92:95]\n"
  "v_mfma_f32_16x16x32_bf16 v[92:95], a[188:191], v[60:63], v[92:95]\n"
  "v_mfma_f32_16x16x32_bf16 v[96:99], a[176:179], v[64:67], 0\n"
  "v_mfma_f32_16x16x32_bf16 v[96:99], a[180:183], v[68:71], v[96:99]\n"
  "v_mfma_f32_16x16x32_bf16 v[96:99], a[184:187], v[72:75], v[96:99]\n"
  "v_mfma_f32_16x16x32_bf16 v[96:99], a[188:191], v[76:79], v[96:99]\n"
  "v_max3_f32 v100, v80, v81, v100\n"
  "v_max3_f32 v100, v82, v83, v100\n"
  "v_max3_f32 v101, v84, v85, v101\n"
  "v_max3_f32 v101, v86, v87, v101\n"
  "v_max3_f32 v102, v88, v89, v102\n"
  "v_max3_f32 v102, v90, v91, v102\n"
  "v_max3_f32 v103, v92, v93, v103\n"
  "v_max3_f32 v103, v94, v95, v103\n"
  "v_max3_f32 v104, v96, v97, v104\n"
  "v_max3_f32 v104, v98, v99, v104\n"
  "ds_read_b128 a[160:163], v232 offset:32768\n"
  "ds_read_b128 a[164:167], v233 offset:32768\n"
  "ds_read_b128 a[168:171], v234 offset:32768\n"
  "ds_read_b128 a[172:175], v235 offset:32768\n"
  "s_waitcnt lgkmcnt(3)\n"
  "v_mfma_f32_16x16x32_bf16 v[80:83], a[160:163], v[0:3], 0\n"
  "s_waitcnt lgkmcnt(2)\n"
  "v_mfma_f32_16x16x32_bf16 v[80:83], a[164:167], v[4:7], v[80:83]\n"
  "s_waitcnt lgkmcnt(1)\n"
  "v_mfma_f32_16x16x32_bf16 v[80:83], a[168:171], v[8:11], v[80:83]\n"
  "s_waitcnt lgkmcnt(0)\n"
  "v_mfma_f32_16x16x32_bf16 v[80:83], a[172:175], v[12:15], v[80:83]\n"
  "v_mfma_f32_16x16x32_bf16 v[84:87], a[160:163], v[16:19], 0\n"
  "v_mfma_f32_16x16x32_bf16 v[84:87], a[164:167], v[20:23], v[84:87]\n"
  "v_mfma_f32_16x16x32_bf16 v[84:87], a[168:171], v[24:27], v[84:87]\n"
  "v_mfma_f32_16x16x32_bf16 v[84:87], a[172:175], v[28:31], v[84:87]\n"
  "v_mfma_f32_16x16x32_bf16 v[88:91], a[160:163], v[32:35], 0\n"
  "v_mfma_f32_16x16x32_bf16 v[88:91], a[164:167], v[36:39], v[88:91]\n"
  "v_mfma_f32_16x16x32_bf16 v[88:91], a[168:171], v[40:43], v[88:91]\n"
  "v_mfma_f32_16x16x32_bf16 v[88:91], a[172:175], v[44:47], v[88:91]\n"
  "v_mfma_f32_16x16x32_bf16 v[92:95], a[160:163], v[48:51], 0\n"
  "v_mfma_f32_16x16x32_bf16 v[92:95], a[164:167], v[52:55], v[92:95]\n"
  "v_mfma_f32_16x16x32_bf16 v[92:95], a[168:171], v[56:59], v[92:95]\n"
  "v_mfma_f32_16x16x32_bf16 v[92:95], a[172:175], v[60:63], v[92:95]\n"
  "v_mfma_f32_16x16x32_bf16 v[96:99], a[160:163], v[64:67], 0\n"
  "v_mfma_f32_16x16x32_bf16 v[96:99], a[164:167], v[68:71], v[96:99]\n"
  "v_mfma_f32_16x16x32_bf16 v[96:99], a[168:171], v[72:75], v[96:99]\n"
  "v_mfma_f32_16x16x32_bf16 v[96:99], a[172:175], v[76:79], v[96:99]\n"
  "v_max3_f32 v100, v80, v81, v100\n"
  "v_max3_f32 v100, v82, v83, v100\n"
  "v_max3_f32 v101, v84, v85, v101\n"
  "v_max3_f32 v101, v86, v87, v101\n"
  "v_max3_f32 v102, v88, v89, v102\n"
  "v_max3_f32 v102, v90, v91, v102\n"
  "v_max3_f32 v103, v92, v93, v103\n"
  "v_max3_f32 v103, v94, v95, v103\n"
  "v_max3_f32 v104, v96, v97, v104\n"
  "v_max3_f32 v104, v98, v99, v104\n"
  "ds_read_b128 a[176:179], v232 offset:34816\n"
  "ds_read_b128 a[180:183], v233 offset:34816\n"
  "ds_read_b128 a[184:187], v234 offset:34816\n"
  "ds_read_b128 a[188:191], v235 offset:34816\n"
  "s_waitcnt lgkmcnt(3)\n"
  "v_mfma_f32_16x16x32_bf16 v[80:83], a[176:179], v[0:3], 0\n"
  "s_waitcnt lgkmcnt(2)\n"
  "v_mfma_f32_16x16x32_bf16 v[80:83], a[180:183], v[4:7], v[80:83]\n"
  "s_waitcnt lgkmcnt(1)\n"
  "v_mfma_f32_16x16x32_bf16 v[80:83], a[184:187], v[8:11], v[80:83]\n"
  "s_waitcnt lgkmcnt(0)\n"
  "v_mfma_f32_16x16x32_bf16 v[80:83], a[188:191], v[12:15], v[80:83]\n"
  "v_mfma_f32_16x16x32_bf16 v[84:87], a[176:179], v[16:19], 0\n"
  "v_mfma_f32_16x16x32_bf16 v[84:87], a[180:183], v[20:23], v[84:87]\n"
  "v_mfma_f32_16x16x32_bf16 v[84:87], a[184:187], v[24:27], v[84:87]\n"
  "v_mfma_f32_16x16x32_bf16 v[84:87], a[188:191], v[28:31], v[84:87]\n"
  "v_mfma_f32_16x16x32_bf16 v[88:91], a[176:179], v[32:35], 0\n"
  "v_mfma_f32_16x16x32_bf16 v[88:91], a[180:183], v[36:39], v[88:91]\n"
  "v_mfma_f32_16x16x32_bf16 v[88:91], a[184:187], v[40:43], v[88:91]\n"
  "v_mfma_f32_16x16x32_bf16 v[88:91], a[188:191], v[44:47], v[88:91]\n"
  "v_mfma_f32_16x16x32_bf16 v[92:95], a[176:179], v[48:51], 0\n"
  "v_mfma_f32_16x16x32_bf16 v[92:95], a[180:183], v[52:55], v[92:95]\n"
  "v_mfma_f32_16x16x32_bf16 v[92:95], a[184:187], v[56:59], v[92:95]\n"
  "v_mfma_f32_16x16x32_bf16 v[92:95], a[188:191], v[60:63], v[92:95]\n"
  "v_mfma_f32_16x16x32_bf16 v[96:99], a[176:179], v[64:67], 0\n"
  "v_mfma_f32_16x16x32_bf16 v[96:99], a[180:183], v[68:71], v[96:99]\n"
  "v_mfma_f32_16x16x32_bf16 v[96:99], a[184:187], v[72:75], v[96:99]\n"
  "v_mfma_f32_16x16x32_bf16 v[96:99], a[188:191], v[76:79], v[96:99]\n"
  "v_max3_f32 v100, v80, v81, v100\n"
  "v_max3_f32 v100, v82, v83, v100\n"
  "v_max3_f32 v101, v84, v85, v101\n"
  "v_max3_f32 v101, v86, v87, v101\n"
  "v_max3_f32 v102, v88, v89, v102\n"
  "v_max3_f32 v102, v90, v91, v102\n"
  "v_max3_f32 v103, v92, v93, v103\n"
  "v_max3_f32 v103, v94, v95, v103\n"
  "v_max3_f32 v104, v96, v97, v104\n"
  "v_max3_f32 v104, v98, v99, v104\n"
  "s_cmp_lt_u32 s71, 524288\n"
  "s_cbranch_scc1 L_fbl_%=\n"
  "s_mov_b32 s71, 0\n"
  "s_mov_b32 s72, 0\n"
  "s_barrier\n"
  "s_add_u32 m0, s67, 147456\n"
  "s_nop 0\n"
  "buffer_load_dwordx4 v252, s[44:47], s71 offen nt lds\n"
  "buffer_load_dwordx4 v253, s[44:47], s71 offen offset:1024 nt lds\n"
  "buffer_load_dwordx4 v252, s[44:47], s71 offen offset:2048 nt lds\n"
  "buffer_load_dwordx4 v253, s[44:47], s71 offen offset:3072 nt lds\n"
  "s_add_u32 s71, s71, s73\n"
  "s_and_b32 s71, s71, s8\n"
  "s_add_u32 m0, s67, 131072\n"
  "s_nop 0\n"
  "buffer_load_dwordx4 v252, s[44:47], s71 offen nt lds\n"
  "buffer_load_dwordx4 v253, s[44:47], s71 offen offset:1024 nt lds\n"
  "buffer_load_dwordx4 v252, s[44:47], s71 offen offset:2048 nt lds\n"
  "buffer_load_dwordx4 v253, s[44:47], s71 offen offset:3072 nt lds\n"
  "s_add_u32 s71, s71, s73\n"
  "s_and_b32 s71, s71, s8\n"
  "s_add_u32 m0, s67, 114688\n"
  "s_nop 0\n"
  "buffer_load_dwordx4 v252, s[44:47], s71 offen nt lds\n"
  "buffer_load_dwordx4 v253, s[44:47], s71 offen offset:1024 nt lds\n"
  "buffer_load_dwordx4 v252, s[44:47], s71 offen offset:2048 nt lds\n"
  "buffer_load_dwordx4 v253, s[44:47], s71 offen offset:3072 nt lds\n"
  "s_add_u32 s71, s71, s73\n"
  "s_and_b32 s71, s71, s8\n"
  "s_add_u32 m0, s16, 0\n"
  "s_nop 0\n"
  "buffer_load_dwordx4 v254, s[48:51], s72 offen nt lds\n"
  "buffer_load_dwordx4 v255, s[48:51], s72 offen offset:1024 nt lds\n"
  "buffer_load_dwordx4 v254, s[48:51], s72 offen offset:2048 nt lds\n"
  "buffer_load_dwordx4 v255, s[48:51], s72 offen offset:3072 nt lds\n"
  "s_add_u32 s72, s72, s74\n"
  "s_and_b32 s72, s72, s9\n"
  "v_mov_b32 v105, v100\n"
  "v_mov_b32 v106, v101\n"
  "v_mov_b32 v107, v102\n"
  "v_mov_b32 v108, v103\n"
  "v_mov_b32 v109, v104\n"
  "v_permlane32_swap_b32 v105, v100\n"
  "v_permlane32_swap_b32 v106, v101\n"
  "v_permlane32_swap_b32 v107, v102\n"
  "v_permlane32_swap_b32 v108, v103\n"
  "v_permlane32_swap_b32 v109, v104\n"
  "v_max_f32 v100, v100, v105\n"
  "v_max_f32 v101, v101, v106\n"
  "v_max_f32 v102, v102, v107\n"
  "v_max_f32 v103, v103, v108\n"
  "v_max_f32 v104, v104, v109\n"
  "v_mov_b32 v105, v100\n"
  "v_mov_b32 v106, v101\n"
  "v_mov_b32 v107, v102\n"
  "v_mov_b32 v108, v103\n"
  "v_mov_b32 v109, v104\n"
  "v_permlane16_swap_b32 v105, v100\n"
  "v_permlane16_swap_b32 v106, v101\n"
  "v_permlane16_swap_b32 v107, v102\n"
  "v_permlane16_swap_b32 v108, v103\n"
  "v_permlane16_swap_b32 v109, v104\n"
  "v_max_f32 v100, v100, v105\n"
  "v_max_f32 v101, v101, v106\n"
  "v_max_f32 v102, v102, v107\n"
  "v_max_f32 v103, v103, v108\n"
  "v_max_f32 v104, v104, v109\n"
  "v_sub_f32 v200, 0, v100\n"
  "v_sub_f32 v201, 0, v100\n"
  "v_sub_f32 v202, 0, v100\n"
  "v_sub_f32 v203, 0, v100\n"
  "v_sub_f32 v204, 0, v101\n"
  "v_sub_f32 v205, 0, v101\n"
  "v_sub_f32 v206, 0, v101\n"
  "v_sub_f32 v207, 0, v101\n"
  "v_sub_f32 v208, 0, v102\n"
  "v_sub_f32 v209, 0, v102\n"
  "v_sub_f32 v210, 0, v102\n"
  "v_sub_f32 v211, 0, v102\n"
  "v_sub_f32 v212, 0, v103\n"
  "v_sub_f32 v213, 0, v103\n"
  "v_sub_f32 v214, 0, v103\n"
  "v_sub_f32 v215, 0, v103\n"
  "v_sub_f32 v216, 0, v104\n"
  "v_sub_f32 v217, 0, v104\n"
  "v_sub_f32 v218, 0, v104\n"
  "v_sub_f32 v219, 0, v104\n"
  "s_waitcnt vmcnt(0)\n"
  "s_barrier\n"
  "ds_read_b128 a[160:163], v228 offset:32768\n"
  "ds_read_b128 a[164:167], v229 offset:32768\n"
  "ds_read_b128 a[168:171], v230 offset:32768\n"
  "ds_read_b128 a[172:175], v231 offset:32768\n"
  "s_waitcnt lgkmcnt(0)\n"
  "s_branch L_join_%=\n"
  "L_end_%=:\n"
  : : "s"(__builtin_amdgcn_kernarg_segment_ptr()), "s"(blockIdx.x), "s"(w), "s"((unsigned)(size_t)lds)
  : "memory", "vcc", "scc", "v0","v1","v2","v3","v4","v5","v6","v7","v8","v9","v10","v11","v12","v13","v14","v15","v16","v17","v18","v19","v20","v21","v22","v23","v24","v25","v26","v27","v28","v29","v30","v31","v32","v33","v34","v35","v36","v37","v38","v39","v40","v41","v42","v43","v44","v45","v46","v47","v48","v49","v50","v51","v52","v53","v54","v55","v56","v57","v58","v59","v60","v61","v62","v63","v64","v65","v66","v67","v68","v69","v70","v71","v72","v73","v74","v75","v76","v77","v78","v79","v80","v81","v82","v83","v84","v85","v86","v87","v88","v89","v90","v91","v92","v93","v94","v95","v96","v97","v98","v99","v100","v101","v102","v103","v104","v105","v106","v107","v108","v109","v110","v111","v112","v113","v114","v115","v116","v117","v118","v119","v120","v121","v122","v123","v124","v125","v126","v127","v128","v129","v130","v131","v132","v133","v134","v135","v136","v137","v138","v139","v140","v141","v142","v143","v144","v145","v146","v147","v148","v149","v150","v151","v152","v153","v154","v155","v156","v157","v158","v159","v160","v161","v162","v163","v164","v165","v166","v167","v168","v169","v170","v171","v172","v173","v174","v175","v176","v177","v178","v179","v180","v181","v182","v183","v184","v185","v186","v187","v188","v189","v190","v191","v192","v193","v194","v195","v196","v197","v198","v199","v200","v201","v202","v203","v204","v205","v206","v207","v208","v209","v210","v211","v212","v213","v214","v215","v216","v217","v218","v219","v220","v221","v222","v223","v224","v225","v226","v227","v228","v229","v230","v231","v232","v233","v234","v235","v236","v237","v238","v239","v240","v241","v242","v243","v244","v245","v246","v247","v248","v249","v250","v251","v252","v253","v254","v255","a0","a1","a2","a3","a4","a5","a6","a7","a8","a9","a10","a11","a12","a13","a14","a15","a16","a17","a18","a19","a20","a21","a22","a23","a24","a25","a26","a27","a28","a29","a30","a31","a32","a33","a34","a35","a36","a37","a38","a39","a40","a41","a42","a43","a44","a45","a46","a47","a48","a49","a50","a51","a52","a53","a54","a55","a56","a57","a58","a59","a60","a61","a62","a63","a64","a65","a66","a67","a68","a69","a70","a71","a72","a73","a74","a75","a76","a77","a78","a79","a80","a81","a82","a83","a84","a85","a86","a87","a88","a89","a90","a91","a92","a93","a94","a95","a96","a97","a98","a99","a100","a101","a102","a103","a104","a105","a106","a107","a108","a109","a110","a111","a112","a113","a114","a115","a116","a117","a118","a119","a120","a121","a122","a123","a124","a125","a126","a127","a128","a129","a130","a131","a132","a133","a134","a135","a136","a137","a138","a139","a140","a141","a142","a143","a144","a145","a146","a147","a148","a149","a150","a151","a152","a153","a154","a155","a156","a157","a158","a159","a160","a161","a162","a163","a164","a165","a166","a167","a168","a169","a170","a171","a172","a173","a174","a175","a176","a177","a178","a179","a180","a181","a182","a183","a184","a185","a186","a187","a188","a189","a190","a191","a192","a193","a194","a195","a196","a197","a198","a199","a200","a201","a202","a203","a204","a205","a206","a207","a208","a209","a210","a211","a212","a213","a214","a215","a216","a217","a218","a219","a220","a221","a222","a223","a224","a225","a226","a227","a228","a229","a230","a231","a232","a233","a234","a235","a236","a237","a238","a239","a240","a241","a242","a243","a244","a245","a246","a247","a248","a249","a250","a251","a252","a253","a254","a255","s5","s6","s7","s8","s9","s10","s11","s12","s13","s14","s15","s16","s17","s18","s19","s20","s21","s22","s23","s24","s25","s26","s27","s28","s29","s30","s31","s33","s34","s35","s36","s37","s38","s39","s40","s41","s42","s43","s44","s45","s46","s47","s48","s49","s50","s51","s52","s53","s54","s55","s56","s57","s58","s59","s60","s61","s62","s63","s64","s65","s66","s67","s68","s69","s70","s71","s72","s73","s74","s75","s76","s77","s78","s79","s80","s81","s82","s83","s84","s85","s86","s87","s88","s89","s90","s91","s92","s93","s94","s95","s96","s97","s98","s99");
}

extern "C" void kernel_launch(void* const* d_in, const int* in_sizes, int n_in, void* d_out, int out_size, void* d_ws, size_t ws_size, hipStream_t stream) {
  mha_fused_kernel<<<256, 256, 0, stream>>>((const char*)d_in[0], (const char*)d_in[1], (const char*)d_in[2], (const char*)d_in[3], (const char*)d_in[4],
                                            (char*)d_out, (char*)d_ws);
}
```
